```python
import torch

def _softmax(x, axis=-1):
    return torch.softmax(x, dim=axis)
def _gelu(x):
    return torch.nn.functional.gelu(x, approximate="tanh")
def _silu(x):
    return torch.nn.functional.silu(x)
def _sigmoid(x):
    return torch.sigmoid(x)
def _layernorm(x, axis=-1, eps=1e-5):
    m = x.mean(dim=axis, keepdim=True)
    v = x.var(dim=axis, keepdim=True, unbiased=False)
    return (x - m) / torch.sqrt(v + eps)
def _scatter_add(tgt, idx, src, axis=0):
    return tgt.clone().index_add_(axis, idx, src)
def _segment_sum(src, seg, n):
    out = torch.zeros((n,) + src.shape[1:], dtype=src.dtype)
    return out.index_add_(0, seg, src)
def _pad(x, pw):
    flat = tuple(v for pair in reversed(pw) for v in pair)
    return torch.nn.functional.pad(x, flat)

kernel_name = 'standalone__mha_block_cached__near_ridge'

def setup_inputs():

    _w = (lambda a: (lambda t: t.to(torch.bfloat16) if t.is_floating_point() else t)(torch.as_tensor(a)))
    B, Sq, D = 8, 256, 512
    H, Sk, Dh = 32, 2048, 128
    return {
        "x": _w((lambda *s: torch.randn(*s, dtype=torch.bfloat16) * 0.8)(B, Sq, D)),
        "k_cache": _w((lambda *s: torch.randn(*s, dtype=torch.bfloat16) * 0.8)(B, H, Sk, Dh)),
        "v_cache": _w((lambda *s: torch.randn(*s, dtype=torch.bfloat16) * 0.05)(B, H, Sk, Dh)),
        "wq": _w((lambda *s: torch.randn(*s, dtype=torch.bfloat16) * 0.8)(D, H * Dh) * (D ** -0.5)),
        "wo": _w((lambda *s: torch.randn(*s, dtype=torch.bfloat16) * 0.05)(H * Dh, D) * ((H * Dh) ** -0.5)),
    }

def reference(x, k_cache, v_cache, wq, wo):
    B, Sq, D = x.shape
    H, Dh = 32, 128
    q = (lambda x, *d: x.permute(*d))((x @ wq).reshape(B, Sq, H, Dh), 0, 2, 1, 3)
    scores = torch.einsum("bhqd,bhkd->bhqk", q, k_cache) * (Dh ** -0.5)
    p = _softmax(scores, axis=-1)
    o = torch.einsum("bhqk,bhkd->bhqd", p, v_cache)
    return (lambda x, *d: x.permute(*d))(o, 0, 2, 1, 3).reshape(B, Sq, H * Dh) @ wo
```

```cpp
#include <hip/hip_runtime.h>
#include <hip/hip_bf16.h>
#include <cstdio>

typedef float    f32x16 __attribute__((ext_vector_type(16)));
typedef short    bf16x8 __attribute__((ext_vector_type(8)));
typedef __bf16   bf16x2v __attribute__((ext_vector_type(2)));
typedef float    f32x2  __attribute__((ext_vector_type(2)));
typedef unsigned u32x4  __attribute__((ext_vector_type(4)));
typedef unsigned u32x2  __attribute__((ext_vector_type(2)));
#define DI __device__ __forceinline__
constexpr int B = 8, SQ = 256, D = 512, H = 32, SK = 2048, DH = 128;

DI unsigned cvtpk(float lo, float hi) { f32x2 v = {lo, hi}; bf16x2v b = __builtin_convertvector(v, bf16x2v); return __builtin_bit_cast(unsigned, b); }
DI f32x16 mfma32(bf16x8 a, bf16x8 b, f32x16 c) { return __builtin_amdgcn_mfma_f32_32x32x16_bf16(__builtin_bit_cast(__attribute__((ext_vector_type(8))) __bf16, a), __builtin_bit_cast(__attribute__((ext_vector_type(8))) __bf16, b), c, 0, 0, 0); }
DI bf16x8 pack8(const f32x16& x, int s) { u32x4 w; w[0] = cvtpk(x[8*s], x[8*s+1]); w[1] = cvtpk(x[8*s+2], x[8*s+3]); w[2] = cvtpk(x[8*s+4], x[8*s+5]); w[3] = cvtpk(x[8*s+6], x[8*s+7]); return __builtin_bit_cast(bf16x8, w); }

DI void safe_unit(const unsigned short* __restrict__ x, const unsigned short* __restrict__ Kc, const unsigned short* __restrict__ Vc,
                  const unsigned short* __restrict__ wq, unsigned short* __restrict__ O, int b, int h, int q0, int lane) {
  const int r = lane & 31, hh = lane >> 5;
  bf16x8 qf[8];
  {
    f32x16 acc[4];
    #pragma unroll
    for (int db = 0; db < 4; ++db) for (int i = 0; i < 16; ++i) acc[db][i] = 0.f;
    const unsigned short* xr = x + ((long)b * SQ + q0 + r) * D;
    for (int k0 = 0; k0 < D; k0 += 16) {
      const bf16x8 bf = *(const bf16x8*)(xr + k0 + 8 * hh);
      #pragma unroll
      for (int db = 0; db < 4; ++db) {
        bf16x8 af;
        #pragma unroll
        for (int j = 0; j < 8; ++j) af[j] = (short)wq[(long)(k0 + 8 * hh + j) * (H * DH) + h * DH + 32 * db + r];
        acc[db] = mfma32(af, bf, acc[db]);
      }
    }
    #pragma unroll
    for (int db = 0; db < 4; ++db) { qf[2 * db] = pack8(acc[db], 0); qf[2 * db + 1] = pack8(acc[db], 1); }
  }
  const float C = 0.08838834764831845f * 1.4426950408889634f;
  float m = -3.0e38f, l = 0.f;
  f32x16 o[4];
  #pragma unroll
  for (int db = 0; db < 4; ++db) for (int i = 0; i < 16; ++i) o[db][i] = 0.f;
  const unsigned short* Kb = Kc + ((long)b * H + h) * SK * DH;
  const unsigned short* Vb = Vc + ((long)b * H + h) * SK * DH;
  for (int k0 = 0; k0 < SK; k0 += 32) {
    f32x16 s; for (int i = 0; i < 16; ++i) s[i] = 0.f;
    const unsigned short* kr = Kb + (long)(k0 + r) * DH;
    #pragma unroll
    for (int t = 0; t < 8; ++t) {
      const u32x2 lo = *(const u32x2*)(kr + 16 * t + 4 * hh), hi = *(const u32x2*)(kr + 16 * t + 8 + 4 * hh);
      u32x4 w = {lo[0], lo[1], hi[0], hi[1]};
      s = mfma32(__builtin_bit_cast(bf16x8, w), qf[t], s);
    }
    float pm = s[0];
    #pragma unroll
    for (int i = 1; i < 16; ++i) pm = fmaxf(pm, s[i]);
    pm = fmaxf(pm, __shfl_xor(pm, 32));
    const float mn = fmaxf(m, pm), alpha = __builtin_amdgcn_exp2f((m - mn) * C), mc = mn * C;
    m = mn;
    float rs = 0.f;
    #pragma unroll
    for (int i = 0; i < 16; ++i) { s[i] = __builtin_amdgcn_exp2f(fmaf(s[i], C, -mc)); rs += s[i]; }
    rs += __shfl_xor(rs, 32);
    l = l * alpha + rs;
    const bf16x8 p0 = pack8(s, 0), p1 = pack8(s, 1);
    #pragma unroll
    for (int db = 0; db < 4; ++db) {
      #pragma unroll
      for (int i = 0; i < 16; ++i) o[db][i] *= alpha;
      bf16x8 a0, a1;
      #pragma unroll
      for (int j = 0; j < 8; ++j) {
        const int kk = 8 * (j >> 2) + 4 * hh + (j & 3);
        a0[j] = (short)Vb[(long)(k0 + kk) * DH + 32 * db + r];
        a1[j] = (short)Vb[(long)(k0 + 16 + kk) * DH + 32 * db + r];
      }
      o[db] = mfma32(a0, p0, o[db]);
      o[db] = mfma32(a1, p1, o[db]);
    }
  }
  const float il = 1.0f / l;
  unsigned short* orow = O + ((long)b * SQ + q0 + r) * (H * DH) + h * DH;
  #pragma unroll
  for (int db = 0; db < 4; ++db)
    #pragma unroll
    for (int g = 0; g < 4; ++g) {
      u32x2 w = {cvtpk(o[db][4*g] * il, o[db][4*g+1] * il), cvtpk(o[db][4*g+2] * il, o[db][4*g+3] * il)};
      *(u32x2*)(orow + 32 * db + 8 * g + 4 * hh) = w;
    }
}

__global__ __launch_bounds__(512) void attn_simple(const unsigned short* __restrict__ x, const unsigned short* __restrict__ Kc, const unsigned short* __restrict__ Vc,
                                                   const unsigned short* __restrict__ wq, unsigned short* __restrict__ O) {
  const int b = blockIdx.x & 7, h = blockIdx.x >> 3, w = threadIdx.x >> 6, lane = threadIdx.x & 63;
  safe_unit(x, Kc, Vc, wq, O, b, h, 32 * w, lane);
}

__global__ __launch_bounds__(256) void outproj_simple(const unsigned short* __restrict__ O, const unsigned short* __restrict__ wo, unsigned short* __restrict__ out) {
  const int w = threadIdx.x >> 6, lane = threadIdx.x & 63, r = lane & 31, hh = lane >> 5;
  const int tile = blockIdx.x * 4 + w, m0 = (tile >> 4) * 32, n0 = (tile & 15) * 32;
  f32x16 acc; for (int i = 0; i < 16; ++i) acc[i] = 0.f;
  const unsigned short* ar = O + (long)(m0 + r) * (H * DH);
  for (int k0 = 0; k0 < H * DH; k0 += 16) {
    const bf16x8 af = *(const bf16x8*)(ar + k0 + 8 * hh);
    bf16x8 bf;
    #pragma unroll
    for (int j = 0; j < 8; ++j) bf[j] = (short)wo[(long)(k0 + 8 * hh + j) * D + n0 + r];
    acc = mfma32(af, bf, acc);
  }
  #pragma unroll
  for (int i = 0; i < 16; ++i) {
    const int row = (i & 3) + 8 * (i >> 2) + 4 * hh;
    out[(long)(m0 + row) * D + n0 + r] = (unsigned short)(cvtpk(acc[i], 0.f) & 0xffffu);
  }
}

extern "C" void kernel_launch(void* const* d_in, const int* in_sizes, int n_in, void* d_out, int out_size, void* d_ws, size_t ws_size, hipStream_t stream) {
  const unsigned short* x = (const unsigned short*)d_in[0];
  const unsigned short* kc = (const unsigned short*)d_in[1];
  const unsigned short* vc = (const unsigned short*)d_in[2];
  const unsigned short* wq = (const unsigned short*)d_in[3];
  const unsigned short* wo = (const unsigned short*)d_in[4];
  unsigned short* O = (unsigned short*)d_ws;
  fprintf(stderr, "ws_size %zu out_size %d\n", ws_size, out_size);
  attn_simple<<<256, 512, 0, stream>>>(x, kc, vc, wq, O);
  outproj_simple<<<256, 256, 0, stream>>>(O, wo, (unsigned short*)d_out);
}
```

```cpp
#include <hip/hip_runtime.h>
#include <hip/hip_bf16.h>

typedef float    f32x16 __attribute__((ext_vector_type(16)));
typedef short    bf16x8 __attribute__((ext_vector_type(8)));
typedef __bf16   bf16x2v __attribute__((ext_vector_type(2)));
typedef __bf16   bf16x8v __attribute__((ext_vector_type(8)));
typedef float    f32x2  __attribute__((ext_vector_type(2)));
typedef unsigned u32x4  __attribute__((ext_vector_type(4)));
typedef unsigned u32x2  __attribute__((ext_vector_type(2)));
#define DI __device__ __forceinline__
constexpr int B = 8, SQ = 256, D = 512, H = 32, SK = 2048, DH = 128;

DI unsigned cvtpk(float lo, float hi) { f32x2 v = {lo, hi}; bf16x2v b = __builtin_convertvector(v, bf16x2v); return __builtin_bit_cast(unsigned, b); }
DI f32x16 mfma32(bf16x8 a, bf16x8 b, f32x16 c) { return __builtin_amdgcn_mfma_f32_32x32x16_bf16(__builtin_bit_cast(bf16x8v, a), __builtin_bit_cast(bf16x8v, b), c, 0, 0, 0); }
DI bf16x8 pack8(const f32x16& x, int s) { u32x4 w; w[0] = cvtpk(x[8*s], x[8*s+1]); w[1] = cvtpk(x[8*s+2], x[8*s+3]); w[2] = cvtpk(x[8*s+4], x[8*s+5]); w[3] = cvtpk(x[8*s+6], x[8*s+7]); return __builtin_bit_cast(bf16x8, w); }

DI void safe_unit(const unsigned short* __restrict__ x, const unsigned short* __restrict__ Kc, const unsigned short* __restrict__ Vc,
                  const unsigned short* __restrict__ wq, unsigned short* __restrict__ O, int b, int h, int q0, int lane) {
  const int r = lane & 31, hh = lane >> 5;
  bf16x8 qf[8];
  {
    f32x16 acc[4];
    #pragma unroll
    for (int db = 0; db < 4; ++db) for (int i = 0; i < 16; ++i) acc[db][i] = 0.f;
    const unsigned short* xr = x + ((long)b * SQ + q0 + r) * D;
    for (int k0 = 0; k0 < D; k0 += 16) {
      const bf16x8 bf = *(const bf16x8*)(xr + k0 + 8 * hh);
      #pragma unroll
      for (int db = 0; db < 4; ++db) {
        bf16x8 af;
        #pragma unroll
        for (int j = 0; j < 8; ++j) af[j] = (short)wq[(long)(k0 + 8 * hh + j) * (H * DH) + h * DH + 32 * db + r];
        acc[db] = mfma32(af, bf, acc[db]);
      }
    }
    #pragma unroll
    for (int db = 0; db < 4; ++db) { qf[2 * db] = pack8(acc[db], 0); qf[2 * db + 1] = pack8(acc[db], 1); }
  }
  const float C = 0.08838834764831845f * 1.4426950408889634f;
  float m = -3.0e38f, l = 0.f;
  f32x16 o[4];
  #pragma unroll
  for (int db = 0; db < 4; ++db) for (int i = 0; i < 16; ++i) o[db][i] = 0.f;
  const unsigned short* Kb = Kc + ((long)b * H + h) * SK * DH;
  const unsigned short* Vb = Vc + ((long)b * H + h) * SK * DH;
  for (int k0 = 0; k0 < SK; k0 += 32) {
    f32x16 s; for (int i = 0; i < 16; ++i) s[i] = 0.f;
    const unsigned short* kr = Kb + (long)(k0 + r) * DH;
    #pragma unroll
    for (int t = 0; t < 8; ++t) {
      const u32x2 lo = *(const u32x2*)(kr + 16 * t + 4 * hh), hi = *(const u32x2*)(kr + 16 * t + 8 + 4 * hh);
      u32x4 w = {lo[0], lo[1], hi[0], hi[1]};
      s = mfma32(__builtin_bit_cast(bf16x8, w), qf[t], s);
    }
    float pm = s[0];
    #pragma unroll
    for (int i = 1; i < 16; ++i) pm = fmaxf(pm, s[i]);
    pm = fmaxf(pm, __shfl_xor(pm, 32));
    const float mn = fmaxf(m, pm), alpha = __builtin_amdgcn_exp2f((m - mn) * C), mc = mn * C;
    m = mn;
    float rs = 0.f;
    #pragma unroll
    for (int i = 0; i < 16; ++i) { s[i] = __builtin_amdgcn_exp2f(fmaf(s[i], C, -mc)); rs += s[i]; }
    rs += __shfl_xor(rs, 32);
    l = l * alpha + rs;
    const bf16x8 p0 = pack8(s, 0), p1 = pack8(s, 1);
    #pragma unroll
    for (int db = 0; db < 4; ++db) {
      #pragma unroll
      for (int i = 0; i < 16; ++i) o[db][i] *= alpha;
      bf16x8 a0, a1;
      #pragma unroll
      for (int j = 0; j < 8; ++j) {
        const int kk = 8 * (j >> 2) + 4 * hh + (j & 3);
        a0[j] = (short)Vb[(long)(k0 + kk) * DH + 32 * db + r];
        a1[j] = (short)Vb[(long)(k0 + 16 + kk) * DH + 32 * db + r];
      }
      o[db] = mfma32(a0, p0, o[db]);
      o[db] = mfma32(a1, p1, o[db]);
    }
  }
  const float il = 1.0f / l;
  const int q = q0 + r;
  unsigned short* ou = O + (long)(b * H + h) * (SQ * DH);
  #pragma unroll
  for (int db = 0; db < 4; ++db)
    #pragma unroll
    for (int g = 0; g < 4; ++g) {
      const int d = 32 * db + 8 * g + 4 * hh;
      u32x2 w = {cvtpk(o[db][4*g] * il, o[db][4*g+1] * il), cvtpk(o[db][4*g+2] * il, o[db][4*g+3] * il)};
      __hip_atomic_store((unsigned long long*)(ou + ((((q >> 4) * 4 + (d >> 5)) * 64 + ((d >> 2) & 3) * 16 + (q & 15)) * 8 + 4 * ((d >> 4) & 1))),
                         (unsigned long long)w[0] | ((unsigned long long)w[1] << 32), __ATOMIC_RELAXED, __HIP_MEMORY_SCOPE_AGENT);
    }
}

__global__ __launch_bounds__(256, 1) void mha_block(const unsigned short* __restrict__ x, const unsigned short* __restrict__ Kc, const unsigned short* __restrict__ Vc,
    const unsigned short* __restrict__ wq, const unsigned short* __restrict__ wo, char* __restrict__ ws, unsigned short* __restrict__ out) {
  extern __shared__ __attribute__((aligned(16))) char lds[];
  const unsigned bid = blockIdx.x;
  const unsigned wid = __builtin_amdgcn_readfirstlane(threadIdx.x >> 6);
  unsigned flag, tag_lo, tag_hi;
  asm volatile(
  "s_mov_b32 s68, 16793600\n"
  "s_mov_b32 s69, 0\n"
  "s_add_u32 s68, s68, %[ws_lo]\n"
  "s_addc_u32 s69, s69, %[ws_hi]\n"
  "s_load_dwordx2 s[72:73], s[68:69], 0x0\n"
  "s_lshr_b32 s75, %[bid], 3\n"
  "s_mov_b32 s71, s75\n"
  "s_and_b32 s74, %[bid], 7\n"
  "s_lshl_b32 s45, s74, 18\n"
  "s_mov_b64 s[28:29], %[x]\n"
  "s_add_u32 s28, s28, s45\n"
  "s_addc_u32 s29, s29, 0\n"
  "s_and_b32 s29, s29, 0xffff\n"
  "s_mov_b32 s30, 0x40000\n"
  "s_mov_b32 s31, 0x20000\n"
  "v_mbcnt_lo_u32_b32 v236, -1, 0\n"
  "v_mbcnt_hi_u32_b32 v236, -1, v236\n"
  "v_lshrrev_b32 v238, 4, v236\n"
  "v_and_b32 v239, 7, v236\n"
  "v_lshrrev_b32 v240, 3, v236\n"
  "v_or_b32 v241, 0, v238\n"
  "v_xor_b32 v241, v241, v239\n"
  "v_lshlrev_b32 v241, 4, v241\n"
  "v_lshl_add_u32 v241, v240, 10, v241\n"
  "v_add_u32 v196, 0, v241\n"
  "v_add_u32 v198, 14336, v241\n"
  "v_or_b32 v241, 4, v238\n"
  "v_xor_b32 v241, v241, v239\n"
  "v_lshlrev_b32 v241, 4, v241\n"
  "v_lshl_add_u32 v241, v240, 10, v241\n"
  "v_add_u32 v197, 7168, v241\n"
  "v_add_u32 v199, 21504, v241\n"
  "s_lshl_b32 s47, %[wid], 13\n"
  "s_add_u32 s47, s47, 131072\n"
  "s_lshl_b32 s42, %[wid], 12\n"
  "s_lshl_b32 s68, %[wid], 16\n"
  "s_lshl_b32 s69, %[wid], 17\n"
  "s_add_u32 s45, s71, 0\n"
  "s_and_b32 s45, s45, 7\n"
  "s_lshl_b32 s46, s45, 7\n"
  "s_add_u32 s52, s68, s46\n"
  "s_lshl_b32 s46, s45, 19\n"
  "s_add_u32 s60, s69, s46\n"
  "s_add_u32 m0, s47, 0\n"
  "s_add_u32 s45, s52, 0\n"
  "s_nop 0\n"
  "buffer_load_dwordx4 v196, s[28:31], s45 offen offset:0 lds\n"
  "buffer_load_dwordx4 v197, s[28:31], s45 offen offset:1024 lds\n"
  "buffer_load_dwordx4 v198, s[28:31], s45 offen offset:2048 lds\n"
  "buffer_load_dwordx4 v199, s[28:31], s45 offen offset:3072 lds\n"
  "s_add_u32 m0, s47, 4096\n"
  "s_add_u32 s45, s52, 32768\n"
  "s_nop 0\n"
  "buffer_load_dwordx4 v196, s[28:31], s45 offen offset:0 lds\n"
  "buffer_load_dwordx4 v197, s[28:31], s45 offen offset:1024 lds\n"
  "buffer_load_dwordx4 v198, s[28:31], s45 offen offset:2048 lds\n"
  "buffer_load_dwordx4 v199, s[28:31], s45 offen offset:3072 lds\n"
  "s_lshl_b32 s45, s71, 8\n"
  "s_mov_b64 s[48:49], %[wq]\n"
  "s_add_u32 s48, s48, s45\n"
  "s_addc_u32 s49, s49, 0\n"
  "s_and_b32 s49, s49, 0xffff\n"
  "s_mov_b32 s50, 0\n"
  "s_mov_b32 s51, 0x20000\n"
  "s_sub_u32 s50, 0x400000, s45\n"
  "v_and_b32 v237, 15, v236\n"
  "v_or_b32 v241, 0, v238\n"
  "v_lshlrev_b32 v241, 1, v241\n"
  "v_xor_b32 v241, v241, v237\n"
  "v_lshlrev_b32 v241, 4, v241\n"
  "v_lshl_add_u32 v241, v238, 13, v241\n"
  "v_add_u32 v192, 0, v241\n"
  "v_add_u32 v193, 31744, v241\n"
  "v_or_b32 v241, 4, v238\n"
  "v_lshlrev_b32 v241, 1, v241\n"
  "v_xor_b32 v241, v241, v237\n"
  "v_lshlrev_b32 v241, 4, v241\n"
  "v_lshl_add_u32 v241, v238, 13, v241\n"
  "v_add_u32 v194, 63488, v241\n"
  "v_add_u32 v195, 95232, v241\n"
  "s_add_u32 m0, s42, 32768\n"
  "s_mov_b32 s46, s60\n"
  "s_nop 0\n"
  "buffer_load_dwordx4 v192, s[48:51], s46 offen offset:0 lds\n"
  "buffer_load_dwordx4 v193, s[48:51], s46 offen offset:1024 lds\n"
  "buffer_load_dwordx4 v194, s[48:51], s46 offen offset:2048 lds\n"
  "buffer_load_dwordx4 v195, s[48:51], s46 offen offset:3072 lds\n"
  "s_lshl_b32 s45, s74, 5\n"
  "s_add_u32 s45, s45, s71\n"
  "s_lshl_b32 s45, s45, 19\n"
  "s_mov_b64 s[20:21], %[kc]\n"
  "s_add_u32 s20, s20, s45\n"
  "s_addc_u32 s21, s21, 0\n"
  "s_and_b32 s21, s21, 0xffff\n"
  "s_mov_b32 s22, 0x81000\n"
  "s_mov_b32 s23, 0x20000\n"
  "s_mov_b64 s[24:25], %[vc]\n"
  "s_add_u32 s24, s24, s45\n"
  "s_addc_u32 s25, s25, 0\n"
  "s_and_b32 s25, s25, 0xffff\n"
  "s_mov_b32 s26, 0x81000\n"
  "s_mov_b32 s27, 0x20000\n"
  "s_sub_u32 s20, s20, 0x1000\n"
  "s_subb_u32 s21, s21, 0\n"
  "s_sub_u32 s24, s24, 0x1000\n"
  "s_subb_u32 s25, s25, 0\n"
  "v_lshlrev_b32 v241, 13, v236\n"
  "s_add_u32 s45, s71, 1\n"
  "s_and_b32 s45, s45, 7\n"
  "s_lshl_b32 s46, s45, 7\n"
  "s_add_u32 s53, s68, s46\n"
  "s_lshl_b32 s46, s45, 19\n"
  "s_add_u32 s61, s69, s46\n"
  "s_add_u32 s45, s71, 2\n"
  "s_and_b32 s45, s45, 7\n"
  "s_lshl_b32 s46, s45, 7\n"
  "s_add_u32 s54, s68, s46\n"
  "s_lshl_b32 s46, s45, 19\n"
  "s_add_u32 s62, s69, s46\n"
  "s_add_u32 s45, s71, 3\n"
  "s_and_b32 s45, s45, 7\n"
  "s_lshl_b32 s46, s45, 7\n"
  "s_add_u32 s55, s68, s46\n"
  "s_lshl_b32 s46, s45, 19\n"
  "s_add_u32 s63, s69, s46\n"
  "s_add_u32 s45, s71, 4\n"
  "s_and_b32 s45, s45, 7\n"
  "s_lshl_b32 s46, s45, 7\n"
  "s_add_u32 s56, s68, s46\n"
  "s_lshl_b32 s46, s45, 19\n"
  "s_add_u32 s64, s69, s46\n"
  "s_add_u32 s45, s71, 5\n"
  "s_and_b32 s45, s45, 7\n"
  "s_lshl_b32 s46, s45, 7\n"
  "s_add_u32 s57, s68, s46\n"
  "s_lshl_b32 s46, s45, 19\n"
  "s_add_u32 s65, s69, s46\n"
  "s_add_u32 s45, s71, 6\n"
  "s_and_b32 s45, s45, 7\n"
  "s_lshl_b32 s46, s45, 7\n"
  "s_add_u32 s58, s68, s46\n"
  "s_lshl_b32 s46, s45, 19\n"
  "s_add_u32 s66, s69, s46\n"
  "s_add_u32 s45, s71, 7\n"
  "s_and_b32 s45, s45, 7\n"
  "s_lshl_b32 s46, s45, 7\n"
  "s_add_u32 s59, s68, s46\n"
  "s_lshl_b32 s46, s45, 19\n"
  "s_add_u32 s67, s69, s46\n"
  "s_add_u32 m0, s42, 49152\n"
  "s_mov_b32 s46, s61\n"
  "s_nop 0\n"
  "buffer_load_dwordx4 v192, s[48:51], s46 offen offset:0 lds\n"
  "buffer_load_dwordx4 v193, s[48:51], s46 offen offset:1024 lds\n"
  "buffer_load_dwordx4 v194, s[48:51], s46 offen offset:2048 lds\n"
  "buffer_load_dwordx4 v195, s[48:51], s46 offen offset:3072 lds\n"
  "s_add_u32 m0, s47, -32768\n"
  "s_add_u32 s45, s53, 0\n"
  "s_nop 0\n"
  "buffer_load_dwordx4 v196, s[28:31], s45 offen offset:0 lds\n"
  "buffer_load_dwordx4 v197, s[28:31], s45 offen offset:1024 lds\n"
  "buffer_load_dwordx4 v198, s[28:31], s45 offen offset:2048 lds\n"
  "buffer_load_dwordx4 v199, s[28:31], s45 offen offset:3072 lds\n"
  "s_add_u32 m0, s47, -28672\n"
  "s_add_u32 s45, s53, 32768\n"
  "s_nop 0\n"
  "buffer_load_dwordx4 v196, s[28:31], s45 offen offset:0 lds\n"
  "buffer_load_dwordx4 v197, s[28:31], s45 offen offset:1024 lds\n"
  "buffer_load_dwordx4 v198, s[28:31], s45 offen offset:2048 lds\n"
  "buffer_load_dwordx4 v199, s[28:31], s45 offen offset:3072 lds\n"
  "v_lshlrev_b32 v241, 7, v236\n"
  "s_lshl_b32 s45, s71, 13\n"
  "buffer_load_dword v210, v241, s[28:31], s45 offen\n"
  "s_getpc_b64 s[68:69]\n"
  "s_and_b32 s45, s75, 7\n"
  "s_lshl_b32 s45, s45, 13\n"
  "s_add_u32 s68, s68, s45\n"
  "s_addc_u32 s69, s69, 0\n"
  "global_load_dword v211, v241, s[68:69]\n"
  "s_add_u32 s43, s42, 65536\n"
  "s_mov_b32 s40, s42\n"
  "s_mov_b32 s41, s42\n"
  "v_lshrrev_b32 v239, 1, v237\n"
  "v_add_u32 v241, 0, v238\n"
  "v_xor_b32 v241, v241, v239\n"
  "v_lshlrev_b32 v241, 4, v241\n"
  "v_lshl_add_u32 v241, v237, 7, v241\n"
  "v_add_u32 v200, s47, v241\n"
  "v_subrev_u32 v200, 32768, v200\n"
  "v_add_u32 v241, 4, v238\n"
  "v_xor_b32 v241, v241, v239\n"
  "v_lshlrev_b32 v241, 4, v241\n"
  "v_lshl_add_u32 v241, v237, 7, v241\n"
  "v_add_u32 v201, s47, v241\n"
  "v_subrev_u32 v201, 32768, v201\n"
  "v_lshrrev_b32 v239, 2, v237\n"
  "v_and_b32 v240, 3, v236\n"
  "v_lshl_add_u32 v241, v238, 3, v239\n"
  "v_lshlrev_b32 v241, 8, v241\n"
  "v_lshl_add_u32 v241, v240, 3, v241\n"
  "v_add_u32 v241, 32768, v241\n"
  "v_and_b32 v242, 1, v238\n"
  "v_lshl_or_b32 v242, v242, 2, v239\n"
  "v_xor_b32 v243, 0, v242\n"
  "v_lshl_add_u32 v202, v243, 5, v241\n"
  "v_xor_b32 v243, 1, v242\n"
  "v_lshl_add_u32 v203, v243, 5, v241\n"
  "v_xor_b32 v243, 2, v242\n"
  "v_lshl_add_u32 v204, v243, 5, v241\n"
  "v_xor_b32 v243, 3, v242\n"
  "v_lshl_add_u32 v205, v243, 5, v241\n"
  "v_xor_b32 v243, 4, v242\n"
  "v_lshl_add_u32 v206, v243, 5, v241\n"
  "v_xor_b32 v243, 5, v242\n"
  "v_lshl_add_u32 v207, v243, 5, v241\n"
  "v_xor_b32 v243, 6, v242\n"
  "v_lshl_add_u32 v208, v243, 5, v241\n"
  "v_xor_b32 v243, 7, v242\n"
  "v_lshl_add_u32 v209, v243, 5, v241\n"
  "s_mov_b32 s45, 4096\n"
  "v_add_u32 v241, 0, v238\n"
  "v_xor_b32 v241, v241, v237\n"
  "v_lshlrev_b32 v241, 4, v241\n"
  "v_lshl_add_u32 v241, v238, 8, v241\n"
  "v_add_u32 v228, s45, v241\n"
  "v_add_u32 v241, 0, v238\n"
  "v_lshlrev_b32 v241, 1, v241\n"
  "v_xor_b32 v241, v241, v237\n"
  "v_lshlrev_b32 v241, 4, v241\n"
  "v_lshl_add_u32 v241, v238, 8, v241\n"
  "v_add_u32 v232, s45, v241\n"
  "s_mov_b32 s45, 4096\n"
  "v_add_u32 v241, 4, v238\n"
  "v_xor_b32 v241, v241, v237\n"
  "v_lshlrev_b32 v241, 4, v241\n"
  "v_lshl_add_u32 v241, v238, 8, v241\n"
  "v_add_u32 v229, s45, v241\n"
  "v_add_u32 v241, 4, v238\n"
  "v_lshlrev_b32 v241, 1, v241\n"
  "v_xor_b32 v241, v241, v237\n"
  "v_lshlrev_b32 v241, 4, v241\n"
  "v_lshl_add_u32 v241, v238, 8, v241\n"
  "v_add_u32 v233, s45, v241\n"
  "s_mov_b32 s45, 4096\n"
  "v_add_u32 v241, 8, v238\n"
  "v_xor_b32 v241, v241, v237\n"
  "v_lshlrev_b32 v241, 4, v241\n"
  "v_lshl_add_u32 v241, v238, 8, v241\n"
  "v_add_u32 v230, s45, v241\n"
  "v_add_u32 v241, 0, v238\n"
  "v_lshlrev_b32 v241, 1, v241\n"
  "v_xor_b32 v241, v241, v237\n"
  "v_lshlrev_b32 v241, 4, v241\n"
  "v_lshl_add_u32 v241, v238, 8, v241\n"
  "v_add_u32 v234, s45, v241\n"
  "s_mov_b32 s45, 4096\n"
  "v_add_u32 v241, 12, v238\n"
  "v_xor_b32 v241, v241, v237\n"
  "v_lshlrev_b32 v241, 4, v241\n"
  "v_lshl_add_u32 v241, v238, 8, v241\n"
  "v_add_u32 v231, s45, v241\n"
  "v_add_u32 v241, 4, v238\n"
  "v_lshlrev_b32 v241, 1, v241\n"
  "v_xor_b32 v241, v241, v237\n"
  "v_lshlrev_b32 v241, 4, v241\n"
  "v_lshl_add_u32 v241, v238, 8, v241\n"
  "v_add_u32 v235, s45, v241\n"
  "s_waitcnt vmcnt(18)\n"
  "ds_read_b128 a[128:131], v200 offset:32768\n"
  "ds_read_b128 a[132:135], v201 offset:32768\n"
  "ds_read_b128 a[136:139], v200 offset:34816\n"
  "ds_read_b128 a[140:143], v201 offset:34816\n"
  "ds_read_b128 a[144:147], v200 offset:36864\n"
  "ds_read_b128 a[148:151], v201 offset:36864\n"
  "ds_read_b128 a[152:155], v200 offset:38912\n"
  "ds_read_b128 a[156:159], v201 offset:38912\n"
  "s_waitcnt lgkmcnt(0)\n"
  "s_add_u32 s72, s72, 0x9e3779b9\n"
  "s_add_u32 s73, s73, 0x7f4a7c15\n"
  "s_waitcnt vmcnt(14)\n"
  "s_barrier\n"
  "ds_read_b64_tr_b16 a[0:1], v202 offset:0\n"
  "ds_read_b64_tr_b16 a[2:3], v202 offset:1024\n"
  "ds_read_b64_tr_b16 a[32:33], v202 offset:8192\n"
  "ds_read_b64_tr_b16 a[34:35], v202 offset:9216\n"
  "ds_read_b64_tr_b16 a[4:5], v203 offset:0\n"
  "ds_read_b64_tr_b16 a[6:7], v203 offset:1024\n"
  "s_waitcnt lgkmcnt(2)\n"
  "v_mfma_f32_16x16x32_bf16 v[0:3], a[0:3], a[128:131], 0\n"
  "s_add_u32 m0, s42, 81920\n"
  "ds_read_b64_tr_b16 a[36:37], v203 offset:8192\n"
  "ds_read_b64_tr_b16 a[38:39], v203 offset:9216\n"
  "v_mfma_f32_16x16x32_bf16 v[0:3], a[32:35], a[132:135], v[0:3]\n"
  "s_mov_b32 s46, s62\n"
  "v_mfma_f32_16x16x32_bf16 v[4:7], a[0:3], a[136:139], 0\n"
  "buffer_load_dwordx4 v192, s[48:51], s46 offen offset:0 lds\n"
  "v_mfma_f32_16x16x32_bf16 v[4:7], a[32:35], a[140:143], v[4:7]\n"
  "buffer_load_dwordx4 v193, s[48:51], s46 offen offset:1024 lds\n"
  "v_mfma_f32_16x16x32_bf16 v[8:11], a[0:3], a[144:147], 0\n"
  "ds_read_b64_tr_b16 a[8:9], v204 offset:0\n"
  "ds_read_b64_tr_b16 a[10:11], v204 offset:1024\n"
  "v_mfma_f32_16x16x32_bf16 v[8:11], a[32:35], a[148:151], v[8:11]\n"
  "v_mfma_f32_16x16x32_bf16 v[12:15], a[0:3], a[152:155], 0\n"
  "buffer_load_dwordx4 v194, s[48:51], s46 offen offset:2048 lds\n"
  "v_mfma_f32_16x16x32_bf16 v[12:15], a[32:35], a[156:159], v[12:15]\n"
  "s_waitcnt lgkmcnt(2)\n"
  "buffer_load_dwordx4 v195, s[48:51], s46 offen offset:3072 lds\n"
  "v_mfma_f32_16x16x32_bf16 v[16:19], a[4:7], a[128:131], 0\n"
  "s_add_u32 m0, s47, 0\n"
  "ds_read_b64_tr_b16 a[40:41], v204 offset:8192\n"
  "ds_read_b64_tr_b16 a[42:43], v204 offset:9216\n"
  "v_mfma_f32_16x16x32_bf16 v[16:19], a[36:39], a[132:135], v[16:19]\n"
  "s_add_u32 s45, s54, 0\n"
  "v_mfma_f32_16x16x32_bf16 v[20:23], a[4:7], a[136:139], 0\n"
  "buffer_load_dwordx4 v196, s[28:31], s45 offen offset:0 lds\n"
  "v_mfma_f32_16x16x32_bf16 v[20:23], a[36:39], a[140:143], v[20:23]\n"
  "buffer_load_dwordx4 v197, s[28:31], s45 offen offset:1024 lds\n"
  "v_mfma_f32_16x16x32_bf16 v[24:27], a[4:7], a[144:147], 0\n"
  "ds_read_b64_tr_b16 a[12:13], v205 offset:0\n"
  "ds_read_b64_tr_b16 a[14:15], v205 offset:1024\n"
  "v_mfma_f32_16x16x32_bf16 v[24:27], a[36:39], a[148:151], v[24:27]\n"
  "v_mfma_f32_16x16x32_bf16 v[28:31], a[4:7], a[152:155], 0\n"
  "buffer_load_dwordx4 v198, s[28:31], s45 offen offset:2048 lds\n"
  "v_mfma_f32_16x16x32_bf16 v[28:31], a[36:39], a[156:159], v[28:31]\n"
  "s_waitcnt lgkmcnt(2)\n"
  "buffer_load_dwordx4 v199, s[28:31], s45 offen offset:3072 lds\n"
  "v_mfma_f32_16x16x32_bf16 v[32:35], a[8:11], a[128:131], 0\n"
  "ds_read_b64_tr_b16 a[44:45], v205 offset:8192\n"
  "ds_read_b64_tr_b16 a[46:47], v205 offset:9216\n"
  "v_mfma_f32_16x16x32_bf16 v[32:35], a[40:43], a[132:135], v[32:35]\n"
  "s_waitcnt vmcnt(10)\n"
  "v_mfma_f32_16x16x32_bf16 v[36:39], a[8:11], a[136:139], 0\n"
  "ds_read_b128 a[160:163], v200 offset:0\n"
  "v_mfma_f32_16x16x32_bf16 v[36:39], a[40:43], a[140:143], v[36:39]\n"
  "s_add_u32 m0, s47, 4096\n"
  "ds_read_b128 a[164:167], v201 offset:0\n"
  "v_mfma_f32_16x16x32_bf16 v[40:43], a[8:11], a[144:147], 0\n"
  "s_add_u32 s45, s54, 32768\n"
  "ds_read_b64_tr_b16 a[16:17], v206 offset:0\n"
  "ds_read_b64_tr_b16 a[18:19], v206 offset:1024\n"
  "v_mfma_f32_16x16x32_bf16 v[40:43], a[40:43], a[148:151], v[40:43]\n"
  "buffer_load_dwordx4 v196, s[28:31], s45 offen offset:0 lds\n"
  "v_mfma_f32_16x16x32_bf16 v[44:47], a[8:11], a[152:155], 0\n"
  "ds_read_b128 a[168:171], v200 offset:2048\n"
  "v_mfma_f32_16x16x32_bf16 v[44:47], a[40:43], a[156:159], v[44:47]\n"
  "s_waitcnt lgkmcnt(5)\n"
  "ds_read_b128 a[172:175], v201 offset:2048\n"
  "v_mfma_f32_16x16x32_bf16 v[48:51], a[12:15], a[128:131], 0\n"
  "ds_read_b64_tr_b16 a[48:49], v206 offset:8192\n"
  "ds_read_b64_tr_b16 a[50:51], v206 offset:9216\n"
  "v_mfma_f32_16x16x32_bf16 v[48:51], a[44:47], a[132:135], v[48:51]\n"
  "buffer_load_dwordx4 v197, s[28:31], s45 offen offset:1024 lds\n"
  "v_mfma_f32_16x16x32_bf16 v[52:55], a[12:15], a[136:139], 0\n"
  "ds_read_b128 a[176:179], v200 offset:4096\n"
  "v_mfma_f32_16x16x32_bf16 v[52:55], a[44:47], a[140:143], v[52:55]\n"
  "ds_read_b128 a[180:183], v201 offset:4096\n"
  "v_mfma_f32_16x16x32_bf16 v[56:59], a[12:15], a[144:147], 0\n"
  "ds_read_b64_tr_b16 a[20:21], v207 offset:0\n"
  "ds_read_b64_tr_b16 a[22:23], v207 offset:1024\n"
  "v_mfma_f32_16x16x32_bf16 v[56:59], a[44:47], a[148:151], v[56:59]\n"
  "buffer_load_dwordx4 v198, s[28:31], s45 offen offset:2048 lds\n"
  "v_mfma_f32_16x16x32_bf16 v[60:63], a[12:15], a[152:155], 0\n"
  "ds_read_b128 a[184:187], v200 offset:6144\n"
  "v_mfma_f32_16x16x32_bf16 v[60:63], a[44:47], a[156:159], v[60:63]\n"
  "s_waitcnt lgkmcnt(5)\n"
  "ds_read_b128 a[188:191], v201 offset:6144\n"
  "v_mfma_f32_16x16x32_bf16 v[64:67], a[16:19], a[128:131], 0\n"
  "ds_read_b64_tr_b16 a[52:53], v207 offset:8192\n"
  "ds_read_b64_tr_b16 a[54:55], v207 offset:9216\n"
  "v_mfma_f32_16x16x32_bf16 v[64:67], a[48:51], a[132:135], v[64:67]\n"
  "s_waitcnt lgkmcnt(0)\n"
  "buffer_load_dwordx4 v199, s[28:31], s45 offen offset:3072 lds\n"
  "v_mfma_f32_16x16x32_bf16 v[68:71], a[16:19], a[136:139], 0\n"
  "v_mfma_f32_16x16x32_bf16 v[68:71], a[48:51], a[140:143], v[68:71]\n"
  "v_mfma_f32_16x16x32_bf16 v[72:75], a[16:19], a[144:147], 0\n"
  "s_add_u32 m0, s47, -32768\n"
  "ds_read_b64_tr_b16 a[24:25], v208 offset:0\n"
  "ds_read_b64_tr_b16 a[26:27], v208 offset:1024\n"
  "v_mfma_f32_16x16x32_bf16 v[72:75], a[48:51], a[148:151], v[72:75]\n"
  "s_add_u32 s45, s55, 0\n"
  "v_mfma_f32_16x16x32_bf16 v[76:79], a[16:19], a[152:155], 0\n"
  "buffer_load_dwordx4 v196, s[28:31], s45 offen offset:0 lds\n"
  "v_mfma_f32_16x16x32_bf16 v[76:79], a[48:51], a[156:159], v[76:79]\n"
  "buffer_load_dwordx4 v197, s[28:31], s45 offen offset:1024 lds\n"
  "v_mfma_f32_16x16x32_bf16 v[80:83], a[20:23], a[128:131], 0\n"
  "ds_read_b64_tr_b16 a[56:57], v208 offset:8192\n"
  "ds_read_b64_tr_b16 a[58:59], v208 offset:9216\n"
  "v_mfma_f32_16x16x32_bf16 v[80:83], a[52:55], a[132:135], v[80:83]\n"
  "v_mfma_f32_16x16x32_bf16 v[84:87], a[20:23], a[136:139], 0\n"
  "buffer_load_dwordx4 v198, s[28:31], s45 offen offset:2048 lds\n"
  "v_mfma_f32_16x16x32_bf16 v[84:87], a[52:55], a[140:143], v[84:87]\n"
  "buffer_load_dwordx4 v199, s[28:31], s45 offen offset:3072 lds\n"
  "v_mfma_f32_16x16x32_bf16 v[88:91], a[20:23], a[144:147], 0\n"
  "s_add_u32 m0, s47, -28672\n"
  "ds_read_b64_tr_b16 a[28:29], v209 offset:0\n"
  "ds_read_b64_tr_b16 a[30:31], v209 offset:1024\n"
  "v_mfma_f32_16x16x32_bf16 v[88:91], a[52:55], a[148:151], v[88:91]\n"
  "s_add_u32 s45, s55, 32768\n"
  "v_mfma_f32_16x16x32_bf16 v[92:95], a[20:23], a[152:155], 0\n"
  "buffer_load_dwordx4 v196, s[28:31], s45 offen offset:0 lds\n"
  "v_mfma_f32_16x16x32_bf16 v[92:95], a[52:55], a[156:159], v[92:95]\n"
  "s_waitcnt lgkmcnt(2)\n"
  "buffer_load_dwordx4 v197, s[28:31], s45 offen offset:1024 lds\n"
  "v_mfma_f32_16x16x32_bf16 v[96:99], a[24:27], a[128:131], 0\n"
  "ds_read_b64_tr_b16 a[60:61], v209 offset:8192\n"
  "ds_read_b64_tr_b16 a[62:63], v209 offset:9216\n"
  "v_mfma_f32_16x16x32_bf16 v[96:99], a[56:59], a[132:135], v[96:99]\n"
  "v_mfma_f32_16x16x32_bf16 v[100:103], a[24:27], a[136:139], 0\n"
  "v_mfma_f32_16x16x32_bf16 v[100:103], a[56:59], a[140:143], v[100:103]\n"
  "s_waitcnt vmcnt(28) lgkmcnt(0)\n"
  "s_barrier\n"
  "v_mfma_f32_16x16x32_bf16 v[104:107], a[24:27], a[144:147], 0\n"
  "ds_read_b64_tr_b16 a[0:1], v202 offset:16384\n"
  "ds_read_b64_tr_b16 a[2:3], v202 offset:17408\n"
  "v_mfma_f32_16x16x32_bf16 v[104:107], a[56:59], a[148:151], v[104:107]\n"
  "v_mfma_f32_16x16x32_bf16 v[108:111], a[24:27], a[152:155], 0\n"
  "buffer_load_dwordx4 v198, s[28:31], s45 offen offset:2048 lds\n"
  "v_mfma_f32_16x16x32_bf16 v[108:111], a[56:59], a[156:159], v[108:111]\n"
  "buffer_load_dwordx4 v199, s[28:31], s45 offen offset:3072 lds\n"
  "v_mfma_f32_16x16x32_bf16 v[112:115], a[28:31], a[128:131], 0\n"
  "ds_read_b64_tr_b16 a[32:33], v202 offset:24576\n"
  "ds_read_b64_tr_b16 a[34:35], v202 offset:25600\n"
  "v_mfma_f32_16x16x32_bf16 v[112:115], a[60:63], a[132:135], v[112:115]\n"
  "v_mfma_f32_16x16x32_bf16 v[116:119], a[28:31], a[136:139], 0\n"
  "v_mfma_f32_16x16x32_bf16 v[116:119], a[60:63], a[140:143], v[116:119]\n"
  "v_mfma_f32_16x16x32_bf16 v[120:123], a[28:31], a[144:147], 0\n"
  "ds_read_b64_tr_b16 a[4:5], v203 offset:16384\n"
  "ds_read_b64_tr_b16 a[6:7], v203 offset:17408\n"
  "v_mfma_f32_16x16x32_bf16 v[120:123], a[60:63], a[148:151], v[120:123]\n"
  "v_mfma_f32_16x16x32_bf16 v[124:127], a[28:31], a[152:155], 0\n"
  "v_mfma_f32_16x16x32_bf16 v[124:127], a[60:63], a[156:159], v[124:127]\n"
  "s_waitcnt lgkmcnt(2)\n"
  "v_mfma_f32_16x16x32_bf16 v[0:3], a[0:3], a[160:163], v[0:3]\n"
  "s_add_u32 m0, s42, 32768\n"
  "ds_read_b64_tr_b16 a[36:37], v203 offset:24576\n"
  "ds_read_b64_tr_b16 a[38:39], v203 offset:25600\n"
  "v_mfma_f32_16x16x32_bf16 v[0:3], a[32:35], a[164:167], v[0:3]\n"
  "s_mov_b32 s46, s63\n"
  "v_and_b32 v128, 15, v236\n"
  "v_mfma_f32_16x16x32_bf16 v[4:7], a[0:3], a[168:171], v[4:7]\n"
  "buffer_load_dwordx4 v192, s[48:51], s46 offen offset:0 lds\n"
  "v_mfma_f32_16x16x32_bf16 v[4:7], a[32:35], a[172:175], v[4:7]\n"
  "buffer_load_dwordx4 v193, s[48:51], s46 offen offset:1024 lds\n"
  "v_mfma_f32_16x16x32_bf16 v[8:11], a[0:3], a[176:179], v[8:11]\n"
  "ds_read_b64_tr_b16 a[8:9], v204 offset:16384\n"
  "ds_read_b64_tr_b16 a[10:11], v204 offset:17408\n"
  "v_mfma_f32_16x16x32_bf16 v[8:11], a[32:35], a[180:183], v[8:11]\n"
  "v_lshrrev_b32 v129, 4, v236\n"
  "v_mfma_f32_16x16x32_bf16 v[12:15], a[0:3], a[184:187], v[12:15]\n"
  "buffer_load_dwordx4 v194, s[48:51], s46 offen offset:2048 lds\n"
  "v_mfma_f32_16x16x32_bf16 v[12:15], a[32:35], a[188:191], v[12:15]\n"
  "s_waitcnt lgkmcnt(2)\n"
  "buffer_load_dwordx4 v195, s[48:51], s46 offen offset:3072 lds\n"
  "v_mfma_f32_16x16x32_bf16 v[16:19], a[4:7], a[160:163], v[16:19]\n"
  "ds_read_b64_tr_b16 a[40:41], v204 offset:24576\n"
  "ds_read_b64_tr_b16 a[42:43], v204 offset:25600\n"
  "v_mfma_f32_16x16x32_bf16 v[16:19], a[36:39], a[164:167], v[16:19]\n"
  "v_add_u32 v132, 0, v129\n"
  "v_mfma_f32_16x16x32_bf16 v[20:23], a[4:7], a[168:171], v[20:23]\n"
  "v_xor_b32 v132, v132, v128\n"
  "v_mfma_f32_16x16x32_bf16 v[20:23], a[36:39], a[172:175], v[20:23]\n"
  "v_lshlrev_b32 v132, 4, v132\n"
  "v_mfma_f32_16x16x32_bf16 v[24:27], a[4:7], a[176:179], v[24:27]\n"
  "ds_read_b64_tr_b16 a[12:13], v205 offset:16384\n"
  "ds_read_b64_tr_b16 a[14:15], v205 offset:17408\n"
  "v_mfma_f32_16x16x32_bf16 v[24:27], a[36:39], a[180:183], v[24:27]\n"
  "v_lshl_add_u32 v224, v128, 8, v132\n"
  "v_mfma_f32_16x16x32_bf16 v[28:31], a[4:7], a[184:187], v[28:31]\n"
  "v_add_u32 v132, 4, v129\n"
  "v_mfma_f32_16x16x32_bf16 v[28:31], a[36:39], a[188:191], v[28:31]\n"
  "s_waitcnt lgkmcnt(2)\n"
  "v_xor_b32 v132, v132, v128\n"
  "v_mfma_f32_16x16x32_bf16 v[32:35], a[8:11], a[160:163], v[32:35]\n"
  "ds_read_b64_tr_b16 a[44:45], v205 offset:24576\n"
  "ds_read_b64_tr_b16 a[46:47], v205 offset:25600\n"
  "v_mfma_f32_16x16x32_bf16 v[32:35], a[40:43], a[164:167], v[32:35]\n"
  "s_waitcnt vmcnt(12)\n"
  "v_lshlrev_b32 v132, 4, v132\n"
  "v_mfma_f32_16x16x32_bf16 v[36:39], a[8:11], a[168:171], v[36:39]\n"
  "ds_read_b128 a[128:131], v200 offset:32768\n"
  "v_mfma_f32_16x16x32_bf16 v[36:39], a[40:43], a[172:175], v[36:39]\n"
  "ds_read_b128 a[132:135], v201 offset:32768\n"
  "v_mfma_f32_16x16x32_bf16 v[40:43], a[8:11], a[176:179], v[40:43]\n"
  "ds_read_b64_tr_b16 a[16:17], v206 offset:16384\n"
  "ds_read_b64_tr_b16 a[18:19], v206 offset:17408\n"
  "v_mfma_f32_16x16x32_bf16 v[40:43], a[40:43], a[180:183], v[40:43]\n"
  "v_lshl_add_u32 v225, v128, 8, v132\n"
  "v_mfma_f32_16x16x32_bf16 v[44:47], a[8:11], a[184:187], v[44:47]\n"
  "ds_read_b128 a[136:139], v200 offset:34816\n"
  "v_mfma_f32_16x16x32_bf16 v[44:47], a[40:43], a[188:191], v[44:47]\n"
  "s_waitcnt lgkmcnt(5)\n"
  "ds_read_b128 a[140:143], v201 offset:34816\n"
  "v_mfma_f32_16x16x32_bf16 v[48:51], a[12:15], a[160:163], v[48:51]\n"
  "ds_read_b64_tr_b16 a[48:49], v206 offset:24576\n"
  "ds_read_b64_tr_b16 a[50:51], v206 offset:25600\n"
  "v_mfma_f32_16x16x32_bf16 v[48:51], a[44:47], a[164:167], v[48:51]\n"
  "v_add_u32 v132, 8, v129\n"
  "v_mfma_f32_16x16x32_bf16 v[52:55], a[12:15], a[168:171], v[52:55]\n"
  "ds_read_b128 a[144:147], v200 offset:36864\n"
  "v_mfma_f32_16x16x32_bf16 v[52:55], a[44:47], a[172:175], v[52:55]\n"
  "ds_read_b128 a[148:151], v201 offset:36864\n"
  "v_mfma_f32_16x16x32_bf16 v[56:59], a[12:15], a[176:179], v[56:59]\n"
  "ds_read_b64_tr_b16 a[20:21], v207 offset:16384\n"
  "ds_read_b64_tr_b16 a[22:23], v207 offset:17408\n"
  "v_mfma_f32_16x16x32_bf16 v[56:59], a[44:47], a[180:183], v[56:59]\n"
  "v_xor_b32 v132, v132, v128\n"
  "v_mfma_f32_16x16x32_bf16 v[60:63], a[12:15], a[184:187], v[60:63]\n"
  "ds_read_b128 a[152:155], v200 offset:38912\n"
  "v_mfma_f32_16x16x32_bf16 v[60:63], a[44:47], a[188:191], v[60:63]\n"
  "s_waitcnt lgkmcnt(5)\n"
  "ds_read_b128 a[156:159], v201 offset:38912\n"
  "v_mfma_f32_16x16x32_bf16 v[64:67], a[16:19], a[160:163], v[64:67]\n"
  "ds_read_b64_tr_b16 a[52:53], v207 offset:24576\n"
  "ds_read_b64_tr_b16 a[54:55], v207 offset:25600\n"
  "v_mfma_f32_16x16x32_bf16 v[64:67], a[48:51], a[164:167], v[64:67]\n"
  "s_waitcnt lgkmcnt(0)\n"
  "v_lshlrev_b32 v132, 4, v132\n"
  "v_mfma_f32_16x16x32_bf16 v[68:71], a[16:19], a[168:171], v[68:71]\n"
  "v_lshl_add_u32 v226, v128, 8, v132\n"
  "v_mfma_f32_16x16x32_bf16 v[68:71], a[48:51], a[172:175], v[68:71]\n"
  "v_add_u32 v132, 12, v129\n"
  "v_mfma_f32_16x16x32_bf16 v[72:75], a[16:19], a[176:179], v[72:75]\n"
  "s_add_u32 m0, s47, 0\n"
  "ds_read_b64_tr_b16 a[24:25], v208 offset:16384\n"
  "ds_read_b64_tr_b16 a[26:27], v208 offset:17408\n"
  "v_mfma_f32_16x16x32_bf16 v[72:75], a[48:51], a[180:183], v[72:75]\n"
  "s_add_u32 s45, s56, 0\n"
  "v_xor_b32 v132, v132, v128\n"
  "v_mfma_f32_16x16x32_bf16 v[76:79], a[16:19], a[184:187], v[76:79]\n"
  "buffer_load_dwordx4 v196, s[28:31], s45 offen offset:0 lds\n"
  "v_mfma_f32_16x16x32_bf16 v[76:79], a[48:51], a[188:191], v[76:79]\n"
  "buffer_load_dwordx4 v197, s[28:31], s45 offen offset:1024 lds\n"
  "v_mfma_f32_16x16x32_bf16 v[80:83], a[20:23], a[160:163], v[80:83]\n"
  "ds_read_b64_tr_b16 a[56:57], v208 offset:24576\n"
  "ds_read_b64_tr_b16 a[58:59], v208 offset:25600\n"
  "v_mfma_f32_16x16x32_bf16 v[80:83], a[52:55], a[164:167], v[80:83]\n"
  "v_lshlrev_b32 v132, 4, v132\n"
  "v_mfma_f32_16x16x32_bf16 v[84:87], a[20:23], a[168:171], v[84:87]\n"
  "buffer_load_dwordx4 v198, s[28:31], s45 offen offset:2048 lds\n"
  "v_mfma_f32_16x16x32_bf16 v[84:87], a[52:55], a[172:175], v[84:87]\n"
  "buffer_load_dwordx4 v199, s[28:31], s45 offen offset:3072 lds\n"
  "v_mfma_f32_16x16x32_bf16 v[88:91], a[20:23], a[176:179], v[88:91]\n"
  "s_add_u32 m0, s47, 4096\n"
  "ds_read_b64_tr_b16 a[28:29], v209 offset:16384\n"
  "ds_read_b64_tr_b16 a[30:31], v209 offset:17408\n"
  "v_mfma_f32_16x16x32_bf16 v[88:91], a[52:55], a[180:183], v[88:91]\n"
  "s_add_u32 s45, s56, 32768\n"
  "v_lshl_add_u32 v227, v128, 8, v132\n"
  "v_mfma_f32_16x16x32_bf16 v[92:95], a[20:23], a[184:187], v[92:95]\n"
  "buffer_load_dwordx4 v196, s[28:31], s45 offen offset:0 lds\n"
  "v_mfma_f32_16x16x32_bf16 v[92:95], a[52:55], a[188:191], v[92:95]\n"
  "s_waitcnt lgkmcnt(2)\n"
  "buffer_load_dwordx4 v197, s[28:31], s45 offen offset:1024 lds\n"
  "v_mfma_f32_16x16x32_bf16 v[96:99], a[24:27], a[160:163], v[96:99]\n"
  "ds_read_b64_tr_b16 a[60:61], v209 offset:24576\n"
  "ds_read_b64_tr_b16 a[62:63], v209 offset:25600\n"
  "v_mfma_f32_16x16x32_bf16 v[96:99], a[56:59], a[164:167], v[96:99]\n"
  "v_lshrrev_b32 v130, 2, v128\n"
  "v_mfma_f32_16x16x32_bf16 v[100:103], a[24:27], a[168:171], v[100:103]\n"
  "v_and_b32 v131, 3, v236\n"
  "v_mfma_f32_16x16x32_bf16 v[100:103], a[56:59], a[172:175], v[100:103]\n"
  "s_waitcnt vmcnt(26) lgkmcnt(0)\n"
  "v_lshl_add_u32 v132, v129, 2, v130\n"
  "s_barrier\n"
  "v_mfma_f32_16x16x32_bf16 v[104:107], a[24:27], a[176:179], v[104:107]\n"
  "ds_read_b64_tr_b16 a[0:1], v202 offset:49152\n"
  "ds_read_b64_tr_b16 a[2:3], v202 offset:50176\n"
  "v_mfma_f32_16x16x32_bf16 v[104:107], a[56:59], a[180:183], v[104:107]\n"
  "v_and_b32 v133, 7, v132\n"
  "v_mfma_f32_16x16x32_bf16 v[108:111], a[24:27], a[184:187], v[108:111]\n"
  "buffer_load_dwordx4 v198, s[28:31], s45 offen offset:2048 lds\n"
  "v_mfma_f32_16x16x32_bf16 v[108:111], a[56:59], a[188:191], v[108:111]\n"
  "buffer_load_dwordx4 v199, s[28:31], s45 offen offset:3072 lds\n"
  "v_mfma_f32_16x16x32_bf16 v[112:115], a[28:31], a[160:163], v[112:115]\n"
  "ds_read_b64_tr_b16 a[32:33], v202 offset:57344\n"
  "ds_read_b64_tr_b16 a[34:35], v202 offset:58368\n"
  "v_mfma_f32_16x16x32_bf16 v[112:115], a[60:63], a[164:167], v[112:115]\n"
  "v_lshlrev_b32 v132, 8, v132\n"
  "v_mfma_f32_16x16x32_bf16 v[116:119], a[28:31], a[168:171], v[116:119]\n"
  "v_lshl_add_u32 v132, v131, 3, v132\n"
  "v_mfma_f32_16x16x32_bf16 v[116:119], a[60:63], a[172:175], v[116:119]\n"
  "v_add_u32 v132, 0x10000, v132\n"
  "v_mfma_f32_16x16x32_bf16 v[120:123], a[28:31], a[176:179], v[120:123]\n"
  "ds_read_b64_tr_b16 a[4:5], v203 offset:49152\n"
  "ds_read_b64_tr_b16 a[6:7], v203 offset:50176\n"
  "v_mfma_f32_16x16x32_bf16 v[120:123], a[60:63], a[180:183], v[120:123]\n"
  "v_xor_b32 v134, 0, v133\n"
  "v_mfma_f32_16x16x32_bf16 v[124:127], a[28:31], a[184:187], v[124:127]\n"
  "v_lshl_add_u32 v216, v134, 5, v132\n"
  "v_mfma_f32_16x16x32_bf16 v[124:127], a[60:63], a[188:191], v[124:127]\n"
  "s_waitcnt lgkmcnt(2)\n"
  "v_xor_b32 v134, 1, v133\n"
  "v_mfma_f32_16x16x32_bf16 v[0:3], a[0:3], a[128:131], v[0:3]\n"
  "s_add_u32 m0, s42, 49152\n"
  "ds_read_b64_tr_b16 a[36:37], v203 offset:57344\n"
  "ds_read_b64_tr_b16 a[38:39], v203 offset:58368\n"
  "v_mfma_f32_16x16x32_bf16 v[0:3], a[32:35], a[132:135], v[0:3]\n"
  "s_mov_b32 s46, s64\n"
  "v_lshl_add_u32 v217, v134, 5, v132\n"
  "v_mfma_f32_16x16x32_bf16 v[4:7], a[0:3], a[136:139], v[4:7]\n"
  "buffer_load_dwordx4 v192, s[48:51], s46 offen offset:0 lds\n"
  "v_mfma_f32_16x16x32_bf16 v[4:7], a[32:35], a[140:143], v[4:7]\n"
  "buffer_load_dwordx4 v193, s[48:51], s46 offen offset:1024 lds\n"
  "v_mfma_f32_16x16x32_bf16 v[8:11], a[0:3], a[144:147], v[8:11]\n"
  "ds_read_b64_tr_b16 a[8:9], v204 offset:49152\n"
  "ds_read_b64_tr_b16 a[10:11], v204 offset:50176\n"
  "v_mfma_f32_16x16x32_bf16 v[8:11], a[32:35], a[148:151], v[8:11]\n"
  "v_xor_b32 v134, 2, v133\n"
  "v_mfma_f32_16x16x32_bf16 v[12:15], a[0:3], a[152:155], v[12:15]\n"
  "buffer_load_dwordx4 v194, s[48:51], s46 offen offset:2048 lds\n"
  "v_mfma_f32_16x16x32_bf16 v[12:15], a[32:35], a[156:159], v[12:15]\n"
  "s_waitcnt lgkmcnt(2)\n"
  "buffer_load_dwordx4 v195, s[48:51], s46 offen offset:3072 lds\n"
  "v_mfma_f32_16x16x32_bf16 v[16:19], a[4:7], a[128:131], v[16:19]\n"
  "ds_read_b64_tr_b16 a[40:41], v204 offset:57344\n"
  "ds_read_b64_tr_b16 a[42:43], v204 offset:58368\n"
  "v_mfma_f32_16x16x32_bf16 v[16:19], a[36:39], a[132:135], v[16:19]\n"
  "v_lshl_add_u32 v218, v134, 5, v132\n"
  "v_mfma_f32_16x16x32_bf16 v[20:23], a[4:7], a[136:139], v[20:23]\n"
  "v_xor_b32 v134, 3, v133\n"
  "v_mfma_f32_16x16x32_bf16 v[20:23], a[36:39], a[140:143], v[20:23]\n"
  "v_lshl_add_u32 v219, v134, 5, v132\n"
  "v_mfma_f32_16x16x32_bf16 v[24:27], a[4:7], a[144:147], v[24:27]\n"
  "ds_read_b64_tr_b16 a[12:13], v205 offset:49152\n"
  "ds_read_b64_tr_b16 a[14:15], v205 offset:50176\n"
  "v_mfma_f32_16x16x32_bf16 v[24:27], a[36:39], a[148:151], v[24:27]\n"
  "v_xor_b32 v134, 4, v133\n"
  "v_mfma_f32_16x16x32_bf16 v[28:31], a[4:7], a[152:155], v[28:31]\n"
  "v_lshl_add_u32 v220, v134, 5, v132\n"
  "v_mfma_f32_16x16x32_bf16 v[28:31], a[36:39], a[156:159], v[28:31]\n"
  "s_waitcnt lgkmcnt(2)\n"
  "v_xor_b32 v134, 5, v133\n"
  "v_mfma_f32_16x16x32_bf16 v[32:35], a[8:11], a[128:131], v[32:35]\n"
  "ds_read_b64_tr_b16 a[44:45], v205 offset:57344\n"
  "ds_read_b64_tr_b16 a[46:47], v205 offset:58368\n"
  "v_mfma_f32_16x16x32_bf16 v[32:35], a[40:43], a[132:135], v[32:35]\n"
  "s_waitcnt vmcnt(16)\n"
  "v_lshl_add_u32 v221, v134, 5, v132\n"
  "v_mfma_f32_16x16x32_bf16 v[36:39], a[8:11], a[136:139], v[36:39]\n"
  "ds_read_b128 a[160:163], v200 offset:0\n"
  "v_mfma_f32_16x16x32_bf16 v[36:39], a[40:43], a[140:143], v[36:39]\n"
  "ds_read_b128 a[164:167], v201 offset:0\n"
  "v_mfma_f32_16x16x32_bf16 v[40:43], a[8:11], a[144:147], v[40:43]\n"
  "ds_read_b64_tr_b16 a[16:17], v206 offset:49152\n"
  "ds_read_b64_tr_b16 a[18:19], v206 offset:50176\n"
  "v_mfma_f32_16x16x32_bf16 v[40:43], a[40:43], a[148:151], v[40:43]\n"
  "v_xor_b32 v134, 6, v133\n"
  "v_mfma_f32_16x16x32_bf16 v[44:47], a[8:11], a[152:155], v[44:47]\n"
  "ds_read_b128 a[168:171], v200 offset:2048\n"
  "v_mfma_f32_16x16x32_bf16 v[44:47], a[40:43], a[156:159], v[44:47]\n"
  "s_waitcnt lgkmcnt(5)\n"
  "ds_read_b128 a[172:175], v201 offset:2048\n"
  "v_mfma_f32_16x16x32_bf16 v[48:51], a[12:15], a[128:131], v[48:51]\n"
  "ds_read_b64_tr_b16 a[48:49], v206 offset:57344\n"
  "ds_read_b64_tr_b16 a[50:51], v206 offset:58368\n"
  "v_mfma_f32_16x16x32_bf16 v[48:51], a[44:47], a[132:135], v[48:51]\n"
  "v_lshl_add_u32 v222, v134, 5, v132\n"
  "v_mfma_f32_16x16x32_bf16 v[52:55], a[12:15], a[136:139], v[52:55]\n"
  "ds_read_b128 a[176:179], v200 offset:4096\n"
  "v_mfma_f32_16x16x32_bf16 v[52:55], a[44:47], a[140:143], v[52:55]\n"
  "ds_read_b128 a[180:183], v201 offset:4096\n"
  "v_mfma_f32_16x16x32_bf16 v[56:59], a[12:15], a[144:147], v[56:59]\n"
  "ds_read_b64_tr_b16 a[20:21], v207 offset:49152\n"
  "ds_read_b64_tr_b16 a[22:23], v207 offset:50176\n"
  "v_mfma_f32_16x16x32_bf16 v[56:59], a[44:47], a[148:151], v[56:59]\n"
  "v_xor_b32 v134, 7, v133\n"
  "v_mfma_f32_16x16x32_bf16 v[60:63], a[12:15], a[152:155], v[60:63]\n"
  "ds_read_b128 a[184:187], v200 offset:6144\n"
  "v_mfma_f32_16x16x32_bf16 v[60:63], a[44:47], a[156:159], v[60:63]\n"
  "s_waitcnt lgkmcnt(5)\n"
  "ds_read_b128 a[188:191], v201 offset:6144\n"
  "v_mfma_f32_16x16x32_bf16 v[64:67], a[16:19], a[128:131], v[64:67]\n"
  "ds_read_b64_tr_b16 a[52:53], v207 offset:57344\n"
  "ds_read_b64_tr_b16 a[54:55], v207 offset:58368\n"
  "v_mfma_f32_16x16x32_bf16 v[64:67], a[48:51], a[132:135], v[64:67]\n"
  "s_waitcnt lgkmcnt(0)\n"
  "v_lshl_add_u32 v223, v134, 5, v132\n"
  "v_mfma_f32_16x16x32_bf16 v[68:71], a[16:19], a[136:139], v[68:71]\n"
  "v_and_b32 v130, 7, v236\n"
  "v_mfma_f32_16x16x32_bf16 v[68:71], a[48:51], a[140:143], v[68:71]\n"
  "v_lshrrev_b32 v131, 3, v236\n"
  "v_mfma_f32_16x16x32_bf16 v[72:75], a[16:19], a[144:147], v[72:75]\n"
  "s_add_u32 m0, s47, -32768\n"
  "ds_read_b64_tr_b16 a[24:25], v208 offset:49152\n"
  "ds_read_b64_tr_b16 a[26:27], v208 offset:50176\n"
  "v_mfma_f32_16x16x32_bf16 v[72:75], a[48:51], a[148:151], v[72:75]\n"
  "s_add_u32 s45, s57, 0\n"
  "v_lshlrev_b32 v132, 1, v129\n"
  "v_mfma_f32_16x16x32_bf16 v[76:79], a[16:19], a[152:155], v[76:79]\n"
  "buffer_load_dwordx4 v196, s[28:31], s45 offen offset:0 lds\n"
  "v_mfma_f32_16x16x32_bf16 v[76:79], a[48:51], a[156:159], v[76:79]\n"
  "buffer_load_dwordx4 v197, s[28:31], s45 offen offset:1024 lds\n"
  "v_mfma_f32_16x16x32_bf16 v[80:83], a[20:23], a[128:131], v[80:83]\n"
  "ds_read_b64_tr_b16 a[56:57], v208 offset:57344\n"
  "ds_read_b64_tr_b16 a[58:59], v208 offset:58368\n"
  "v_mfma_f32_16x16x32_bf16 v[80:83], a[52:55], a[132:135], v[80:83]\n"
  "v_xor_b32 v132, v132, v130\n"
  "v_mfma_f32_16x16x32_bf16 v[84:87], a[20:23], a[136:139], v[84:87]\n"
  "buffer_load_dwordx4 v198, s[28:31], s45 offen offset:2048 lds\n"
  "v_mfma_f32_16x16x32_bf16 v[84:87], a[52:55], a[140:143], v[84:87]\n"
  "buffer_load_dwordx4 v199, s[28:31], s45 offen offset:3072 lds\n"
  "v_mfma_f32_16x16x32_bf16 v[88:91], a[20:23], a[144:147], v[88:91]\n"
  "s_add_u32 m0, s47, -28672\n"
  "ds_read_b64_tr_b16 a[28:29], v209 offset:49152\n"
  "ds_read_b64_tr_b16 a[30:31], v209 offset:50176\n"
  "v_mfma_f32_16x16x32_bf16 v[88:91], a[52:55], a[148:151], v[88:91]\n"
  "s_add_u32 s45, s57, 32768\n"
  "v_lshlrev_b32 v132, 4, v132\n"
  "v_mfma_f32_16x16x32_bf16 v[92:95], a[20:23], a[152:155], v[92:95]\n"
  "buffer_load_dwordx4 v196, s[28:31], s45 offen offset:0 lds\n"
  "v_mfma_f32_16x16x32_bf16 v[92:95], a[52:55], a[156:159], v[92:95]\n"
  "s_waitcnt lgkmcnt(2)\n"
  "buffer_load_dwordx4 v197, s[28:31], s45 offen offset:1024 lds\n"
  "v_mfma_f32_16x16x32_bf16 v[96:99], a[24:27], a[128:131], v[96:99]\n"
  "ds_read_b64_tr_b16 a[60:61], v209 offset:57344\n"
  "ds_read_b64_tr_b16 a[62:63], v209 offset:58368\n"
  "v_mfma_f32_16x16x32_bf16 v[96:99], a[56:59], a[132:135], v[96:99]\n"
  "v_lshl_add_u32 v132, v131, 10, v132\n"
  "v_mfma_f32_16x16x32_bf16 v[100:103], a[24:27], a[136:139], v[100:103]\n"
  "v_add_u32 v244, 0, v132\n"
  "v_mfma_f32_16x16x32_bf16 v[100:103], a[56:59], a[140:143], v[100:103]\n"
  "s_waitcnt vmcnt(18) lgkmcnt(0)\n"
  "v_add_u32 v245, 7168, v132\n"
  "s_barrier\n"
  "v_mfma_f32_16x16x32_bf16 v[104:107], a[24:27], a[144:147], v[104:107]\n"
  "ds_read_b64_tr_b16 a[0:1], v202 offset:0\n"
  "ds_read_b64_tr_b16 a[2:3], v202 offset:1024\n"
  "v_mfma_f32_16x16x32_bf16 v[104:107], a[56:59], a[148:151], v[104:107]\n"
  "v_add_u32 v246, 14336, v132\n"
  "v_mfma_f32_16x16x32_bf16 v[108:111], a[24:27], a[152:155], v[108:111]\n"
  "buffer_load_dwordx4 v198, s[28:31], s45 offen offset:2048 lds\n"
  "v_mfma_f32_16x16x32_bf16 v[108:111], a[56:59], a[156:159], v[108:111]\n"
  "buffer_load_dwordx4 v199, s[28:31], s45 offen offset:3072 lds\n"
  "v_mfma_f32_16x16x32_bf16 v[112:115], a[28:31], a[128:131], v[112:115]\n"
  "ds_read_b64_tr_b16 a[32:33], v202 offset:8192\n"
  "ds_read_b64_tr_b16 a[34:35], v202 offset:9216\n"
  "v_mfma_f32_16x16x32_bf16 v[112:115], a[60:63], a[132:135], v[112:115]\n"
  "v_add_u32 v247, 21504, v132\n"
  "v_mfma_f32_16x16x32_bf16 v[116:119], a[28:31], a[136:139], v[116:119]\n"
  "v_mfma_f32_16x16x32_bf16 v[116:119], a[60:63], a[140:143], v[116:119]\n"
  "v_mfma_f32_16x16x32_bf16 v[120:123], a[28:31], a[144:147], v[120:123]\n"
  "ds_read_b64_tr_b16 a[4:5], v203 offset:0\n"
  "ds_read_b64_tr_b16 a[6:7], v203 offset:1024\n"
  "v_mfma_f32_16x16x32_bf16 v[120:123], a[60:63], a[148:151], v[120:123]\n"
  "v_mfma_f32_16x16x32_bf16 v[124:127], a[28:31], a[152:155], v[124:127]\n"
  "v_mfma_f32_16x16x32_bf16 v[124:127], a[60:63], a[156:159], v[124:127]\n"
  "s_waitcnt lgkmcnt(2)\n"
  "v_mfma_f32_16x16x32_bf16 v[0:3], a[0:3], a[160:163], v[0:3]\n"
  "s_add_u32 m0, s42, 81920\n"
  "ds_read_b64_tr_b16 a[36:37], v203 offset:8192\n"
  "ds_read_b64_tr_b16 a[38:39], v203 offset:9216\n"
  "v_mfma_f32_16x16x32_bf16 v[0:3], a[32:35], a[164:167], v[0:3]\n"
  "s_mov_b32 s46, s65\n"
  "v_mfma_f32_16x16x32_bf16 v[4:7], a[0:3], a[168:171], v[4:7]\n"
  "buffer_load_dwordx4 v192, s[48:51], s46 offen offset:0 lds\n"
  "v_mfma_f32_16x16x32_bf16 v[4:7], a[32:35], a[172:175], v[4:7]\n"
  "buffer_load_dwordx4 v193, s[48:51], s46 offen offset:1024 lds\n"
  "v_mfma_f32_16x16x32_bf16 v[8:11], a[0:3], a[176:179], v[8:11]\n"
  "ds_read_b64_tr_b16 a[8:9], v204 offset:0\n"
  "ds_read_b64_tr_b16 a[10:11], v204 offset:1024\n"
  "v_mfma_f32_16x16x32_bf16 v[8:11], a[32:35], a[180:183], v[8:11]\n"
  "v_mfma_f32_16x16x32_bf16 v[12:15], a[0:3], a[184:187], v[12:15]\n"
  "buffer_load_dwordx4 v194, s[48:51], s46 offen offset:2048 lds\n"
  "v_mfma_f32_16x16x32_bf16 v[12:15], a[32:35], a[188:191], v[12:15]\n"
  "s_waitcnt lgkmcnt(2)\n"
  "buffer_load_dwordx4 v195, s[48:51], s46 offen offset:3072 lds\n"
  "v_mfma_f32_16x16x32_bf16 v[16:19], a[4:7], a[160:163], v[16:19]\n"
  "ds_read_b64_tr_b16 a[40:41], v204 offset:8192\n"
  "ds_read_b64_tr_b16 a[42:43], v204 offset:9216\n"
  "v_mfma_f32_16x16x32_bf16 v[16:19], a[36:39], a[164:167], v[16:19]\n"
  "v_mfma_f32_16x16x32_bf16 v[20:23], a[4:7], a[168:171], v[20:23]\n"
  "v_mfma_f32_16x16x32_bf16 v[20:23], a[36:39], a[172:175], v[20:23]\n"
  "v_mfma_f32_16x16x32_bf16 v[24:27], a[4:7], a[176:179], v[24:27]\n"
  "ds_read_b64_tr_b16 a[12:13], v205 offset:0\n"
  "ds_read_b64_tr_b16 a[14:15], v205 offset:1024\n"
  "v_mfma_f32_16x16x32_bf16 v[24:27], a[36:39], a[180:183], v[24:27]\n"
  "v_mfma_f32_16x16x32_bf16 v[28:31], a[4:7], a[184:187], v[28:31]\n"
  "v_mfma_f32_16x16x32_bf16 v[28:31], a[36:39], a[188:191], v[28:31]\n"
  "s_waitcnt lgkmcnt(2)\n"
  "v_mfma_f32_16x16x32_bf16 v[32:35], a[8:11], a[160:163], v[32:35]\n"
  "ds_read_b64_tr_b16 a[44:45], v205 offset:8192\n"
  "ds_read_b64_tr_b16 a[46:47], v205 offset:9216\n"
  "v_mfma_f32_16x16x32_bf16 v[32:35], a[40:43], a[164:167], v[32:35]\n"
  "s_waitcnt vmcnt(16)\n"
  "v_mfma_f32_16x16x32_bf16 v[36:39], a[8:11], a[168:171], v[36:39]\n"
  "ds_read_b128 a[128:131], v200 offset:32768\n"
  "v_mfma_f32_16x16x32_bf16 v[36:39], a[40:43], a[172:175], v[36:39]\n"
  "ds_read_b128 a[132:135], v201 offset:32768\n"
  "v_mfma_f32_16x16x32_bf16 v[40:43], a[8:11], a[176:179], v[40:43]\n"
  "ds_read_b64_tr_b16 a[16:17], v206 offset:0\n"
  "ds_read_b64_tr_b16 a[18:19], v206 offset:1024\n"
  "v_mfma_f32_16x16x32_bf16 v[40:43], a[40:43], a[180:183], v[40:43]\n"
  "v_mfma_f32_16x16x32_bf16 v[44:47], a[8:11], a[184:187], v[44:47]\n"
  "ds_read_b128 a[136:139], v200 offset:34816\n"
  "v_mfma_f32_16x16x32_bf16 v[44:47], a[40:43], a[188:191], v[44:47]\n"
  "s_waitcnt lgkmcnt(5)\n"
  "ds_read_b128 a[140:143], v201 offset:34816\n"
  "v_mfma_f32_16x16x32_bf16 v[48:51], a[12:15], a[160:163], v[48:51]\n"
  "ds_read_b64_tr_b16 a[48:49], v206 offset:8192\n"
  "ds_read_b64_tr_b16 a[50:51], v206 offset:9216\n"
  "v_mfma_f32_16x16x32_bf16 v[48:51], a[44:47], a[164:167], v[48:51]\n"
  "v_mfma_f32_16x16x32_bf16 v[52:55], a[12:15], a[168:171], v[52:55]\n"
  "ds_read_b128 a[144:147], v200 offset:36864\n"
  "v_mfma_f32_16x16x32_bf16 v[52:55], a[44:47], a[172:175], v[52:55]\n"
  "ds_read_b128 a[148:151], v201 offset:36864\n"
  "v_mfma_f32_16x16x32_bf16 v[56:59], a[12:15], a[176:179], v[56:59]\n"
  "ds_read_b64_tr_b16 a[20:21], v207 offset:0\n"
  "ds_read_b64_tr_b16 a[22:23], v207 offset:1024\n"
  "v_mfma_f32_16x16x32_bf16 v[56:59], a[44:47], a[180:183], v[56:59]\n"
  "v_mfma_f32_16x16x32_bf16 v[60:63], a[12:15], a[184:187], v[60:63]\n"
  "ds_read_b128 a[152:155], v200 offset:38912\n"
  "v_mfma_f32_16x16x32_bf16 v[60:63], a[44:47], a[188:191], v[60:63]\n"
  "s_waitcnt lgkmcnt(5)\n"
  "ds_read_b128 a[156:159], v201 offset:38912\n"
  "v_mfma_f32_16x16x32_bf16 v[64:67], a[16:19], a[160:163], v[64:67]\n"
  "ds_read_b64_tr_b16 a[52:53], v207 offset:8192\n"
  "ds_read_b64_tr_b16 a[54:55], v207 offset:9216\n"
  "v_mfma_f32_16x16x32_bf16 v[64:67], a[48:51], a[164:167], v[64:67]\n"
  "s_waitcnt lgkmcnt(0)\n"
  "v_mfma_f32_16x16x32_bf16 v[68:71], a[16:19], a[168:171], v[68:71]\n"
  "v_mfma_f32_16x16x32_bf16 v[68:71], a[48:51], a[172:175], v[68:71]\n"
  "v_mfma_f32_16x16x32_bf16 v[72:75], a[16:19], a[176:179], v[72:75]\n"
  "s_add_u32 m0, s47, 0\n"
  "ds_read_b64_tr_b16 a[24:25], v208 offset:0\n"
  "ds_read_b64_tr_b16 a[26:27], v208 offset:1024\n"
  "v_mfma_f32_16x16x32_bf16 v[72:75], a[48:51], a[180:183], v[72:75]\n"
  "s_add_u32 s45, s58, 0\n"
  "v_mfma_f32_16x16x32_bf16 v[76:79], a[16:19], a[184:187], v[76:79]\n"
  "buffer_load_dwordx4 v196, s[28:31], s45 offen offset:0 lds\n"
  "v_mfma_f32_16x16x32_bf16 v[76:79], a[48:51], a[188:191], v[76:79]\n"
  "buffer_load_dwordx4 v197, s[28:31], s45 offen offset:1024 lds\n"
  "v_mfma_f32_16x16x32_bf16 v[80:83], a[20:23], a[160:163], v[80:83]\n"
  "ds_read_b64_tr_b16 a[56:57], v208 offset:8192\n"
  "ds_read_b64_tr_b16 a[58:59], v208 offset:9216\n"
  "v_mfma_f32_16x16x32_bf16 v[80:83], a[52:55], a[164:167], v[80:83]\n"
  "v_mfma_f32_16x16x32_bf16 v[84:87], a[20:23], a[168:171], v[84:87]\n"
  "buffer_load_dwordx4 v198, s[28:31], s45 offen offset:2048 lds\n"
  "v_mfma_f32_16x16x32_bf16 v[84:87], a[52:55], a[172:175], v[84:87]\n"
  "buffer_load_dwordx4 v199, s[28:31], s45 offen offset:3072 lds\n"
  "v_mfma_f32_16x16x32_bf16 v[88:91], a[20:23], a[176:179], v[88:91]\n"
  "s_add_u32 m0, s47, 4096\n"
  "ds_read_b64_tr_b16 a[28:29], v209 offset:0\n"
  "ds_read_b64_tr_b16 a[30:31], v209 offset:1024\n"
  "v_mfma_f32_16x16x32_bf16 v[88:91], a[52:55], a[180:183], v[88:91]\n"
  "s_add_u32 s45, s58, 32768\n"
  "v_mfma_f32_16x16x32_bf16 v[92:95], a[20:23], a[184:187], v[92:95]\n"
  "buffer_load_dwordx4 v196, s[28:31], s45 offen offset:0 lds\n"
  "v_mfma_f32_16x16x32_bf16 v[92:95], a[52:55], a[188:191], v[92:95]\n"
  "s_waitcnt lgkmcnt(2)\n"
  "buffer_load_dwordx4 v197, s[28:31], s45 offen offset:1024 lds\n"
  "v_mfma_f32_16x16x32_bf16 v[96:99], a[24:27], a[160:163], v[96:99]\n"
  "ds_read_b64_tr_b16 a[60:61], v209 offset:8192\n"
  "ds_read_b64_tr_b16 a[62:63], v209 offset:9216\n"
  "v_mfma_f32_16x16x32_bf16 v[96:99], a[56:59], a[164:167], v[96:99]\n"
  "v_mfma_f32_16x16x32_bf16 v[100:103], a[24:27], a[168:171], v[100:103]\n"
  "v_mfma_f32_16x16x32_bf16 v[100:103], a[56:59], a[172:175], v[100:103]\n"
  "s_waitcnt vmcnt(18) lgkmcnt(0)\n"
  "s_barrier\n"
  "v_mfma_f32_16x16x32_bf16 v[104:107], a[24:27], a[176:179], v[104:107]\n"
  "ds_read_b64_tr_b16 a[0:1], v202 offset:16384\n"
  "ds_read_b64_tr_b16 a[2:3], v202 offset:17408\n"
  "v_mfma_f32_16x16x32_bf16 v[104:107], a[56:59], a[180:183], v[104:107]\n"
  "v_mfma_f32_16x16x32_bf16 v[108:111], a[24:27], a[184:187], v[108:111]\n"
  "buffer_load_dwordx4 v198, s[28:31], s45 offen offset:2048 lds\n"
  "v_mfma_f32_16x16x32_bf16 v[108:111], a[56:59], a[188:191], v[108:111]\n"
  "buffer_load_dwordx4 v199, s[28:31], s45 offen offset:3072 lds\n"
  "v_mfma_f32_16x16x32_bf16 v[112:115], a[28:31], a[160:163], v[112:115]\n"
  "ds_read_b64_tr_b16 a[32:33], v202 offset:24576\n"
  "ds_read_b64_tr_b16 a[34:35], v202 offset:25600\n"
  "v_mfma_f32_16x16x32_bf16 v[112:115], a[60:63], a[164:167], v[112:115]\n"
  "v_mfma_f32_16x16x32_bf16 v[116:119], a[28:31], a[168:171], v[116:119]\n"
  "v_mfma_f32_16x16x32_bf16 v[116:119], a[60:63], a[172:175], v[116:119]\n"
  "v_mfma_f32_16x16x32_bf16 v[120:123], a[28:31], a[176:179], v[120:123]\n"
  "ds_read_b64_tr_b16 a[4:5], v203 offset:16384\n"
  "ds_read_b64_tr_b16 a[6:7], v203 offset:17408\n"
  "v_mfma_f32_16x16x32_bf16 v[120:123], a[60:63], a[180:183], v[120:123]\n"
  "v_mfma_f32_16x16x32_bf16 v[124:127], a[28:31], a[184:187], v[124:127]\n"
  "v_mfma_f32_16x16x32_bf16 v[124:127], a[60:63], a[188:191], v[124:127]\n"
  "s_waitcnt lgkmcnt(2)\n"
  "v_mfma_f32_16x16x32_bf16 v[0:3], a[0:3], a[128:131], v[0:3]\n"
  "s_add_u32 m0, s42, 32768\n"
  "ds_read_b64_tr_b16 a[36:37], v203 offset:24576\n"
  "ds_read_b64_tr_b16 a[38:39], v203 offset:25600\n"
  "v_mfma_f32_16x16x32_bf16 v[0:3], a[32:35], a[132:135], v[0:3]\n"
  "s_mov_b32 s46, s66\n"
  "v_mfma_f32_16x16x32_bf16 v[4:7], a[0:3], a[136:139], v[4:7]\n"
  "buffer_load_dwordx4 v192, s[48:51], s46 offen offset:0 lds\n"
  "v_mfma_f32_16x16x32_bf16 v[4:7], a[32:35], a[140:143], v[4:7]\n"
  "buffer_load_dwordx4 v193, s[48:51], s46 offen offset:1024 lds\n"
  "v_mfma_f32_16x16x32_bf16 v[8:11], a[0:3], a[144:147], v[8:11]\n"
  "ds_read_b64_tr_b16 a[8:9], v204 offset:16384\n"
  "ds_read_b64_tr_b16 a[10:11], v204 offset:17408\n"
  "v_mfma_f32_16x16x32_bf16 v[8:11], a[32:35], a[148:151], v[8:11]\n"
  "v_mfma_f32_16x16x32_bf16 v[12:15], a[0:3], a[152:155], v[12:15]\n"
  "buffer_load_dwordx4 v194, s[48:51], s46 offen offset:2048 lds\n"
  "v_mfma_f32_16x16x32_bf16 v[12:15], a[32:35], a[156:159], v[12:15]\n"
  "s_waitcnt lgkmcnt(2)\n"
  "buffer_load_dwordx4 v195, s[48:51], s46 offen offset:3072 lds\n"
  "v_mfma_f32_16x16x32_bf16 v[16:19], a[4:7], a[128:131], v[16:19]\n"
  "s_add_u32 m0, s42, 0\n"
  "ds_read_b64_tr_b16 a[40:41], v204 offset:24576\n"
  "ds_read_b64_tr_b16 a[42:43], v204 offset:25600\n"
  "v_mfma_f32_16x16x32_bf16 v[16:19], a[36:39], a[132:135], v[16:19]\n"
  "s_add_u32 s40, s42, 0\n"
  "v_mfma_f32_16x16x32_bf16 v[20:23], a[4:7], a[136:139], v[20:23]\n"
  "buffer_load_dwordx4 v228, s[20:23], s40 offen offset:0 nt lds\n"
  "v_mfma_f32_16x16x32_bf16 v[20:23], a[36:39], a[140:143], v[20:23]\n"
  "buffer_load_dwordx4 v229, s[20:23], s40 offen offset:1024 nt lds\n"
  "v_mfma_f32_16x16x32_bf16 v[24:27], a[4:7], a[144:147], v[24:27]\n"
  "ds_read_b64_tr_b16 a[12:13], v205 offset:16384\n"
  "ds_read_b64_tr_b16 a[14:15], v205 offset:17408\n"
  "v_mfma_f32_16x16x32_bf16 v[24:27], a[36:39], a[148:151], v[24:27]\n"
  "v_mfma_f32_16x16x32_bf16 v[28:31], a[4:7], a[152:155], v[28:31]\n"
  "buffer_load_dwordx4 v230, s[20:23], s40 offen offset:2048 nt lds\n"
  "v_mfma_f32_16x16x32_bf16 v[28:31], a[36:39], a[156:159], v[28:31]\n"
  "s_waitcnt lgkmcnt(2)\n"
  "buffer_load_dwordx4 v231, s[20:23], s40 offen offset:3072 nt lds\n"
  "v_mfma_f32_16x16x32_bf16 v[32:35], a[8:11], a[128:131], v[32:35]\n"
  "ds_read_b64_tr_b16 a[44:45], v205 offset:24576\n"
  "ds_read_b64_tr_b16 a[46:47], v205 offset:25600\n"
  "v_mfma_f32_16x16x32_bf16 v[32:35], a[40:43], a[132:135], v[32:35]\n"
  "s_waitcnt vmcnt(20)\n"
  "v_mfma_f32_16x16x32_bf16 v[36:39], a[8:11], a[136:139], v[36:39]\n"
  "ds_read_b128 a[160:163], v200 offset:0\n"
  "v_mfma_f32_16x16x32_bf16 v[36:39], a[40:43], a[140:143], v[36:39]\n"
  "ds_read_b128 a[164:167], v201 offset:0\n"
  "v_mfma_f32_16x16x32_bf16 v[40:43], a[8:11], a[144:147], v[40:43]\n"
  "ds_read_b64_tr_b16 a[16:17], v206 offset:16384\n"
  "ds_read_b64_tr_b16 a[18:19], v206 offset:17408\n"
  "v_mfma_f32_16x16x32_bf16 v[40:43], a[40:43], a[148:151], v[40:43]\n"
  "v_mfma_f32_16x16x32_bf16 v[44:47], a[8:11], a[152:155], v[44:47]\n"
  "ds_read_b128 a[168:171], v200 offset:2048\n"
  "v_mfma_f32_16x16x32_bf16 v[44:47], a[40:43], a[156:159], v[44:47]\n"
  "s_waitcnt lgkmcnt(5)\n"
  "ds_read_b128 a[172:175], v201 offset:2048\n"
  "v_mfma_f32_16x16x32_bf16 v[48:51], a[12:15], a[128:131], v[48:51]\n"
  "ds_read_b64_tr_b16 a[48:49], v206 offset:24576\n"
  "ds_read_b64_tr_b16 a[50:51], v206 offset:25600\n"
  "v_mfma_f32_16x16x32_bf16 v[48:51], a[44:47], a[132:135], v[48:51]\n"
  "v_mfma_f32_16x16x32_bf16 v[52:55], a[12:15], a[136:139], v[52:55]\n"
  "ds_read_b128 a[176:179], v200 offset:4096\n"
  "v_mfma_f32_16x16x32_bf16 v[52:55], a[44:47], a[140:143], v[52:55]\n"
  "ds_read_b128 a[180:183], v201 offset:4096\n"
  "v_mfma_f32_16x16x32_bf16 v[56:59], a[12:15], a[144:147], v[56:59]\n"
  "ds_read_b64_tr_b16 a[20:21], v207 offset:16384\n"
  "ds_read_b64_tr_b16 a[22:23], v207 offset:17408\n"
  "v_mfma_f32_16x16x32_bf16 v[56:59], a[44:47], a[148:151], v[56:59]\n"
  "v_mfma_f32_16x16x32_bf16 v[60:63], a[12:15], a[152:155], v[60:63]\n"
  "ds_read_b128 a[184:187], v200 offset:6144\n"
  "v_mfma_f32_16x16x32_bf16 v[60:63], a[44:47], a[156:159], v[60:63]\n"
  "s_waitcnt lgkmcnt(5)\n"
  "ds_read_b128 a[188:191], v201 offset:6144\n"
  "v_mfma_f32_16x16x32_bf16 v[64:67], a[16:19], a[128:131], v[64:67]\n"
  "ds_read_b64_tr_b16 a[52:53], v207 offset:24576\n"
  "ds_read_b64_tr_b16 a[54:55], v207 offset:25600\n"
  "v_mfma_f32_16x16x32_bf16 v[64:67], a[48:51], a[132:135], v[64:67]\n"
  "s_waitcnt lgkmcnt(0)\n"
  "v_mfma_f32_16x16x32_bf16 v[68:71], a[16:19], a[136:139], v[68:71]\n"
  "v_mfma_f32_16x16x32_bf16 v[68:71], a[48:51], a[140:143], v[68:71]\n"
  "v_mfma_f32_16x16x32_bf16 v[72:75], a[16:19], a[144:147], v[72:75]\n"
  "s_add_u32 m0, s47, -32768\n"
  "ds_read_b64_tr_b16 a[24:25], v208 offset:16384\n"
  "ds_read_b64_tr_b16 a[26:27], v208 offset:17408\n"
  "v_mfma_f32_16x16x32_bf16 v[72:75], a[48:51], a[148:151], v[72:75]\n"
  "s_add_u32 s45, s59, 0\n"
  "v_mfma_f32_16x16x32_bf16 v[76:79], a[16:19], a[152:155], v[76:79]\n"
  "buffer_load_dwordx4 v196, s[28:31], s45 offen offset:0 lds\n"
  "v_mfma_f32_16x16x32_bf16 v[76:79], a[48:51], a[156:159], v[76:79]\n"
  "buffer_load_dwordx4 v197, s[28:31], s45 offen offset:1024 lds\n"
  "v_mfma_f32_16x16x32_bf16 v[80:83], a[20:23], a[128:131], v[80:83]\n"
  "ds_read_b64_tr_b16 a[56:57], v208 offset:24576\n"
  "ds_read_b64_tr_b16 a[58:59], v208 offset:25600\n"
  "v_mfma_f32_16x16x32_bf16 v[80:83], a[52:55], a[132:135], v[80:83]\n"
  "v_mfma_f32_16x16x32_bf16 v[84:87], a[20:23], a[136:139], v[84:87]\n"
  "buffer_load_dwordx4 v198, s[28:31], s45 offen offset:2048 lds\n"
  "v_mfma_f32_16x16x32_bf16 v[84:87], a[52:55], a[140:143], v[84:87]\n"
  "buffer_load_dwordx4 v199, s[28:31], s45 offen offset:3072 lds\n"
  "v_mfma_f32_16x16x32_bf16 v[88:91], a[20:23], a[144:147], v[88:91]\n"
  "s_add_u32 m0, s47, -28672\n"
  "ds_read_b64_tr_b16 a[28:29], v209 offset:16384\n"
  "ds_read_b64_tr_b16 a[30:31], v209 offset:17408\n"
  "v_mfma_f32_16x16x32_bf16 v[88:91], a[52:55], a[148:151], v[88:91]\n"
  "s_add_u32 s45, s59, 32768\n"
  "v_mfma_f32_16x16x32_bf16 v[92:95], a[20:23], a[152:155], v[92:95]\n"
  "buffer_load_dwordx4 v196, s[28:31], s45 offen offset:0 lds\n"
  "v_mfma_f32_16x16x32_bf16 v[92:95], a[52:55], a[156:159], v[92:95]\n"
  "s_waitcnt lgkmcnt(2)\n"
  "buffer_load_dwordx4 v197, s[28:31], s45 offen offset:1024 lds\n"
  "v_mfma_f32_16x16x32_bf16 v[96:99], a[24:27], a[128:131], v[96:99]\n"
  "ds_read_b64_tr_b16 a[60:61], v209 offset:24576\n"
  "ds_read_b64_tr_b16 a[62:63], v209 offset:25600\n"
  "v_mfma_f32_16x16x32_bf16 v[96:99], a[56:59], a[132:135], v[96:99]\n"
  "v_mfma_f32_16x16x32_bf16 v[100:103], a[24:27], a[136:139], v[100:103]\n"
  "v_mfma_f32_16x16x32_bf16 v[100:103], a[56:59], a[140:143], v[100:103]\n"
  "s_waitcnt vmcnt(22) lgkmcnt(0)\n"
  "s_barrier\n"
  "v_mfma_f32_16x16x32_bf16 v[104:107], a[24:27], a[144:147], v[104:107]\n"
  "ds_read_b64_tr_b16 a[0:1], v202 offset:49152\n"
  "ds_read_b64_tr_b16 a[2:3], v202 offset:50176\n"
  "v_mfma_f32_16x16x32_bf16 v[104:107], a[56:59], a[148:151], v[104:107]\n"
  "v_mfma_f32_16x16x32_bf16 v[108:111], a[24:27], a[152:155], v[108:111]\n"
  "buffer_load_dwordx4 v198, s[28:31], s45 offen offset:2048 lds\n"
  "v_mfma_f32_16x16x32_bf16 v[108:111], a[56:59], a[156:159], v[108:111]\n"
  "buffer_load_dwordx4 v199, s[28:31], s45 offen offset:3072 lds\n"
  "v_mfma_f32_16x16x32_bf16 v[112:115], a[28:31], a[128:131], v[112:115]\n"
  "ds_read_b64_tr_b16 a[32:33], v202 offset:57344\n"
  "ds_read_b64_tr_b16 a[34:35], v202 offset:58368\n"
  "v_mfma_f32_16x16x32_bf16 v[112:115], a[60:63], a[132:135], v[112:115]\n"
  "v_mfma_f32_16x16x32_bf16 v[116:119], a[28:31], a[136:139], v[116:119]\n"
  "v_mfma_f32_16x16x32_bf16 v[116:119], a[60:63], a[140:143], v[116:119]\n"
  "v_mfma_f32_16x16x32_bf16 v[120:123], a[28:31], a[144:147], v[120:123]\n"
  "ds_read_b64_tr_b16 a[4:5], v203 offset:49152\n"
  "ds_read_b64_tr_b16 a[6:7], v203 offset:50176\n"
  "v_mfma_f32_16x16x32_bf16 v[120:123], a[60:63], a[148:151], v[120:123]\n"
  "v_mfma_f32_16x16x32_bf16 v[124:127], a[28:31], a[152:155], v[124:127]\n"
  "v_mfma_f32_16x16x32_bf16 v[124:127], a[60:63], a[156:159], v[124:127]\n"
  "s_waitcnt lgkmcnt(2)\n"
  "v_mfma_f32_16x16x32_bf16 v[0:3], a[0:3], a[160:163], v[0:3]\n"
  "s_add_u32 m0, s42, 49152\n"
  "ds_read_b64_tr_b16 a[36:37], v203 offset:57344\n"
  "ds_read_b64_tr_b16 a[38:39], v203 offset:58368\n"
  "v_mfma_f32_16x16x32_bf16 v[0:3], a[32:35], a[164:167], v[0:3]\n"
  "s_mov_b32 s46, s67\n"
  "v_mfma_f32_16x16x32_bf16 v[4:7], a[0:3], a[168:171], v[4:7]\n"
  "buffer_load_dwordx4 v192, s[48:51], s46 offen offset:0 lds\n"
  "v_mfma_f32_16x16x32_bf16 v[4:7], a[32:35], a[172:175], v[4:7]\n"
  "buffer_load_dwordx4 v193, s[48:51], s46 offen offset:1024 lds\n"
  "v_mfma_f32_16x16x32_bf16 v[8:11], a[0:3], a[176:179], v[8:11]\n"
  "ds_read_b64_tr_b16 a[8:9], v204 offset:49152\n"
  "ds_read_b64_tr_b16 a[10:11], v204 offset:50176\n"
  "v_mfma_f32_16x16x32_bf16 v[8:11], a[32:35], a[180:183], v[8:11]\n"
  "v_mfma_f32_16x16x32_bf16 v[12:15], a[0:3], a[184:187], v[12:15]\n"
  "buffer_load_dwordx4 v194, s[48:51], s46 offen offset:2048 lds\n"
  "v_mfma_f32_16x16x32_bf16 v[12:15], a[32:35], a[188:191], v[12:15]\n"
  "s_waitcnt lgkmcnt(2)\n"
  "buffer_load_dwordx4 v195, s[48:51], s46 offen offset:3072 lds\n"
  "v_mfma_f32_16x16x32_bf16 v[16:19], a[4:7], a[160:163], v[16:19]\n"
  "s_add_u32 m0, s42, 16384\n"
  "ds_read_b64_tr_b16 a[40:41], v204 offset:57344\n"
  "ds_read_b64_tr_b16 a[42:43], v204 offset:58368\n"
  "v_mfma_f32_16x16x32_bf16 v[16:19], a[36:39], a[164:167], v[16:19]\n"
  "s_add_u32 s40, s42, 16384\n"
  "v_mfma_f32_16x16x32_bf16 v[20:23], a[4:7], a[168:171], v[20:23]\n"
  "buffer_load_dwordx4 v228, s[20:23], s40 offen offset:0 nt lds\n"
  "v_mfma_f32_16x16x32_bf16 v[20:23], a[36:39], a[172:175], v[20:23]\n"
  "buffer_load_dwordx4 v229, s[20:23], s40 offen offset:1024 nt lds\n"
  "v_mfma_f32_16x16x32_bf16 v[24:27], a[4:7], a[176:179], v[24:27]\n"
  "ds_read_b64_tr_b16 a[12:13], v205 offset:49152\n"
  "ds_read_b64_tr_b16 a[14:15], v205 offset:50176\n"
  "v_mfma_f32_16x16x32_bf16 v[24:27], a[36:39], a[180:183], v[24:27]\n"
  "v_mfma_f32_16x16x32_bf16 v[28:31], a[4:7], a[184:187], v[28:31]\n"
  "buffer_load_dwordx4 v230, s[20:23], s40 offen offset:2048 nt lds\n"
  "v_mfma_f32_16x16x32_bf16 v[28:31], a[36:39], a[188:191], v[28:31]\n"
  "s_waitcnt lgkmcnt(2)\n"
  "buffer_load_dwordx4 v231, s[20:23], s40 offen offset:3072 nt lds\n"
  "v_mfma_f32_16x16x32_bf16 v[32:35], a[8:11], a[160:163], v[32:35]\n"
  "s_lshr_b32 s68, s75, 2\n"
  "ds_read_b64_tr_b16 a[44:45], v205 offset:57344\n"
  "ds_read_b64_tr_b16 a[46:47], v205 offset:58368\n"
  "v_mfma_f32_16x16x32_bf16 v[32:35], a[40:43], a[164:167], v[32:35]\n"
  "s_waitcnt vmcnt(24)\n"
  "v_mfma_f32_16x16x32_bf16 v[36:39], a[8:11], a[168:171], v[36:39]\n"
  "s_lshl_b32 s68, s68, 7\n"
  "ds_read_b128 a[128:131], v200 offset:32768\n"
  "v_mfma_f32_16x16x32_bf16 v[36:39], a[40:43], a[172:175], v[36:39]\n"
  "s_mov_b64 s[76:77], %[wo]\n"
  "ds_read_b128 a[132:135], v201 offset:32768\n"
  "v_mfma_f32_16x16x32_bf16 v[40:43], a[8:11], a[176:179], v[40:43]\n"
  "s_add_u32 s76, s76, s68\n"
  "s_addc_u32 s77, s77, 0\n"
  "ds_read_b64_tr_b16 a[16:17], v206 offset:49152\n"
  "ds_read_b64_tr_b16 a[18:19], v206 offset:50176\n"
  "v_mfma_f32_16x16x32_bf16 v[40:43], a[40:43], a[180:183], v[40:43]\n"
  "s_and_b32 s77, s77, 0xffff\n"
  "v_mfma_f32_16x16x32_bf16 v[44:47], a[8:11], a[184:187], v[44:47]\n"
  "s_mov_b32 s78, 0\n"
  "ds_read_b128 a[136:139], v200 offset:34816\n"
  "v_mfma_f32_16x16x32_bf16 v[44:47], a[40:43], a[188:191], v[44:47]\n"
  "s_waitcnt lgkmcnt(5)\n"
  "ds_read_b128 a[140:143], v201 offset:34816\n"
  "v_mfma_f32_16x16x32_bf16 v[48:51], a[12:15], a[160:163], v[48:51]\n"
  "s_mov_b32 s79, 0x20000\n"
  "ds_read_b64_tr_b16 a[48:49], v206 offset:57344\n"
  "ds_read_b64_tr_b16 a[50:51], v206 offset:58368\n"
  "v_mfma_f32_16x16x32_bf16 v[48:51], a[44:47], a[164:167], v[48:51]\n"
  "s_sub_u32 s78, 0x400000, s68\n"
  "v_mfma_f32_16x16x32_bf16 v[52:55], a[12:15], a[168:171], v[52:55]\n"
  "s_lshl_b32 s52, %[wid], 20\n"
  "ds_read_b128 a[144:147], v200 offset:36864\n"
  "v_mfma_f32_16x16x32_bf16 v[52:55], a[44:47], a[172:175], v[52:55]\n"
  "s_add_u32 s52, s52, 0\n"
  "ds_read_b128 a[148:151], v201 offset:36864\n"
  "v_mfma_f32_16x16x32_bf16 v[56:59], a[12:15], a[176:179], v[56:59]\n"
  "s_lshl_b32 s53, %[wid], 20\n"
  "ds_read_b64_tr_b16 a[20:21], v207 offset:49152\n"
  "ds_read_b64_tr_b16 a[22:23], v207 offset:50176\n"
  "v_mfma_f32_16x16x32_bf16 v[56:59], a[44:47], a[180:183], v[56:59]\n"
  "s_add_u32 s53, s53, 131072\n"
  "v_mfma_f32_16x16x32_bf16 v[60:63], a[12:15], a[184:187], v[60:63]\n"
  "s_lshl_b32 s54, %[wid], 20\n"
  "ds_read_b128 a[152:155], v200 offset:38912\n"
  "v_mfma_f32_16x16x32_bf16 v[60:63], a[44:47], a[188:191], v[60:63]\n"
  "s_waitcnt lgkmcnt(5)\n"
  "ds_read_b128 a[156:159], v201 offset:38912\n"
  "v_mfma_f32_16x16x32_bf16 v[64:67], a[16:19], a[160:163], v[64:67]\n"
  "s_add_u32 s54, s54, 262144\n"
  "ds_read_b64_tr_b16 a[52:53], v207 offset:57344\n"
  "ds_read_b64_tr_b16 a[54:55], v207 offset:58368\n"
  "v_mfma_f32_16x16x32_bf16 v[64:67], a[48:51], a[164:167], v[64:67]\n"
  "s_lshl_b32 s55, %[wid], 20\n"
  "v_mfma_f32_16x16x32_bf16 v[68:71], a[16:19], a[168:171], v[68:71]\n"
  "s_add_u32 s55, s55, 393216\n"
  "v_mfma_f32_16x16x32_bf16 v[68:71], a[48:51], a[172:175], v[68:71]\n"
  "s_lshl_b32 s56, %[wid], 20\n"
  "v_mfma_f32_16x16x32_bf16 v[72:75], a[16:19], a[176:179], v[72:75]\n"
  "s_add_u32 m0, s43, 0\n"
  "ds_read_b64_tr_b16 a[24:25], v208 offset:49152\n"
  "ds_read_b64_tr_b16 a[26:27], v208 offset:50176\n"
  "v_mfma_f32_16x16x32_bf16 v[72:75], a[48:51], a[180:183], v[72:75]\n"
  "s_add_u32 s41, s42, 0\n"
  "v_mfma_f32_16x16x32_bf16 v[76:79], a[16:19], a[184:187], v[76:79]\n"
  "s_add_u32 s56, s56, 524288\n"
  "buffer_load_dwordx4 v232, s[24:27], s41 offen offset:0 nt lds\n"
  "v_mfma_f32_16x16x32_bf16 v[76:79], a[48:51], a[188:191], v[76:79]\n"
  "s_waitcnt lgkmcnt(2)\n"
  "buffer_load_dwordx4 v233, s[24:27], s41 offen offset:1024 nt lds\n"
  "v_mfma_f32_16x16x32_bf16 v[80:83], a[20:23], a[160:163], v[80:83]\n"
  "s_lshl_b32 s57, %[wid], 20\n"
  "ds_read_b64_tr_b16 a[56:57], v208 offset:57344\n"
  "ds_read_b64_tr_b16 a[58:59], v208 offset:58368\n"
  "v_mfma_f32_16x16x32_bf16 v[80:83], a[52:55], a[164:167], v[80:83]\n"
  "s_add_u32 s57, s57, 655360\n"
  "v_mfma_f32_16x16x32_bf16 v[84:87], a[20:23], a[168:171], v[84:87]\n"
  "s_lshl_b32 s58, %[wid], 20\n"
  "buffer_load_dwordx4 v234, s[24:27], s41 offen offset:2048 nt lds\n"
  "v_mfma_f32_16x16x32_bf16 v[84:87], a[52:55], a[172:175], v[84:87]\n"
  "s_add_u32 s58, s58, 786432\n"
  "buffer_load_dwordx4 v235, s[24:27], s41 offen offset:3072 nt lds\n"
  "v_mfma_f32_16x16x32_bf16 v[88:91], a[20:23], a[176:179], v[88:91]\n"
  "s_lshl_b32 s59, %[wid], 20\n"
  "ds_read_b64_tr_b16 a[28:29], v209 offset:49152\n"
  "ds_read_b64_tr_b16 a[30:31], v209 offset:50176\n"
  "v_mfma_f32_16x16x32_bf16 v[88:91], a[52:55], a[180:183], v[88:91]\n"
  "s_add_u32 s59, s59, 917504\n"
  "v_mfma_f32_16x16x32_bf16 v[92:95], a[20:23], a[184:187], v[92:95]\n"
  "v_mfma_f32_16x16x32_bf16 v[92:95], a[52:55], a[188:191], v[92:95]\n"
  "s_waitcnt lgkmcnt(2)\n"
  "v_mfma_f32_16x16x32_bf16 v[96:99], a[24:27], a[160:163], v[96:99]\n"
  "ds_read_b64_tr_b16 a[60:61], v209 offset:57344\n"
  "ds_read_b64_tr_b16 a[62:63], v209 offset:58368\n"
  "v_mfma_f32_16x16x32_bf16 v[96:99], a[56:59], a[164:167], v[96:99]\n"
  "v_mfma_f32_16x16x32_bf16 v[100:103], a[24:27], a[168:171], v[100:103]\n"
  "v_mfma_f32_16x16x32_bf16 v[100:103], a[56:59], a[172:175], v[100:103]\n"
  "s_waitcnt vmcnt(24) lgkmcnt(0)\n"
  "s_barrier\n"
  "v_mfma_f32_16x16x32_bf16 v[104:107], a[24:27], a[176:179], v[104:107]\n"
  "ds_read_b64_tr_b16 a[0:1], v202 offset:0\n"
  "ds_read_b64_tr_b16 a[2:3], v202 offset:1024\n"
  "v_mfma_f32_16x16x32_bf16 v[104:107], a[56:59], a[180:183], v[104:107]\n"
  "v_mfma_f32_16x16x32_bf16 v[108:111], a[24:27], a[184:187], v[108:111]\n"
  "v_mfma_f32_16x16x32_bf16 v[108:111], a[56:59], a[188:191], v[108:111]\n"
  "v_mfma_f32_16x16x32_bf16 v[112:115], a[28:31], a[160:163], v[112:115]\n"
  "ds_read_b64_tr_b16 a[32:33], v202 offset:8192\n"
  "ds_read_b64_tr_b16 a[34:35], v202 offset:9216\n"
  "v_mfma_f32_16x16x32_bf16 v[112:115], a[60:63], a[164:167], v[112:115]\n"
  "v_mfma_f32_16x16x32_bf16 v[116:119], a[28:31], a[168:171], v[116:119]\n"
  "v_mfma_f32_16x16x32_bf16 v[116:119], a[60:63], a[172:175], v[116:119]\n"
  "v_mfma_f32_16x16x32_bf16 v[120:123], a[28:31], a[176:179], v[120:123]\n"
  "ds_read_b64_tr_b16 a[4:5], v203 offset:0\n"
  "ds_read_b64_tr_b16 a[6:7], v203 offset:1024\n"
  "v_mfma_f32_16x16x32_bf16 v[120:123], a[60:63], a[180:183], v[120:123]\n"
  "v_mfma_f32_16x16x32_bf16 v[124:127], a[28:31], a[184:187], v[124:127]\n"
  "v_mfma_f32_16x16x32_bf16 v[124:127], a[60:63], a[188:191], v[124:127]\n"
  "s_waitcnt lgkmcnt(2)\n"
  "v_mfma_f32_16x16x32_bf16 v[0:3], a[0:3], a[128:131], v[0:3]\n"
  "ds_read_b64_tr_b16 a[36:37], v203 offset:8192\n"
  "ds_read_b64_tr_b16 a[38:39], v203 offset:9216\n"
  "v_mfma_f32_16x16x32_bf16 v[0:3], a[32:35], a[132:135], v[0:3]\n"
  "v_mfma_f32_16x16x32_bf16 v[4:7], a[0:3], a[136:139], v[4:7]\n"
  "v_mfma_f32_16x16x32_bf16 v[4:7], a[32:35], a[140:143], v[4:7]\n"
  "v_mfma_f32_16x16x32_bf16 v[8:11], a[0:3], a[144:147], v[8:11]\n"
  "ds_read_b64_tr_b16 a[8:9], v204 offset:0\n"
  "ds_read_b64_tr_b16 a[10:11], v204 offset:1024\n"
  "v_mfma_f32_16x16x32_bf16 v[8:11], a[32:35], a[148:151], v[8:11]\n"
  "v_mfma_f32_16x16x32_bf16 v[12:15], a[0:3], a[152:155], v[12:15]\n"
  "v_mfma_f32_16x16x32_bf16 v[12:15], a[32:35], a[156:159], v[12:15]\n"
  "s_waitcnt lgkmcnt(2)\n"
  "v_mfma_f32_16x16x32_bf16 v[16:19], a[4:7], a[128:131], v[16:19]\n"
  "ds_read_b64_tr_b16 a[40:41], v204 offset:8192\n"
  "ds_read_b64_tr_b16 a[42:43], v204 offset:9216\n"
  "v_mfma_f32_16x16x32_bf16 v[16:19], a[36:39], a[132:135], v[16:19]\n"
  "v_mfma_f32_16x16x32_bf16 v[20:23], a[4:7], a[136:139], v[20:23]\n"
  "v_mfma_f32_16x16x32_bf16 v[20:23], a[36:39], a[140:143], v[20:23]\n"
  "v_mfma_f32_16x16x32_bf16 v[24:27], a[4:7], a[144:147], v[24:27]\n"
  "ds_read_b64_tr_b16 a[12:13], v205 offset:0\n"
  "ds_read_b64_tr_b16 a[14:15], v205 offset:1024\n"
  "v_mfma_f32_16x16x32_bf16 v[24:27], a[36:39], a[148:151], v[24:27]\n"
  "v_mfma_f32_16x16x32_bf16 v[28:31], a[4:7], a[152:155], v[28:31]\n"
  "v_mfma_f32_16x16x32_bf16 v[28:31], a[36:39], a[156:159], v[28:31]\n"
  "s_waitcnt lgkmcnt(2)\n"
  "v_mfma_f32_16x16x32_bf16 v[32:35], a[8:11], a[128:131], v[32:35]\n"
  "ds_read_b64_tr_b16 a[44:45], v205 offset:8192\n"
  "ds_read_b64_tr_b16 a[46:47], v205 offset:9216\n"
  "v_mfma_f32_16x16x32_bf16 v[32:35], a[40:43], a[132:135], v[32:35]\n"
  "s_waitcnt vmcnt(12)\n"
  "v_mfma_f32_16x16x32_bf16 v[36:39], a[8:11], a[136:139], v[36:39]\n"
  "ds_read_b128 a[160:163], v200 offset:0\n"
  "v_mfma_f32_16x16x32_bf16 v[36:39], a[40:43], a[140:143], v[36:39]\n"
  "ds_read_b128 a[164:167], v201 offset:0\n"
  "v_mfma_f32_16x16x32_bf16 v[40:43], a[8:11], a[144:147], v[40:43]\n"
  "ds_read_b64_tr_b16 a[16:17], v206 offset:0\n"
  "ds_read_b64_tr_b16 a[18:19], v206 offset:1024\n"
  "v_mfma_f32_16x16x32_bf16 v[40:43], a[40:43], a[148:151], v[40:43]\n"
  "v_mfma_f32_16x16x32_bf16 v[44:47], a[8:11], a[152:155], v[44:47]\n"
  "ds_read_b128 a[168:171], v200 offset:2048\n"
  "v_mfma_f32_16x16x32_bf16 v[44:47], a[40:43], a[156:159], v[44:47]\n"
  "s_waitcnt lgkmcnt(5)\n"
  "ds_read_b128 a[172:175], v201 offset:2048\n"
  "v_mfma_f32_16x16x32_bf16 v[48:51], a[12:15], a[128:131], v[48:51]\n"
  "ds_read_b64_tr_b16 a[48:49], v206 offset:8192\n"
  "ds_read_b64_tr_b16 a[50:51], v206 offset:9216\n"
  "v_mfma_f32_16x16x32_bf16 v[48:51], a[44:47], a[132:135], v[48:51]\n"
  "v_mfma_f32_16x16x32_bf16 v[52:55], a[12:15], a[136:139], v[52:55]\n"
  "ds_read_b128 a[176:179], v200 offset:4096\n"
  "v_mfma_f32_16x16x32_bf16 v[52:55], a[44:47], a[140:143], v[52:55]\n"
  "ds_read_b128 a[180:183], v201 offset:4096\n"
  "v_mfma_f32_16x16x32_bf16 v[56:59], a[12:15], a[144:147], v[56:59]\n"
  "ds_read_b64_tr_b16 a[20:21], v207 offset:0\n"
  "ds_read_b64_tr_b16 a[22:23], v207 offset:1024\n"
  "v_mfma_f32_16x16x32_bf16 v[56:59], a[44:47], a[148:151], v[56:59]\n"
  "v_mfma_f32_16x16x32_bf16 v[60:63], a[12:15], a[152:155], v[60:63]\n"
  "ds_read_b128 a[184:187], v200 offset:6144\n"
  "v_mfma_f32_16x16x32_bf16 v[60:63], a[44:47], a[156:159], v[60:63]\n"
  "s_waitcnt lgkmcnt(5)\n"
  "ds_read_b128 a[188:191], v201 offset:6144\n"
  "v_mfma_f32_16x16x32_bf16 v[64:67], a[16:19], a[128:131], v[64:67]\n"
  "ds_read_b64_tr_b16 a[52:53], v207 offset:8192\n"
  "ds_read_b64_tr_b16 a[54:55], v207 offset:9216\n"
  "v_mfma_f32_16x16x32_bf16 v[64:67], a[48:51], a[132:135], v[64:67]\n"
  "v_mfma_f32_16x16x32_bf16 v[68:71], a[16:19], a[136:139], v[68:71]\n"
  "v_mfma_f32_16x16x32_bf16 v[68:71], a[48:51], a[140:143], v[68:71]\n"
  "v_mfma_f32_16x16x32_bf16 v[72:75], a[16:19], a[144:147], v[72:75]\n"
  "ds_read_b64_tr_b16 a[24:25], v208 offset:0\n"
  "ds_read_b64_tr_b16 a[26:27], v208 offset:1024\n"
  "v_mfma_f32_16x16x32_bf16 v[72:75], a[48:51], a[148:151], v[72:75]\n"
  "v_mfma_f32_16x16x32_bf16 v[76:79], a[16:19], a[152:155], v[76:79]\n"
  "v_mfma_f32_16x16x32_bf16 v[76:79], a[48:51], a[156:159], v[76:79]\n"
  "s_waitcnt lgkmcnt(2)\n"
  "v_mfma_f32_16x16x32_bf16 v[80:83], a[20:23], a[128:131], v[80:83]\n"
  "ds_read_b64_tr_b16 a[56:57], v208 offset:8192\n"
  "ds_read_b64_tr_b16 a[58:59], v208 offset:9216\n"
  "v_mfma_f32_16x16x32_bf16 v[80:83], a[52:55], a[132:135], v[80:83]\n"
  "v_mfma_f32_16x16x32_bf16 v[84:87], a[20:23], a[136:139], v[84:87]\n"
  "v_mfma_f32_16x16x32_bf16 v[84:87], a[52:55], a[140:143], v[84:87]\n"
  "v_mfma_f32_16x16x32_bf16 v[88:91], a[20:23], a[144:147], v[88:91]\n"
  "ds_read_b64_tr_b16 a[28:29], v209 offset:0\n"
  "ds_read_b64_tr_b16 a[30:31], v209 offset:1024\n"
  "v_mfma_f32_16x16x32_bf16 v[88:91], a[52:55], a[148:151], v[88:91]\n"
  "v_mfma_f32_16x16x32_bf16 v[92:95], a[20:23], a[152:155], v[92:95]\n"
  "v_mfma_f32_16x16x32_bf16 v[92:95], a[52:55], a[156:159], v[92:95]\n"
  "s_waitcnt lgkmcnt(2)\n"
  "v_mfma_f32_16x16x32_bf16 v[96:99], a[24:27], a[128:131], v[96:99]\n"
  "ds_read_b64_tr_b16 a[60:61], v209 offset:8192\n"
  "ds_read_b64_tr_b16 a[62:63], v209 offset:9216\n"
  "v_mfma_f32_16x16x32_bf16 v[96:99], a[56:59], a[132:135], v[96:99]\n"
  "v_mfma_f32_16x16x32_bf16 v[100:103], a[24:27], a[136:139], v[100:103]\n"
  "v_mfma_f32_16x16x32_bf16 v[100:103], a[56:59], a[140:143], v[100:103]\n"
  "s_waitcnt vmcnt(8) lgkmcnt(0)\n"
  "s_barrier\n"
  "v_mfma_f32_16x16x32_bf16 v[104:107], a[24:27], a[144:147], v[104:107]\n"
  "ds_read_b64_tr_b16 a[0:1], v202 offset:16384\n"
  "ds_read_b64_tr_b16 a[2:3], v202 offset:17408\n"
  "v_mfma_f32_16x16x32_bf16 v[104:107], a[56:59], a[148:151], v[104:107]\n"
  "v_mfma_f32_16x16x32_bf16 v[108:111], a[24:27], a[152:155], v[108:111]\n"
  "v_mfma_f32_16x16x32_bf16 v[108:111], a[56:59], a[156:159], v[108:111]\n"
  "v_mfma_f32_16x16x32_bf16 v[112:115], a[28:31], a[128:131], v[112:115]\n"
  "ds_read_b64_tr_b16 a[32:33], v202 offset:24576\n"
  "ds_read_b64_tr_b16 a[34:35], v202 offset:25600\n"
  "v_mfma_f32_16x16x32_bf16 v[112:115], a[60:63], a[132:135], v[112:115]\n"
  "v_mfma_f32_16x16x32_bf16 v[116:119], a[28:31], a[136:139], v[116:119]\n"
  "v_mfma_f32_16x16x32_bf16 v[116:119], a[60:63], a[140:143], v[116:119]\n"
  "v_mfma_f32_16x16x32_bf16 v[120:123], a[28:31], a[144:147], v[120:123]\n"
  "ds_read_b64_tr_b16 a[4:5], v203 offset:16384\n"
  "ds_read_b64_tr_b16 a[6:7], v203 offset:17408\n"
  "v_mfma_f32_16x16x32_bf16 v[120:123], a[60:63], a[148:151], v[120:123]\n"
  "v_mfma_f32_16x16x32_bf16 v[124:127], a[28:31], a[152:155], v[124:127]\n"
  "v_mfma_f32_16x16x32_bf16 v[124:127], a[60:63], a[156:159], v[124:127]\n"
  "s_waitcnt lgkmcnt(2)\n"
  "v_mfma_f32_16x16x32_bf16 v[0:3], a[0:3], a[160:163], v[0:3]\n"
  "s_add_u32 m0, s42, 32768\n"
  "ds_read_b64_tr_b16 a[36:37], v203 offset:24576\n"
  "ds_read_b64_tr_b16 a[38:39], v203 offset:25600\n"
  "v_mfma_f32_16x16x32_bf16 v[0:3], a[32:35], a[164:167], v[0:3]\n"
  "s_add_u32 s40, s42, 32768\n"
  "v_mfma_f32_16x16x32_bf16 v[4:7], a[0:3], a[168:171], v[4:7]\n"
  "buffer_load_dwordx4 v228, s[20:23], s40 offen offset:0 nt lds\n"
  "v_mfma_f32_16x16x32_bf16 v[4:7], a[32:35], a[172:175], v[4:7]\n"
  "buffer_load_dwordx4 v229, s[20:23], s40 offen offset:1024 nt lds\n"
  "v_mfma_f32_16x16x32_bf16 v[8:11], a[0:3], a[176:179], v[8:11]\n"
  "ds_read_b64_tr_b16 a[8:9], v204 offset:16384\n"
  "ds_read_b64_tr_b16 a[10:11], v204 offset:17408\n"
  "v_mfma_f32_16x16x32_bf16 v[8:11], a[32:35], a[180:183], v[8:11]\n"
  "v_mfma_f32_16x16x32_bf16 v[12:15], a[0:3], a[184:187], v[12:15]\n"
  "buffer_load_dwordx4 v230, s[20:23], s40 offen offset:2048 nt lds\n"
  "v_mfma_f32_16x16x32_bf16 v[12:15], a[32:35], a[188:191], v[12:15]\n"
  "s_waitcnt lgkmcnt(2)\n"
  "buffer_load_dwordx4 v231, s[20:23], s40 offen offset:3072 nt lds\n"
  "v_mfma_f32_16x16x32_bf16 v[16:19], a[4:7], a[160:163], v[16:19]\n"
  "ds_read_b64_tr_b16 a[40:41], v204 offset:24576\n"
  "ds_read_b64_tr_b16 a[42:43], v204 offset:25600\n"
  "v_mfma_f32_16x16x32_bf16 v[16:19], a[36:39], a[164:167], v[16:19]\n"
  "v_mfma_f32_16x16x32_bf16 v[20:23], a[4:7], a[168:171], v[20:23]\n"
  "v_mfma_f32_16x16x32_bf16 v[20:23], a[36:39], a[172:175], v[20:23]\n"
  "v_mfma_f32_16x16x32_bf16 v[24:27], a[4:7], a[176:179], v[24:27]\n"
  "ds_read_b64_tr_b16 a[12:13], v205 offset:16384\n"
  "ds_read_b64_tr_b16 a[14:15], v205 offset:17408\n"
  "v_mfma_f32_16x16x32_bf16 v[24:27], a[36:39], a[180:183], v[24:27]\n"
  "v_mfma_f32_16x16x32_bf16 v[28:31], a[4:7], a[184:187], v[28:31]\n"
  "v_mfma_f32_16x16x32_bf16 v[28:31], a[36:39], a[188:191], v[28:31]\n"
  "s_waitcnt lgkmcnt(2)\n"
  "v_mfma_f32_16x16x32_bf16 v[32:35], a[8:11], a[160:163], v[32:35]\n"
  "ds_read_b64_tr_b16 a[44:45], v205 offset:24576\n"
  "ds_read_b64_tr_b16 a[46:47], v205 offset:25600\n"
  "v_mfma_f32_16x16x32_bf16 v[32:35], a[40:43], a[164:167], v[32:35]\n"
  "v_mfma_f32_16x16x32_bf16 v[36:39], a[8:11], a[168:171], v[36:39]\n"
  "v_mfma_f32_16x16x32_bf16 v[36:39], a[40:43], a[172:175], v[36:39]\n"
  "v_mfma_f32_16x16x32_bf16 v[40:43], a[8:11], a[176:179], v[40:43]\n"
  "ds_read_b64_tr_b16 a[16:17], v206 offset:16384\n"
  "ds_read_b64_tr_b16 a[18:19], v206 offset:17408\n"
  "v_mfma_f32_16x16x32_bf16 v[40:43], a[40:43], a[180:183], v[40:43]\n"
  "v_mfma_f32_16x16x32_bf16 v[44:47], a[8:11], a[184:187], v[44:47]\n"
  "v_mfma_f32_16x16x32_bf16 v[44:47], a[40:43], a[188:191], v[44:47]\n"
  "s_waitcnt lgkmcnt(2)\n"
  "v_mfma_f32_16x16x32_bf16 v[48:51], a[12:15], a[160:163], v[48:51]\n"
  "ds_read_b64_tr_b16 a[48:49], v206 offset:24576\n"
  "ds_read_b64_tr_b16 a[50:51], v206 offset:25600\n"
  "v_mfma_f32_16x16x32_bf16 v[48:51], a[44:47], a[164:167], v[48:51]\n"
  "v_mfma_f32_16x16x32_bf16 v[52:55], a[12:15], a[168:171], v[52:55]\n"
  "v_mfma_f32_16x16x32_bf16 v[52:55], a[44:47], a[172:175], v[52:55]\n"
  "v_mfma_f32_16x16x32_bf16 v[56:59], a[12:15], a[176:179], v[56:59]\n"
  "ds_read_b64_tr_b16 a[20:21], v207 offset:16384\n"
  "ds_read_b64_tr_b16 a[22:23], v207 offset:17408\n"
  "v_mfma_f32_16x16x32_bf16 v[56:59], a[44:47], a[180:183], v[56:59]\n"
  "v_mfma_f32_16x16x32_bf16 v[60:63], a[12:15], a[184:187], v[60:63]\n"
  "v_mfma_f32_16x16x32_bf16 v[60:63], a[44:47], a[188:191], v[60:63]\n"
  "s_waitcnt lgkmcnt(2)\n"
  "v_mfma_f32_16x16x32_bf16 v[64:67], a[16:19], a[160:163], v[64:67]\n"
  "ds_read_b64_tr_b16 a[52:53], v207 offset:24576\n"
  "ds_read_b64_tr_b16 a[54:55], v207 offset:25600\n"
  "v_mfma_f32_16x16x32_bf16 v[64:67], a[48:51], a[164:167], v[64:67]\n"
  "v_mfma_f32_16x16x32_bf16 v[68:71], a[16:19], a[168:171], v[68:71]\n"
  "v_mfma_f32_16x16x32_bf16 v[68:71], a[48:51], a[172:175], v[68:71]\n"
  "v_mfma_f32_16x16x32_bf16 v[72:75], a[16:19], a[176:179], v[72:75]\n"
  "ds_read_b64_tr_b16 a[24:25], v208 offset:16384\n"
  "ds_read_b64_tr_b16 a[26:27], v208 offset:17408\n"
  "v_mfma_f32_16x16x32_bf16 v[72:75], a[48:51], a[180:183], v[72:75]\n"
  "v_mfma_f32_16x16x32_bf16 v[76:79], a[16:19], a[184:187], v[76:79]\n"
  "v_mfma_f32_16x16x32_bf16 v[76:79], a[48:51], a[188:191], v[76:79]\n"
  "s_waitcnt lgkmcnt(2)\n"
  "v_mfma_f32_16x16x32_bf16 v[80:83], a[20:23], a[160:163], v[80:83]\n"
  "ds_read_b64_tr_b16 a[56:57], v208 offset:24576\n"
  "ds_read_b64_tr_b16 a[58:59], v208 offset:25600\n"
  "v_mfma_f32_16x16x32_bf16 v[80:83], a[52:55], a[164:167], v[80:83]\n"
  "v_mfma_f32_16x16x32_bf16 v[84:87], a[20:23], a[168:171], v[84:87]\n"
  "v_mfma_f32_16x16x32_bf16 v[84:87], a[52:55], a[172:175], v[84:87]\n"
  "v_mfma_f32_16x16x32_bf16 v[88:91], a[20:23], a[176:179], v[88:91]\n"
  "ds_read_b64_tr_b16 a[28:29], v209 offset:16384\n"
  "ds_read_b64_tr_b16 a[30:31], v209 offset:17408\n"
  "v_mfma_f32_16x16x32_bf16 v[88:91], a[52:55], a[180:183], v[88:91]\n"
  "v_mfma_f32_16x16x32_bf16 v[92:95], a[20:23], a[184:187], v[92:95]\n"
  "v_mfma_f32_16x16x32_bf16 v[92:95], a[52:55], a[188:191], v[92:95]\n"
  "s_waitcnt lgkmcnt(2)\n"
  "v_mfma_f32_16x16x32_bf16 v[96:99], a[24:27], a[160:163], v[96:99]\n"
  "ds_read_b64_tr_b16 a[60:61], v209 offset:24576\n"
  "ds_read_b64_tr_b16 a[62:63], v209 offset:25600\n"
  "v_mfma_f32_16x16x32_bf16 v[96:99], a[56:59], a[164:167], v[96:99]\n"
  "v_mfma_f32_16x16x32_bf16 v[100:103], a[24:27], a[168:171], v[100:103]\n"
  "v_mfma_f32_16x16x32_bf16 v[100:103], a[56:59], a[172:175], v[100:103]\n"
  "v_mfma_f32_16x16x32_bf16 v[104:107], a[24:27], a[176:179], v[104:107]\n"
  "v_mfma_f32_16x16x32_bf16 v[104:107], a[56:59], a[180:183], v[104:107]\n"
  "v_mfma_f32_16x16x32_bf16 v[108:111], a[24:27], a[184:187], v[108:111]\n"
  "v_mfma_f32_16x16x32_bf16 v[108:111], a[56:59], a[188:191], v[108:111]\n"
  "s_waitcnt lgkmcnt(0)\n"
  "v_mfma_f32_16x16x32_bf16 v[112:115], a[28:31], a[160:163], v[112:115]\n"
  "v_mfma_f32_16x16x32_bf16 v[112:115], a[60:63], a[164:167], v[112:115]\n"
  "v_mfma_f32_16x16x32_bf16 v[116:119], a[28:31], a[168:171], v[116:119]\n"
  "v_mfma_f32_16x16x32_bf16 v[116:119], a[60:63], a[172:175], v[116:119]\n"
  "v_mfma_f32_16x16x32_bf16 v[120:123], a[28:31], a[176:179], v[120:123]\n"
  "v_mfma_f32_16x16x32_bf16 v[120:123], a[60:63], a[180:183], v[120:123]\n"
  "v_mfma_f32_16x16x32_bf16 v[124:127], a[28:31], a[184:187], v[124:127]\n"
  "v_mfma_f32_16x16x32_bf16 v[124:127], a[60:63], a[188:191], v[124:127]\n"
  "s_waitcnt lgkmcnt(0)\n"
  "s_barrier\n"
  "s_mov_b32 s68, 0x3e0293ee\n"
  "s_mov_b32 s69, 0x3e0293ee\n"
  "v_pk_mul_f32 v[0:1], v[0:1], s[68:69]\n"
  "v_pk_mul_f32 v[2:3], v[2:3], s[68:69]\n"
  "v_pk_mul_f32 v[16:17], v[16:17], s[68:69]\n"
  "v_pk_mul_f32 v[18:19], v[18:19], s[68:69]\n"
  "v_cvt_pk_bf16_f32 v128, v0, v1\n"
  "v_cvt_pk_bf16_f32 v129, v2, v3\n"
  "v_cvt_pk_bf16_f32 v130, v16, v17\n"
  "v_cvt_pk_bf16_f32 v131, v18, v19\n"
  "v_pk_mul_f32 v[32:33], v[32:33], s[68:69]\n"
  "v_pk_mul_f32 v[34:35], v[34:35], s[68:69]\n"
  "v_pk_mul_f32 v[48:49], v[48:49], s[68:69]\n"
  "v_pk_mul_f32 v[50:51], v[50:51], s[68:69]\n"
  "v_cvt_pk_bf16_f32 v132, v32, v33\n"
  "v_cvt_pk_bf16_f32 v133, v34, v35\n"
  "v_cvt_pk_bf16_f32 v134, v48, v49\n"
  "v_cvt_pk_bf16_f32 v135, v50, v51\n"
  "v_pk_mul_f32 v[64:65], v[64:65], s[68:69]\n"
  "v_pk_mul_f32 v[66:67], v[66:67], s[68:69]\n"
  "v_pk_mul_f32 v[80:81], v[80:81], s[68:69]\n"
  "v_pk_mul_f32 v[82:83], v[82:83], s[68:69]\n"
  "v_cvt_pk_bf16_f32 v136, v64, v65\n"
  "v_cvt_pk_bf16_f32 v137, v66, v67\n"
  "v_cvt_pk_bf16_f32 v138, v80, v81\n"
  "v_cvt_pk_bf16_f32 v139, v82, v83\n"
  "v_pk_mul_f32 v[96:97], v[96:97], s[68:69]\n"
  "v_pk_mul_f32 v[98:99], v[98:99], s[68:69]\n"
  "v_pk_mul_f32 v[112:113], v[112:113], s[68:69]\n"
  "v_pk_mul_f32 v[114:115], v[114:115], s[68:69]\n"
  "v_cvt_pk_bf16_f32 v140, v96, v97\n"
  "v_cvt_pk_bf16_f32 v141, v98, v99\n"
  "v_cvt_pk_bf16_f32 v142, v112, v113\n"
  "v_cvt_pk_bf16_f32 v143, v114, v115\n"
  "v_pk_mul_f32 v[4:5], v[4:5], s[68:69]\n"
  "v_pk_mul_f32 v[6:7], v[6:7], s[68:69]\n"
  "v_pk_mul_f32 v[20:21], v[20:21], s[68:69]\n"
  "v_pk_mul_f32 v[22:23], v[22:23], s[68:69]\n"
  "v_cvt_pk_bf16_f32 v144, v4, v5\n"
  "v_cvt_pk_bf16_f32 v145, v6, v7\n"
  "v_cvt_pk_bf16_f32 v146, v20, v21\n"
  "v_cvt_pk_bf16_f32 v147, v22, v23\n"
  "v_pk_mul_f32 v[36:37], v[36:37], s[68:69]\n"
  "v_pk_mul_f32 v[38:39], v[38:39], s[68:69]\n"
  "v_pk_mul_f32 v[52:53], v[52:53], s[68:69]\n"
  "v_pk_mul_f32 v[54:55], v[54:55], s[68:69]\n"
  "v_cvt_pk_bf16_f32 v148, v36, v37\n"
  "v_cvt_pk_bf16_f32 v149, v38, v39\n"
  "v_cvt_pk_bf16_f32 v150, v52, v53\n"
  "v_cvt_pk_bf16_f32 v151, v54, v55\n"
  "v_pk_mul_f32 v[68:69], v[68:69], s[68:69]\n"
  "v_pk_mul_f32 v[70:71], v[70:71], s[68:69]\n"
  "v_pk_mul_f32 v[84:85], v[84:85], s[68:69]\n"
  "v_pk_mul_f32 v[86:87], v[86:87], s[68:69]\n"
  "v_cvt_pk_bf16_f32 v152, v68, v69\n"
  "v_cvt_pk_bf16_f32 v153, v70, v71\n"
  "v_cvt_pk_bf16_f32 v154, v84, v85\n"
  "v_cvt_pk_bf16_f32 v155, v86, v87\n"
  "v_pk_mul_f32 v[100:101], v[100:101], s[68:69]\n"
  "v_pk_mul_f32 v[102:103], v[102:103], s[68:69]\n"
  "v_pk_mul_f32 v[116:117], v[116:117], s[68:69]\n"
  "v_pk_mul_f32 v[118:119], v[118:119], s[68:69]\n"
  "v_cvt_pk_bf16_f32 v156, v100, v101\n"
  "v_cvt_pk_bf16_f32 v157, v102, v103\n"
  "v_cvt_pk_bf16_f32 v158, v116, v117\n"
  "v_cvt_pk_bf16_f32 v159, v118, v119\n"
  "v_pk_mul_f32 v[8:9], v[8:9], s[68:69]\n"
  "v_pk_mul_f32 v[10:11], v[10:11], s[68:69]\n"
  "v_pk_mul_f32 v[24:25], v[24:25], s[68:69]\n"
  "v_pk_mul_f32 v[26:27], v[26:27], s[68:69]\n"
  "v_cvt_pk_bf16_f32 v160, v8, v9\n"
  "v_cvt_pk_bf16_f32 v161, v10, v11\n"
  "v_cvt_pk_bf16_f32 v162, v24, v25\n"
  "v_cvt_pk_bf16_f32 v163, v26, v27\n"
  "v_pk_mul_f32 v[40:41], v[40:41], s[68:69]\n"
  "v_pk_mul_f32 v[42:43], v[42:43], s[68:69]\n"
  "v_pk_mul_f32 v[56:57], v[56:57], s[68:69]\n"
  "v_pk_mul_f32 v[58:59], v[58:59], s[68:69]\n"
  "v_cvt_pk_bf16_f32 v164, v40, v41\n"
  "v_cvt_pk_bf16_f32 v165, v42, v43\n"
  "v_cvt_pk_bf16_f32 v166, v56, v57\n"
  "v_cvt_pk_bf16_f32 v167, v58, v59\n"
  "v_pk_mul_f32 v[72:73], v[72:73], s[68:69]\n"
  "v_pk_mul_f32 v[74:75], v[74:75], s[68:69]\n"
  "v_pk_mul_f32 v[88:89], v[88:89], s[68:69]\n"
  "v_pk_mul_f32 v[90:91], v[90:91], s[68:69]\n"
  "v_cvt_pk_bf16_f32 v168, v72, v73\n"
  "v_cvt_pk_bf16_f32 v169, v74, v75\n"
  "v_cvt_pk_bf16_f32 v170, v88, v89\n"
  "v_cvt_pk_bf16_f32 v171, v90, v91\n"
  "v_pk_mul_f32 v[104:105], v[104:105], s[68:69]\n"
  "v_pk_mul_f32 v[106:107], v[106:107], s[68:69]\n"
  "v_pk_mul_f32 v[120:121], v[120:121], s[68:69]\n"
  "v_pk_mul_f32 v[122:123], v[122:123], s[68:69]\n"
  "v_cvt_pk_bf16_f32 v172, v104, v105\n"
  "v_cvt_pk_bf16_f32 v173, v106, v107\n"
  "v_cvt_pk_bf16_f32 v174, v120, v121\n"
  "v_cvt_pk_bf16_f32 v175, v122, v123\n"
  "v_pk_mul_f32 v[12:13], v[12:13], s[68:69]\n"
  "v_pk_mul_f32 v[14:15], v[14:15], s[68:69]\n"
  "v_pk_mul_f32 v[28:29], v[28:29], s[68:69]\n"
  "v_pk_mul_f32 v[30:31], v[30:31], s[68:69]\n"
  "v_cvt_pk_bf16_f32 v176, v12, v13\n"
  "v_cvt_pk_bf16_f32 v177, v14, v15\n"
  "v_cvt_pk_bf16_f32 v178, v28, v29\n"
  "v_cvt_pk_bf16_f32 v179, v30, v31\n"
  "v_pk_mul_f32 v[44:45], v[44:45], s[68:69]\n"
  "v_pk_mul_f32 v[46:47], v[46:47], s[68:69]\n"
  "v_pk_mul_f32 v[60:61], v[60:61], s[68:69]\n"
  "v_pk_mul_f32 v[62:63], v[62:63], s[68:69]\n"
  "v_cvt_pk_bf16_f32 v180, v44, v45\n"
  "v_cvt_pk_bf16_f32 v181, v46, v47\n"
  "v_cvt_pk_bf16_f32 v182, v60, v61\n"
  "v_cvt_pk_bf16_f32 v183, v62, v63\n"
  "v_pk_mul_f32 v[76:77], v[76:77], s[68:69]\n"
  "v_pk_mul_f32 v[78:79], v[78:79], s[68:69]\n"
  "v_pk_mul_f32 v[92:93], v[92:93], s[68:69]\n"
  "v_pk_mul_f32 v[94:95], v[94:95], s[68:69]\n"
  "v_cvt_pk_bf16_f32 v184, v76, v77\n"
  "v_cvt_pk_bf16_f32 v185, v78, v79\n"
  "v_cvt_pk_bf16_f32 v186, v92, v93\n"
  "v_cvt_pk_bf16_f32 v187, v94, v95\n"
  "v_pk_mul_f32 v[108:109], v[108:109], s[68:69]\n"
  "v_pk_mul_f32 v[110:111], v[110:111], s[68:69]\n"
  "v_pk_mul_f32 v[124:125], v[124:125], s[68:69]\n"
  "v_pk_mul_f32 v[126:127], v[126:127], s[68:69]\n"
  "v_cvt_pk_bf16_f32 v188, v108, v109\n"
  "v_cvt_pk_bf16_f32 v189, v110, v111\n"
  "v_cvt_pk_bf16_f32 v190, v124, v125\n"
  "v_cvt_pk_bf16_f32 v191, v126, v127\n"
  "s_nop 1\n"
  "v_permlane32_swap_b32 v128, v130\n"
  "v_permlane32_swap_b32 v129, v131\n"
  "v_permlane32_swap_b32 v132, v134\n"
  "v_permlane32_swap_b32 v133, v135\n"
  "v_permlane32_swap_b32 v136, v138\n"
  "v_permlane32_swap_b32 v137, v139\n"
  "v_permlane32_swap_b32 v140, v142\n"
  "v_permlane32_swap_b32 v141, v143\n"
  "v_permlane32_swap_b32 v144, v146\n"
  "v_permlane32_swap_b32 v145, v147\n"
  "v_permlane32_swap_b32 v148, v150\n"
  "v_permlane32_swap_b32 v149, v151\n"
  "v_permlane32_swap_b32 v152, v154\n"
  "v_permlane32_swap_b32 v153, v155\n"
  "v_permlane32_swap_b32 v156, v158\n"
  "v_permlane32_swap_b32 v157, v159\n"
  "v_permlane32_swap_b32 v160, v162\n"
  "v_permlane32_swap_b32 v161, v163\n"
  "v_permlane32_swap_b32 v164, v166\n"
  "v_permlane32_swap_b32 v165, v167\n"
  "v_permlane32_swap_b32 v168, v170\n"
  "v_permlane32_swap_b32 v169, v171\n"
  "v_permlane32_swap_b32 v172, v174\n"
  "v_permlane32_swap_b32 v173, v175\n"
  "v_permlane32_swap_b32 v176, v178\n"
  "v_permlane32_swap_b32 v177, v179\n"
  "v_permlane32_swap_b32 v180, v182\n"
  "v_permlane32_swap_b32 v181, v183\n"
  "v_permlane32_swap_b32 v184, v186\n"
  "v_permlane32_swap_b32 v185, v187\n"
  "v_permlane32_swap_b32 v188, v190\n"
  "v_permlane32_swap_b32 v189, v191\n"
  "s_nop 1\n"
  "v_permlane16_swap_b32 v128, v130\n"
  "v_permlane16_swap_b32 v129, v131\n"
  "v_permlane16_swap_b32 v132, v134\n"
  "v_permlane16_swap_b32 v133, v135\n"
  "v_permlane16_swap_b32 v136, v138\n"
  "v_permlane16_swap_b32 v137, v139\n"
  "v_permlane16_swap_b32 v140, v142\n"
  "v_permlane16_swap_b32 v141, v143\n"
  "v_permlane16_swap_b32 v144, v146\n"
  "v_permlane16_swap_b32 v145, v147\n"
  "v_permlane16_swap_b32 v148, v150\n"
  "v_permlane16_swap_b32 v149, v151\n"
  "v_permlane16_swap_b32 v152, v154\n"
  "v_permlane16_swap_b32 v153, v155\n"
  "v_permlane16_swap_b32 v156, v158\n"
  "v_permlane16_swap_b32 v157, v159\n"
  "v_permlane16_swap_b32 v160, v162\n"
  "v_permlane16_swap_b32 v161, v163\n"
  "v_permlane16_swap_b32 v164, v166\n"
  "v_permlane16_swap_b32 v165, v167\n"
  "v_permlane16_swap_b32 v168, v170\n"
  "v_permlane16_swap_b32 v169, v171\n"
  "v_permlane16_swap_b32 v172, v174\n"
  "v_permlane16_swap_b32 v173, v175\n"
  "v_permlane16_swap_b32 v176, v178\n"
  "v_permlane16_swap_b32 v177, v179\n"
  "v_permlane16_swap_b32 v180, v182\n"
  "v_permlane16_swap_b32 v181, v183\n"
  "v_permlane16_swap_b32 v184, v186\n"
  "v_permlane16_swap_b32 v185, v187\n"
  "v_permlane16_swap_b32 v188, v190\n"
  "v_permlane16_swap_b32 v189, v191\n"
  "s_nop 1\n"
  "s_add_u32 s40, s42, 49152\n"
  "s_add_u32 s41, s42, 16384\n"
  "s_waitcnt vmcnt(24)\n"
  "s_barrier\n"
  "ds_read_b128 a[144:147], v224 offset:0\n"
  "ds_read_b128 a[148:151], v225 offset:0\n"
  "ds_read_b128 a[152:155], v226 offset:0\n"
  "ds_read_b128 a[156:159], v227 offset:0\n"
  "ds_read_b128 a[160:163], v224 offset:4096\n"
  "ds_read_b128 a[164:167], v225 offset:4096\n"
  "ds_read_b128 a[168:171], v226 offset:4096\n"
  "ds_read_b128 a[172:175], v227 offset:4096\n"
  "v_and_b32 v237, 15, v236\n"
  "v_cmp_eq_u32 vcc, 0, v237\n"
  "v_mov_b32 v243, 0x3f803f80\n"
  "s_nop 1\n"
  "v_cndmask_b32 v208, 0, v243, vcc\n"
  "v_cndmask_b32 v209, 0, v243, vcc\n"
  "v_cndmask_b32 v210, 0, v243, vcc\n"
  "v_cndmask_b32 v211, 0, v243, vcc\n"
  "s_lshl_b32 s45, %[bid], 14\n"
  "s_mov_b64 s[68:69], %[wo]\n"
  "s_add_u32 s68, s68, s45\n"
  "s_addc_u32 s69, s69, 0\n"
  "v_lshlrev_b32 v241, 7, v236\n"
  "s_lshl_b32 s45, %[wid], 12\n"
  "v_add_u32 v241, s45, v241\n"
  "v_and_b32 v241, 0x3fff, v241\n"
  "global_load_dword v248, v241, s[68:69]\n"
  "s_waitcnt lgkmcnt(0)\n"
  "v_mfma_f32_16x16x32_bf16 v[0:3], a[144:147], v[128:131], 0\n"
  "v_mfma_f32_16x16x32_bf16 v[0:3], a[148:151], v[132:135], v[0:3]\n"
  "v_mfma_f32_16x16x32_bf16 v[0:3], a[152:155], v[136:139], v[0:3]\n"
  "v_mfma_f32_16x16x32_bf16 v[0:3], a[156:159], v[140:143], v[0:3]\n"
  "v_mfma_f32_16x16x32_bf16 v[8:11], a[144:147], v[144:147], 0\n"
  "v_mfma_f32_16x16x32_bf16 v[8:11], a[148:151], v[148:151], v[8:11]\n"
  "v_mfma_f32_16x16x32_bf16 v[8:11], a[152:155], v[152:155], v[8:11]\n"
  "v_mfma_f32_16x16x32_bf16 v[8:11], a[156:159], v[156:159], v[8:11]\n"
  "v_mfma_f32_16x16x32_bf16 v[16:19], a[144:147], v[160:163], 0\n"
  "v_mfma_f32_16x16x32_bf16 v[16:19], a[148:151], v[164:167], v[16:19]\n"
  "v_mfma_f32_16x16x32_bf16 v[16:19], a[152:155], v[168:171], v[16:19]\n"
  "v_mfma_f32_16x16x32_bf16 v[16:19], a[156:159], v[172:175], v[16:19]\n"
  "v_mfma_f32_16x16x32_bf16 v[24:27], a[144:147], v[176:179], 0\n"
  "v_mfma_f32_16x16x32_bf16 v[24:27], a[148:151], v[180:183], v[24:27]\n"
  "v_mfma_f32_16x16x32_bf16 v[24:27], a[152:155], v[184:187], v[24:27]\n"
  "v_mfma_f32_16x16x32_bf16 v[24:27], a[156:159], v[188:191], v[24:27]\n"
  "v_mfma_f32_16x16x32_bf16 v[4:7], a[160:163], v[128:131], 0\n"
  "v_mfma_f32_16x16x32_bf16 v[4:7], a[164:167], v[132:135], v[4:7]\n"
  "v_mfma_f32_16x16x32_bf16 v[4:7], a[168:171], v[136:139], v[4:7]\n"
  "v_mfma_f32_16x16x32_bf16 v[4:7], a[172:175], v[140:143], v[4:7]\n"
  "v_mfma_f32_16x16x32_bf16 v[12:15], a[160:163], v[144:147], 0\n"
  "v_mfma_f32_16x16x32_bf16 v[12:15], a[164:167], v[148:151], v[12:15]\n"
  "v_mfma_f32_16x16x32_bf16 v[12:15], a[168:171], v[152:155], v[12:15]\n"
  "v_mfma_f32_16x16x32_bf16 v[12:15], a[172:175], v[156:159], v[12:15]\n"
  "v_mfma_f32_16x16x32_bf16 v[20:23], a[160:163], v[160:163], 0\n"
  "v_mfma_f32_16x16x32_bf16 v[20:23], a[164:167], v[164:167], v[20:23]\n"
  "v_mfma_f32_16x16x32_bf16 v[20:23], a[168:171], v[168:171], v[20:23]\n"
  "v_mfma_f32_16x16x32_bf16 v[20:23], a[172:175], v[172:175], v[20:23]\n"
  "v_mfma_f32_16x16x32_bf16 v[28:31], a[160:163], v[176:179], 0\n"
  "v_mfma_f32_16x16x32_bf16 v[28:31], a[164:167], v[180:183], v[28:31]\n"
  "v_mfma_f32_16x16x32_bf16 v[28:31], a[168:171], v[184:187], v[28:31]\n"
  "v_mfma_f32_16x16x32_bf16 v[28:31], a[172:175], v[188:191], v[28:31]\n"
  "ds_read_b128 a[144:147], v224 offset:8192\n"
  "ds_read_b128 a[148:151], v225 offset:8192\n"
  "ds_read_b128 a[152:155], v226 offset:8192\n"
  "ds_read_b128 a[156:159], v227 offset:8192\n"
  "ds_read_b128 a[160:163], v224 offset:12288\n"
  "ds_read_b128 a[164:167], v225 offset:12288\n"
  "ds_read_b128 a[168:171], v226 offset:12288\n"
  "ds_read_b128 a[172:175], v227 offset:12288\n"
  "s_nop 3\n"
  "v_max3_f32 v64, v0, v1, v2\n"
  "v_max3_f32 v65, v8, v9, v10\n"
  "v_max3_f32 v66, v16, v17, v18\n"
  "v_max3_f32 v67, v24, v25, v26\n"
  "v_max3_f32 v64, v64, v3, v4\n"
  "v_max3_f32 v65, v65, v11, v12\n"
  "v_max3_f32 v66, v66, v19, v20\n"
  "v_max3_f32 v67, v67, v27, v28\n"
  "v_max3_f32 v64, v64, v5, v6\n"
  "v_max3_f32 v65, v65, v13, v14\n"
  "v_max3_f32 v66, v66, v21, v22\n"
  "v_max3_f32 v67, v67, v29, v30\n"
  "v_max_f32 v64, v64, v7\n"
  "v_max_f32 v65, v65, v15\n"
  "v_max_f32 v66, v66, v23\n"
  "v_max_f32 v67, v67, v31\n"
  "v_mov_b32 v68, v64\n"
  "v_mov_b32 v69, v65\n"
  "v_mov_b32 v70, v66\n"
  "v_mov_b32 v71, v67\n"
  "s_nop 0\n"
  "v_permlane32_swap_b32 v64, v68\n"
  "v_permlane32_swap_b32 v65, v69\n"
  "v_permlane32_swap_b32 v66, v70\n"
  "v_permlane32_swap_b32 v67, v71\n"
  "s_nop 0\n"
  "v_max_f32 v64, v64, v68\n"
  "v_max_f32 v65, v65, v69\n"
  "v_max_f32 v66, v66, v70\n"
  "v_max_f32 v67, v67, v71\n"
  "v_mov_b32 v68, v64\n"
  "v_mov_b32 v69, v65\n"
  "v_mov_b32 v70, v66\n"
  "v_mov_b32 v71, v67\n"
  "s_nop 0\n"
  "v_permlane16_swap_b32 v64, v68\n"
  "v_permlane16_swap_b32 v65, v69\n"
  "v_permlane16_swap_b32 v66, v70\n"
  "v_permlane16_swap_b32 v67, v71\n"
  "s_nop 0\n"
  "v_max_f32 v64, v64, v68\n"
  "v_max_f32 v65, v65, v69\n"
  "v_max_f32 v66, v66, v70\n"
  "v_max_f32 v67, v67, v71\n"
  "v_sub_f32 v192, 0, v64\n"
  "v_sub_f32 v193, 0, v64\n"
  "v_sub_f32 v194, 0, v64\n"
  "v_sub_f32 v195, 0, v64\n"
  "v_sub_f32 v196, 0, v65\n"
  "v_sub_f32 v197, 0, v65\n"
  "v_sub_f32 v198, 0, v65\n"
  "v_sub_f32 v199, 0, v65\n"
  "v_sub_f32 v200, 0, v66\n"
  "v_sub_f32 v201, 0, v66\n"
  "v_sub_f32 v202, 0, v66\n"
  "v_sub_f32 v203, 0, v66\n"
  "v_sub_f32 v204, 0, v67\n"
  "v_sub_f32 v205, 0, v67\n"
  "v_sub_f32 v206, 0, v67\n"
  "v_sub_f32 v207, 0, v67\n"
  "s_waitcnt lgkmcnt(0)\n"
  "s_nop 0\n"
  "v_mfma_f32_16x16x32_bf16 v[32:35], a[144:147], v[128:131], v[192:195]\n"
  "v_mfma_f32_16x16x32_bf16 v[32:35], a[148:151], v[132:135], v[32:35]\n"
  "v_mfma_f32_16x16x32_bf16 v[32:35], a[152:155], v[136:139], v[32:35]\n"
  "v_mfma_f32_16x16x32_bf16 v[32:35], a[156:159], v[140:143], v[32:35]\n"
  "v_mfma_f32_16x16x32_bf16 v[40:43], a[144:147], v[144:147], v[196:199]\n"
  "v_mfma_f32_16x16x32_bf16 v[40:43], a[148:151], v[148:151], v[40:43]\n"
  "v_mfma_f32_16x16x32_bf16 v[40:43], a[152:155], v[152:155], v[40:43]\n"
  "v_mfma_f32_16x16x32_bf16 v[40:43], a[156:159], v[156:159], v[40:43]\n"
  "v_mfma_f32_16x16x32_bf16 v[48:51], a[144:147], v[160:163], v[200:203]\n"
  "v_mfma_f32_16x16x32_bf16 v[48:51], a[148:151], v[164:167], v[48:51]\n"
  "v_mfma_f32_16x16x32_bf16 v[48:51], a[152:155], v[168:171], v[48:51]\n"
  "v_mfma_f32_16x16x32_bf16 v[48:51], a[156:159], v[172:175], v[48:51]\n"
  "v_mfma_f32_16x16x32_bf16 v[56:59], a[144:147], v[176:179], v[204:207]\n"
  "v_mfma_f32_16x16x32_bf16 v[56:59], a[148:151], v[180:183], v[56:59]\n"
  "v_mfma_f32_16x16x32_bf16 v[56:59], a[152:155], v[184:187], v[56:59]\n"
  "v_mfma_f32_16x16x32_bf16 v[56:59], a[156:159], v[188:191], v[56:59]\n"
  "v_mfma_f32_16x16x32_bf16 v[36:39], a[160:163], v[128:131], v[192:195]\n"
  "v_mfma_f32_16x16x32_bf16 v[36:39], a[164:167], v[132:135], v[36:39]\n"
  "v_mfma_f32_16x16x32_bf16 v[36:39], a[168:171], v[136:139], v[36:39]\n"
  "v_mfma_f32_16x16x32_bf16 v[36:39], a[172:175], v[140:143], v[36:39]\n"
  "v_mfma_f32_16x16x32_bf16 v[44:47], a[160:163], v[144:147], v[196:199]\n"
  "v_mfma_f32_16x16x32_bf16 v[44:47], a[164:167], v[148:151], v[44:47]\n"
  "v_mfma_f32_16x16x32_bf16 v[44:47], a[168:171], v[152:155], v[44:47]\n"
  "v_mfma_f32_16x16x32_bf16 v[44:47], a[172:175], v[156:159], v[44:47]\n"
  "v_mfma_f32_16x16x32_bf16 v[52:55], a[160:163], v[160:163], v[200:203]\n"
  "v_mfma_f32_16x16x32_bf16 v[52:55], a[164:167], v[164:167], v[52:55]\n"
  "v_mfma_f32_16x16x32_bf16 v[52:55], a[168:171], v[168:171], v[52:55]\n"
  "v_mfma_f32_16x16x32_bf16 v[52:55], a[172:175], v[172:175], v[52:55]\n"
  "v_mfma_f32_16x16x32_bf16 v[60:63], a[160:163], v[176:179], v[204:207]\n"
  "v_mfma_f32_16x16x32_bf16 v[60:63], a[164:167], v[180:183], v[60:63]\n"
  "v_mfma_f32_16x16x32_bf16 v[60:63], a[168:171], v[184:187], v[60:63]\n"
  "v_mfma_f32_16x16x32_bf16 v[60:63], a[172:175], v[188:191], v[60:63]\n"
  "v_sub_f32 v0, v0, v64\n"
  "v_sub_f32 v1, v1, v64\n"
  "v_sub_f32 v2, v2, v64\n"
  "v_sub_f32 v3, v3, v64\n"
  "v_sub_f32 v4, v4, v64\n"
  "v_sub_f32 v5, v5, v64\n"
  "v_sub_f32 v6, v6, v64\n"
  "v_sub_f32 v7, v7, v64\n"
  "v_sub_f32 v8, v8, v65\n"
  "v_sub_f32 v9, v9, v65\n"
  "v_sub_f32 v10, v10, v65\n"
  "v_sub_f32 v11, v11, v65\n"
  "v_sub_f32 v12, v12, v65\n"
  "v_sub_f32 v13, v13, v65\n"
  "v_sub_f32 v14, v14, v65\n"
  "v_sub_f32 v15, v15, v65\n"
  "v_sub_f32 v16, v16, v66\n"
  "v_sub_f32 v17, v17, v66\n"
  "v_sub_f32 v18, v18, v66\n"
  "v_sub_f32 v19, v19, v66\n"
  "v_sub_f32 v20, v20, v66\n"
  "v_sub_f32 v21, v21, v66\n"
  "v_sub_f32 v22, v22, v66\n"
  "v_sub_f32 v23, v23, v66\n"
  "v_sub_f32 v24, v24, v67\n"
  "v_sub_f32 v25, v25, v67\n"
  "v_sub_f32 v26, v26, v67\n"
  "v_sub_f32 v27, v27, v67\n"
  "v_sub_f32 v28, v28, v67\n"
  "v_sub_f32 v29, v29, v67\n"
  "v_sub_f32 v30, v30, v67\n"
  "v_sub_f32 v31, v31, v67\n"
  "s_waitcnt vmcnt(5) lgkmcnt(0)\n"
  "s_barrier\n"
  "s_add_u32 m0, s42, 81920\n"
  "s_nop 0\n"
  "buffer_load_dwordx4 v232, s[24:27], s41 offen offset:0 nt lds\n"
  "v_exp_f32 v0, v0\n"
  "s_nop 0\n"
  "buffer_load_dwordx4 v233, s[24:27], s41 offen offset:1024 nt lds\n"
  "v_exp_f32 v1, v1\n"
  "s_nop 0\n"
  "v_cvt_pk_bf16_f32 v0, v0, v1\n"
  "v_exp_f32 v2, v2\n"
  "s_nop 0\n"
  "ds_read_b128 a[144:147], v224 offset:16384\n"
  "v_exp_f32 v3, v3\n"
  "s_nop 0\n"
  "ds_read_b128 a[148:151], v225 offset:16384\n"
  "v_cvt_pk_bf16_f32 v1, v2, v3\n"
  "v_exp_f32 v4, v4\n"
  "s_nop 0\n"
  "ds_read_b128 a[152:155], v226 offset:16384\n"
  "v_exp_f32 v5, v5\n"
  "s_nop 0\n"
  "ds_read_b128 a[156:159], v227 offset:16384\n"
  "v_cvt_pk_bf16_f32 v2, v4, v5\n"
  "v_exp_f32 v6, v6\n"
  "s_nop 0\n"
  "ds_read_b128 a[160:163], v224 offset:20480\n"
  "v_exp_f32 v7, v7\n"
  "s_nop 0\n"
  "ds_read_b128 a[164:167], v225 offset:20480\n"
  "v_cvt_pk_bf16_f32 v3, v6, v7\n"
  "v_exp_f32 v8, v8\n"
  "s_nop 0\n"
  "ds_read_b128 a[168:171], v226 offset:20480\n"
  "v_exp_f32 v9, v9\n"
  "s_nop 0\n"
  "ds_read_b128 a[172:175], v227 offset:20480\n"
  "v_cvt_pk_bf16_f32 v8, v8, v9\n"
  "v_exp_f32 v10, v10\n"
  "s_nop 0\n"
  "buffer_load_dwordx4 v234, s[24:27], s41 offen offset:2048 nt lds\n"
  "v_exp_f32 v11, v11\n"
  "s_nop 0\n"
  "v_cvt_pk_bf16_f32 v9, v10, v11\n"
  "v_exp_f32 v12, v12\n"
  "s_nop 0\n"
  "buffer_load_dwordx4 v235, s[24:27], s41 offen offset:3072 nt lds\n"
  "s_add_u32 s41, s41, 16384\n"
  "v_exp_f32 v13, v13\n"
  "s_nop 0\n"
  "v_cvt_pk_bf16_f32 v10, v12, v13\n"
  "v_exp_f32 v14, v14\n"
  "v_exp_f32 v15, v15\n"
  "s_nop 0\n"
  "v_cvt_pk_bf16_f32 v11, v14, v15\n"
  "v_exp_f32 v16, v16\n"
  "v_exp_f32 v17, v17\n"
  "s_nop 0\n"
  "v_cvt_pk_bf16_f32 v16, v16, v17\n"
  "s_add_u32 m0, s42, 49152\n"
  "v_exp_f32 v18, v18\n"
  "s_nop 0\n"
  "buffer_load_dwordx4 v228, s[20:23], s40 offen offset:0 nt lds\n"
  "v_exp_f32 v19, v19\n"
  "s_nop 0\n"
  "v_cvt_pk_bf16_f32 v17, v18, v19\n"
  "v_exp_f32 v20, v20\n"
  "s_nop 0\n"
  "buffer_load_dwordx4 v229, s[20:23], s40 offen offset:1024 nt lds\n"
  "v_exp_f32 v21, v21\n"
  "s_nop 0\n"
  "v_cvt_pk_bf16_f32 v18, v20, v21\n"
  "v_exp_f32 v22, v22\n"
  "s_nop 0\n"
  "buffer_load_dwordx4 v230, s[20:23], s40 offen offset:2048 nt lds\n"
  "v_exp_f32 v23, v23\n"
  "s_nop 0\n"
  "v_cvt_pk_bf16_f32 v19, v22, v23\n"
  "v_exp_f32 v24, v24\n"
  "s_nop 0\n"
  "buffer_load_dwordx4 v231, s[20:23], s40 offen offset:3072 nt lds\n"
  "s_add_u32 s40, s40, 16384\n"
  "v_exp_f32 v25, v25\n"
  "s_nop 0\n"
  "v_cvt_pk_bf16_f32 v24, v24, v25\n"
  "s_add_u32 m0, s47, 0\n"
  "s_add_u32 s46, s52, 0\n"
  "v_exp_f32 v26, v26\n"
  "s_nop 0\n"
  "buffer_load_dwordx4 v244, s[76:79], s46 offen offset:0 lds\n"
  "v_exp_f32 v27, v27\n"
  "s_nop 0\n"
  "v_cvt_pk_bf16_f32 v25, v26, v27\n"
  "v_exp_f32 v28, v28\n"
  "s_nop 0\n"
  "buffer_load_dwordx4 v245, s[76:79], s46 offen offset:1024 lds\n"
  "v_exp_f32 v29, v29\n"
  "s_nop 0\n"
  "v_cvt_pk_bf16_f32 v26, v28, v29\n"
  "v_exp_f32 v30, v30\n"
  "s_nop 0\n"
  "buffer_load_dwordx4 v246, s[76:79], s46 offen offset:2048 lds\n"
  "v_exp_f32 v31, v31\n"
  "s_nop 0\n"
  "v_cvt_pk_bf16_f32 v27, v30, v31\n"
  "v_exp_f32 v32, v32\n"
  "s_nop 0\n"
  "buffer_load_dwordx4 v247, s[76:79], s46 offen offset:3072 lds\n"
  "v_exp_f32 v33, v33\n"
  "s_nop 0\n"
  "v_cvt_pk_bf16_f32 v32, v32, v33\n"
  "v_exp_f32 v34, v34\n"
  "s_waitcnt lgkmcnt(0)\n"
  "v_mfma_f32_16x16x32_bf16 v[64:67], a[144:147], v[128:131], v[192:195]\n"
  "v_exp_f32 v35, v35\n"
  "v_mfma_f32_16x16x32_bf16 v[64:67], a[148:151], v[132:135], v[64:67]\n"
  "ds_read_b64_tr_b16 a[176:177], v216 offset:0\n"
  "v_cvt_pk_bf16_f32 v33, v34, v35\n"
  "v_mfma_f32_16x16x32_bf16 v[64:67], a[152:155], v[136:139], v[64:67]\n"
  "v_exp_f32 v36, v36\n"
  "v_mfma_f32_16x16x32_bf16 v[64:67], a[156:159], v[140:143], v[64:67]\n"
  "ds_read_b64_tr_b16 a[178:179], v216 offset:4096\n"
  "ds_read_b64_tr_b16 a[208:209], v216 offset:8192\n"
  "v_mfma_f32_16x16x32_bf16 v[72:75], a[144:147], v[144:147], v[196:199]\n"
  "v_exp_f32 v37, v37\n"
  "v_mfma_f32_16x16x32_bf16 v[72:75], a[148:151], v[148:151], v[72:75]\n"
  "ds_read_b64_tr_b16 a[210:211], v216 offset:12288\n"
  "v_cvt_pk_bf16_f32 v34, v36, v37\n"
  "v_mfma_f32_16x16x32_bf16 v[72:75], a[152:155], v[152:155], v[72:75]\n"
  "v_exp_f32 v38, v38\n"
  "v_mfma_f32_16x16x32_bf16 v[72:75], a[156:159], v[156:159], v[72:75]\n"
  "ds_read_b64_tr_b16 a[180:181], v217 offset:0\n"
  "ds_read_b64_tr_b16 a[182:183], v217 offset:4096\n"
  "v_mfma_f32_16x16x32_bf16 v[80:83], a[144:147], v[160:163], v[200:203]\n"
  "v_exp_f32 v39, v39\n"
  "v_mfma_f32_16x16x32_bf16 v[80:83], a[148:151], v[164:167], v[80:83]\n"
  "ds_read_b64_tr_b16 a[212:213], v217 offset:8192\n"
  "v_cvt_pk_bf16_f32 v35, v38, v39\n"
  "v_mfma_f32_16x16x32_bf16 v[80:83], a[152:155], v[168:171], v[80:83]\n"
  "v_exp_f32 v40, v40\n"
  "v_mfma_f32_16x16x32_bf16 v[80:83], a[156:159], v[172:175], v[80:83]\n"
  "ds_read_b64_tr_b16 a[214:215], v217 offset:12288\n"
  "ds_read_b64_tr_b16 a[184:185], v218 offset:0\n"
  "v_mfma_f32_16x16x32_bf16 v[88:91], a[144:147], v[176:179], v[204:207]\n"
  "v_exp_f32 v41, v41\n"
  "v_mfma_f32_16x16x32_bf16 v[88:91], a[148:151], v[180:183], v[88:91]\n"
  "ds_read_b64_tr_b16 a[186:187], v218 offset:4096\n"
  "v_cvt_pk_bf16_f32 v40, v40, v41\n"
  "v_mfma_f32_16x16x32_bf16 v[88:91], a[152:155], v[184:187], v[88:91]\n"
  "v_exp_f32 v42, v42\n"
  "v_mfma_f32_16x16x32_bf16 v[88:91], a[156:159], v[188:191], v[88:91]\n"
  "ds_read_b64_tr_b16 a[216:217], v218 offset:8192\n"
  "ds_read_b64_tr_b16 a[218:219], v218 offset:12288\n"
  "v_mfma_f32_16x16x32_bf16 v[68:71], a[160:163], v[128:131], v[192:195]\n"
  "v_exp_f32 v43, v43\n"
  "v_mfma_f32_16x16x32_bf16 v[68:71], a[164:167], v[132:135], v[68:71]\n"
  "ds_read_b128 a[144:147], v224 offset:24576\n"
  "v_cvt_pk_bf16_f32 v41, v42, v43\n"
  "v_mfma_f32_16x16x32_bf16 v[68:71], a[168:171], v[136:139], v[68:71]\n"
  "v_exp_f32 v44, v44\n"
  "v_mfma_f32_16x16x32_bf16 v[68:71], a[172:175], v[140:143], v[68:71]\n"
  "ds_read_b128 a[148:151], v225 offset:24576\n"
  "ds_read_b128 a[152:155], v226 offset:24576\n"
  "v_mfma_f32_16x16x32_bf16 v[76:79], a[160:163], v[144:147], v[196:199]\n"
  "v_exp_f32 v45, v45\n"
  "v_mfma_f32_16x16x32_bf16 v[76:79], a[164:167], v[148:151], v[76:79]\n"
  "ds_read_b128 a[156:159], v227 offset:24576\n"
  "v_cvt_pk_bf16_f32 v42, v44, v45\n"
  "v_mfma_f32_16x16x32_bf16 v[76:79], a[168:171], v[152:155], v[76:79]\n"
  "v_exp_f32 v46, v46\n"
  "v_mfma_f32_16x16x32_bf16 v[76:79], a[172:175], v[156:159], v[76:79]\n"
  "ds_read_b64_tr_b16 a[188:189], v219 offset:0\n"
  "ds_read_b64_tr_b16 a[190:191], v219 offset:4096\n"
  "v_mfma_f32_16x16x32_bf16 v[84:87], a[160:163], v[160:163], v[200:203]\n"
  "v_exp_f32 v47, v47\n"
  "v_mfma_f32_16x16x32_bf16 v[84:87], a[164:167], v[164:167], v[84:87]\n"
  "ds_read_b64_tr_b16 a[220:221], v219 offset:8192\n"
  "v_cvt_pk_bf16_f32 v43, v46, v47\n"
  "v_mfma_f32_16x16x32_bf16 v[84:87], a[168:171], v[168:171], v[84:87]\n"
  "v_exp_f32 v48, v48\n"
  "v_mfma_f32_16x16x32_bf16 v[84:87], a[172:175], v[172:175], v[84:87]\n"
  "ds_read_b64_tr_b16 a[222:223], v219 offset:12288\n"
  "ds_read_b64_tr_b16 a[192:193], v220 offset:0\n"
  "v_mfma_f32_16x16x32_bf16 v[92:95], a[160:163], v[176:179], v[204:207]\n"
  "v_exp_f32 v49, v49\n"
  "v_mfma_f32_16x16x32_bf16 v[92:95], a[164:167], v[180:183], v[92:95]\n"
  "ds_read_b64_tr_b16 a[194:195], v220 offset:4096\n"
  "v_cvt_pk_bf16_f32 v48, v48, v49\n"
  "v_mfma_f32_16x16x32_bf16 v[92:95], a[168:171], v[184:187], v[92:95]\n"
  "v_exp_f32 v50, v50\n"
  "v_mfma_f32_16x16x32_bf16 v[92:95], a[172:175], v[188:191], v[92:95]\n"
  "s_waitcnt lgkmcnt(6)\n"
  "ds_read_b64_tr_b16 a[224:225], v220 offset:8192\n"
  "ds_read_b64_tr_b16 a[226:227], v220 offset:12288\n"
  "v_mfma_f32_16x16x32_bf16 v[96:99], a[144:147], v[128:131], v[192:195]\n"
  "v_exp_f32 v51, v51\n"
  "v_mfma_f32_16x16x32_bf16 v[96:99], a[148:151], v[132:135], v[96:99]\n"
  "ds_read_b128 a[160:163], v224 offset:28672\n"
  "v_cvt_pk_bf16_f32 v49, v50, v51\n"
  "v_mfma_f32_16x16x32_bf16 v[96:99], a[152:155], v[136:139], v[96:99]\n"
  "v_exp_f32 v52, v52\n"
  "v_mfma_f32_16x16x32_bf16 v[96:99], a[156:159], v[140:143], v[96:99]\n"
  "ds_read_b128 a[164:167], v225 offset:28672\n"
  "ds_read_b128 a[168:171], v226 offset:28672\n"
  "v_mfma_f32_16x16x32_bf16 v[104:107], a[144:147], v[144:147], v[196:199]\n"
  "v_exp_f32 v53, v53\n"
  "v_mfma_f32_16x16x32_bf16 v[104:107], a[148:151], v[148:151], v[104:107]\n"
  "ds_read_b128 a[172:175], v227 offset:28672\n"
  "v_cvt_pk_bf16_f32 v50, v52, v53\n"
  "v_mfma_f32_16x16x32_bf16 v[104:107], a[152:155], v[152:155], v[104:107]\n"
  "v_exp_f32 v54, v54\n"
  "v_mfma_f32_16x16x32_bf16 v[104:107], a[156:159], v[156:159], v[104:107]\n"
  "ds_read_b64_tr_b16 a[196:197], v221 offset:0\n"
  "ds_read_b64_tr_b16 a[198:199], v221 offset:4096\n"
  "v_mfma_f32_16x16x32_bf16 v[112:115], a[144:147], v[160:163], v[200:203]\n"
  "v_exp_f32 v55, v55\n"
  "v_mfma_f32_16x16x32_bf16 v[112:115], a[148:151], v[164:167], v[112:115]\n"
  "ds_read_b64_tr_b16 a[228:229], v221 offset:8192\n"
  "v_cvt_pk_bf16_f32 v51, v54, v55\n"
  "v_mfma_f32_16x16x32_bf16 v[112:115], a[152:155], v[168:171], v[112:115]\n"
  "v_exp_f32 v56, v56\n"
  "v_mfma_f32_16x16x32_bf16 v[112:115], a[156:159], v[172:175], v[112:115]\n"
  "ds_read_b64_tr_b16 a[230:231], v221 offset:12288\n"
  "ds_read_b64_tr_b16 a[200:201], v222 offset:0\n"
  "v_mfma_f32_16x16x32_bf16 v[120:123], a[144:147], v[176:179], v[204:207]\n"
  "v_exp_f32 v57, v57\n"
  "v_mfma_f32_16x16x32_bf16 v[120:123], a[148:151], v[180:183], v[120:123]\n"
  "ds_read_b64_tr_b16 a[202:203], v222 offset:4096\n"
  "v_cvt_pk_bf16_f32 v56, v56, v57\n"
  "v_mfma_f32_16x16x32_bf16 v[120:123], a[152:155], v[184:187], v[120:123]\n"
  "v_exp_f32 v58, v58\n"
  "v_mfma_f32_16x16x32_bf16 v[120:123], a[156:159], v[188:191], v[120:123]\n"
  "s_waitcnt lgkmcnt(6)\n"
  "ds_read_b64_tr_b16 a[232:233], v222 offset:8192\n"
  "ds_read_b64_tr_b16 a[234:235], v222 offset:12288\n"
  "v_mfma_f32_16x16x32_bf16 v[100:103], a[160:163], v[128:131], v[192:195]\n"
  "v_exp_f32 v59, v59\n"
  "v_mfma_f32_16x16x32_bf16 v[100:103], a[164:167], v[132:135], v[100:103]\n"
  "v_cvt_pk_bf16_f32 v57, v58, v59\n"
  "v_mfma_f32_16x16x32_bf16 v[100:103], a[168:171], v[136:139], v[100:103]\n"
  "v_exp_f32 v60, v60\n"
  "v_mfma_f32_16x16x32_bf16 v[100:103], a[172:175], v[140:143], v[100:103]\n"
  "ds_read_b64_tr_b16 a[204:205], v223 offset:0\n"
  "ds_read_b64_tr_b16 a[206:207], v223 offset:4096\n"
  "v_mfma_f32_16x16x32_bf16 v[108:111], a[160:163], v[144:147], v[196:199]\n"
  "v_exp_f32 v61, v61\n"
  "v_mfma_f32_16x16x32_bf16 v[108:111], a[164:167], v[148:151], v[108:111]\n"
  "v_cvt_pk_bf16_f32 v58, v60, v61\n"
  "v_mfma_f32_16x16x32_bf16 v[108:111], a[168:171], v[152:155], v[108:111]\n"
  "v_exp_f32 v62, v62\n"
  "v_mfma_f32_16x16x32_bf16 v[108:111], a[172:175], v[156:159], v[108:111]\n"
  "ds_read_b64_tr_b16 a[236:237], v223 offset:8192\n"
  "ds_read_b64_tr_b16 a[238:239], v223 offset:12288\n"
  "v_mfma_f32_16x16x32_bf16 v[116:119], a[160:163], v[160:163], v[200:203]\n"
  "v_exp_f32 v63, v63\n"
  "v_mfma_f32_16x16x32_bf16 v[116:119], a[164:167], v[164:167], v[116:119]\n"
  "v_cvt_pk_bf16_f32 v59, v62, v63\n"
  "v_mfma_f32_16x16x32_bf16 v[116:119], a[168:171], v[168:171], v[116:119]\n"
  "v_mfma_f32_16x16x32_bf16 v[116:119], a[172:175], v[172:175], v[116:119]\n"
  "v_mfma_f32_16x16x32_bf16 v[124:127], a[160:163], v[176:179], v[204:207]\n"
  "v_mfma_f32_16x16x32_bf16 v[124:127], a[164:167], v[180:183], v[124:127]\n"
  "v_mfma_f32_16x16x32_bf16 v[124:127], a[168:171], v[184:187], v[124:127]\n"
  "v_mfma_f32_16x16x32_bf16 v[124:127], a[172:175], v[188:191], v[124:127]\n"
  "s_waitcnt vmcnt(8) lgkmcnt(0)\n"
  "s_barrier\n"
  "v_mfma_f32_16x16x32_bf16 a[0:3], a[176:179], v[0:3], 0\n"
  "s_add_u32 m0, s42, 98304\n"
  "v_mfma_f32_16x16x32_bf16 a[0:3], a[208:211], v[32:35], a[0:3]\n"
  "buffer_load_dwordx4 v232, s[24:27], s41 offen offset:0 nt lds\n"
  "v_mfma_f32_16x16x32_bf16 a[16:19], a[180:183], v[0:3], 0\n"
  "v_exp_f32 v64, v64\n"
  "v_mfma_f32_16x16x32_bf16 a[16:19], a[212:215], v[32:35], a[16:19]\n"
  "buffer_load_dwordx4 v233, s[24:27], s41 offen offset:1024 nt lds\n"
  "v_mfma_f32_16x16x32_bf16 a[32:35], a[184:187], v[0:3], 0\n"
  "v_exp_f32 v65, v65\n"
  "v_mfma_f32_16x16x32_bf16 a[32:35], a[216:219], v[32:35], a[32:35]\n"
  "v_cvt_pk_bf16_f32 v64, v64, v65\n"
  "v_mfma_f32_16x16x32_bf16 a[48:51], a[188:191], v[0:3], 0\n"
  "v_exp_f32 v66, v66\n"
  "v_mfma_f32_16x16x32_bf16 a[48:51], a[220:223], v[32:35], a[48:51]\n"
  "ds_read_b128 a[144:147], v224 offset:32768\n"
  "v_mfma_f32_16x16x32_bf16 a[64:67], a[192:195], v[0:3], 0\n"
  "v_exp_f32 v67, v67\n"
  "v_mfma_f32_16x16x32_bf16 a[64:67], a[224:227], v[32:35], a[64:67]\n"
  "ds_read_b128 a[148:151], v225 offset:32768\n"
  "v_cvt_pk_bf16_f32 v65, v66, v67\n"
  "v_mfma_f32_16x16x32_bf16 a[80:83], a[196:199], v[0:3], 0\n"
  "v_exp_f32 v68, v68\n"
  "v_mfma_f32_16x16x32_bf16 a[80:83], a[228:231], v[32:35], a[80:83]\n"
  "ds_read_b128 a[152:155], v226 offset:32768\n"
  "v_mfma_f32_16x16x32_bf16 a[96:99], a[200:203], v[0:3], 0\n"
  "v_exp_f32 v69, v69\n"
  "v_mfma_f32_16x16x32_bf16 a[96:99], a[232:235], v[32:35], a[96:99]\n"
  "ds_read_b128 a[156:159], v227 offset:32768\n"
  "v_cvt_pk_bf16_f32 v66, v68, v69\n"
  "v_mfma_f32_16x16x32_bf16 a[112:115], a[204:207], v[0:3], 0\n"
  "v_exp_f32 v70, v70\n"
  "v_mfma_f32_16x16x32_bf16 a[112:115], a[236:239], v[32:35], a[112:115]\n"
  "ds_read_b128 a[160:163], v224 offset:36864\n"
  "v_mfma_f32_16x16x32_bf16 a[128:131], v[208:211], v[0:3], 0\n"
  "v_exp_f32 v71, v71\n"
  "v_mfma_f32_16x16x32_bf16 a[128:131], v[208:211], v[32:35], a[128:131]\n"
  "ds_read_b128 a[164:167], v225 offset:36864\n"
  "v_cvt_pk_bf16_f32 v67, v70, v71\n"
  "v_mfma_f32_16x16x32_bf16 a[4:7], a[176:179], v[8:11], 0\n"
  "v_exp_f32 v72, v72\n"
  "v_mfma_f32_16x16x32_bf16 a[4:7], a[208:211], v[40:43], a[4:7]\n"
  "ds_read_b128 a[168:171], v226 offset:36864\n"
  "v_mfma_f32_16x16x32_bf16 a[20:23], a[180:183], v[8:11], 0\n"
  "v_exp_f32 v73, v73\n"
  "v_mfma_f32_16x16x32_bf16 a[20:23], a[212:215], v[40:43], a[20:23]\n"
  "ds_read_b128 a[172:175], v227 offset:36864\n"
  "v_cvt_pk_bf16_f32 v72, v72, v73\n"
  "v_mfma_f32_16x16x32_bf16 a[36:39], a[184:187], v[8:11], 0\n"
  "v_exp_f32 v74, v74\n"
  "v_mfma_f32_16x16x32_bf16 a[36:39], a[216:219], v[40:43], a[36:39]\n"
  "buffer_load_dwordx4 v234, s[24:27], s41 offen offset:2048 nt lds\n"
  "v_mfma_f32_16x16x32_bf16 a[52:55], a[188:191], v[8:11], 0\n"
  "v_exp_f32 v75, v75\n"
  "v_mfma_f32_16x16x32_bf16 a[52:55], a[220:223], v[40:43], a[52:55]\n"
  "v_cvt_pk_bf16_f32 v73, v74, v75\n"
  "v_mfma_f32_16x16x32_bf16 a[68:71], a[192:195], v[8:11], 0\n"
  "v_exp_f32 v76, v76\n"
  "v_mfma_f32_16x16x32_bf16 a[68:71], a[224:227], v[40:43], a[68:71]\n"
  "buffer_load_dwordx4 v235, s[24:27], s41 offen offset:3072 nt lds\n"
  "v_mfma_f32_16x16x32_bf16 a[84:87], a[196:199], v[8:11], 0\n"
  "s_add_u32 s41, s41, 16384\n"
  "v_exp_f32 v77, v77\n"
  "v_mfma_f32_16x16x32_bf16 a[84:87], a[228:231], v[40:43], a[84:87]\n"
  "v_cvt_pk_bf16_f32 v74, v76, v77\n"
  "v_mfma_f32_16x16x32_bf16 a[100:103], a[200:203], v[8:11], 0\n"
  "v_exp_f32 v78, v78\n"
  "v_mfma_f32_16x16x32_bf16 a[100:103], a[232:235], v[40:43], a[100:103]\n"
  "v_mfma_f32_16x16x32_bf16 a[116:119], a[204:207], v[8:11], 0\n"
  "v_exp_f32 v79, v79\n"
  "v_mfma_f32_16x16x32_bf16 a[116:119], a[236:239], v[40:43], a[116:119]\n"
  "v_cvt_pk_bf16_f32 v75, v78, v79\n"
  "v_mfma_f32_16x16x32_bf16 a[132:135], v[208:211], v[8:11], 0\n"
  "v_exp_f32 v80, v80\n"
  "v_mfma_f32_16x16x32_bf16 a[132:135], v[208:211], v[40:43], a[132:135]\n"
  "v_mfma_f32_16x16x32_bf16 a[8:11], a[176:179], v[16:19], 0\n"
  "v_exp_f32 v81, v81\n"
  "v_mfma_f32_16x16x32_bf16 a[8:11], a[208:211], v[48:51], a[8:11]\n"
  "v_cvt_pk_bf16_f32 v80, v80, v81\n"
  "v_mfma_f32_16x16x32_bf16 a[24:27], a[180:183], v[16:19], 0\n"
  "s_add_u32 m0, s42, 0\n"
  "v_exp_f32 v82, v82\n"
  "v_mfma_f32_16x16x32_bf16 a[24:27], a[212:215], v[48:51], a[24:27]\n"
  "buffer_load_dwordx4 v228, s[20:23], s40 offen offset:0 nt lds\n"
  "v_mfma_f32_16x16x32_bf16 a[40:43], a[184:187], v[16:19], 0\n"
  "v_exp_f32 v83, v83\n"
  "v_mfma_f32_16x16x32_bf16 a[40:43], a[216:219], v[48:51], a[40:43]\n"
  "v_cvt_pk_bf16_f32 v81, v82, v83\n"
  "v_mfma_f32_16x16x32_bf16 a[56:59], a[188:191], v[16:19], 0\n"
  "v_exp_f32 v84, v84\n"
  "v_mfma_f32_16x16x32_bf16 a[56:59], a[220:223], v[48:51], a[56:59]\n"
  "buffer_load_dwordx4 v229, s[20:23], s40 offen offset:1024 nt lds\n"
  "v_mfma_f32_16x16x32_bf16 a[72:75], a[192:195], v[16:19], 0\n"
  "v_exp_f32 v85, v85\n"
  "v_mfma_f32_16x16x32_bf16 a[72:75], a[224:227], v[48:51], a[72:75]\n"
  "v_cvt_pk_bf16_f32 v82, v84, v85\n"
  "v_mfma_f32_16x16x32_bf16 a[88:91], a[196:199], v[16:19], 0\n"
  "v_exp_f32 v86, v86\n"
  "v_mfma_f32_16x16x32_bf16 a[88:91], a[228:231], v[48:51], a[88:91]\n"
  "buffer_load_dwordx4 v230, s[20:23], s40 offen offset:2048 nt lds\n"
  "v_mfma_f32_16x16x32_bf16 a[104:107], a[200:203], v[16:19], 0\n"
  "v_exp_f32 v87, v87\n"
  "v_mfma_f32_16x16x32_bf16 a[104:107], a[232:235], v[48:51], a[104:107]\n"
  "v_cvt_pk_bf16_f32 v83, v86, v87\n"
  "v_mfma_f32_16x16x32_bf16 a[120:123], a[204:207], v[16:19], 0\n"
  "v_exp_f32 v88, v88\n"
  "v_mfma_f32_16x16x32_bf16 a[120:123], a[236:239], v[48:51], a[120:123]\n"
  "buffer_load_dwordx4 v231, s[20:23], s40 offen offset:3072 nt lds\n"
  "v_mfma_f32_16x16x32_bf16 a[136:139], v[208:211], v[16:19], 0\n"
  "s_add_u32 s40, s40, 16384\n"
  "v_exp_f32 v89, v89\n"
  "v_mfma_f32_16x16x32_bf16 a[136:139], v[208:211], v[48:51], a[136:139]\n"
  "v_cvt_pk_bf16_f32 v88, v88, v89\n"
  "v_mfma_f32_16x16x32_bf16 a[12:15], a[176:179], v[24:27], 0\n"
  "s_add_u32 m0, s47, 4096\n"
  "s_add_u32 s46, s52, 32768\n"
  "v_exp_f32 v90, v90\n"
  "v_mfma_f32_16x16x32_bf16 a[12:15], a[208:211], v[56:59], a[12:15]\n"
  "buffer_load_dwordx4 v244, s[76:79], s46 offen offset:0 lds\n"
  "v_mfma_f32_16x16x32_bf16 a[28:31], a[180:183], v[24:27], 0\n"
  "v_exp_f32 v91, v91\n"
  "v_mfma_f32_16x16x32_bf16 a[28:31], a[212:215], v[56:59], a[28:31]\n"
  "v_cvt_pk_bf16_f32 v89, v90, v91\n"
  "v_mfma_f32_16x16x32_bf16 a[44:47], a[184:187], v[24:27], 0\n"
  "v_exp_f32 v92, v92\n"
  "v_mfma_f32_16x16x32_bf16 a[44:47], a[216:219], v[56:59], a[44:47]\n"
  "buffer_load_dwordx4 v245, s[76:79], s46 offen offset:1024 lds\n"
  "v_mfma_f32_16x16x32_bf16 a[60:63], a[188:191], v[24:27], 0\n"
  "v_exp_f32 v93, v93\n"
  "v_mfma_f32_16x16x32_bf16 a[60:63], a[220:223], v[56:59], a[60:63]\n"
  "v_cvt_pk_bf16_f32 v90, v92, v93\n"
  "v_mfma_f32_16x16x32_bf16 a[76:79], a[192:195], v[24:27], 0\n"
  "v_exp_f32 v94, v94\n"
  "v_mfma_f32_16x16x32_bf16 a[76:79], a[224:227], v[56:59], a[76:79]\n"
  "buffer_load_dwordx4 v246, s[76:79], s46 offen offset:2048 lds\n"
  "v_mfma_f32_16x16x32_bf16 a[92:95], a[196:199], v[24:27], 0\n"
  "v_exp_f32 v95, v95\n"
  "v_mfma_f32_16x16x32_bf16 a[92:95], a[228:231], v[56:59], a[92:95]\n"
  "v_cvt_pk_bf16_f32 v91, v94, v95\n"
  "v_mfma_f32_16x16x32_bf16 a[108:111], a[200:203], v[24:27], 0\n"
  "v_exp_f32 v96, v96\n"
  "v_mfma_f32_16x16x32_bf16 a[108:111], a[232:235], v[56:59], a[108:111]\n"
  "buffer_load_dwordx4 v247, s[76:79], s46 offen offset:3072 lds\n"
  "v_mfma_f32_16x16x32_bf16 a[124:127], a[204:207], v[24:27], 0\n"
  "v_exp_f32 v97, v97\n"
  "v_mfma_f32_16x16x32_bf16 a[124:127], a[236:239], v[56:59], a[124:127]\n"
  "v_cvt_pk_bf16_f32 v96, v96, v97\n"
  "v_mfma_f32_16x16x32_bf16 a[140:143], v[208:211], v[24:27], 0\n"
  "v_exp_f32 v98, v98\n"
  "v_mfma_f32_16x16x32_bf16 a[140:143], v[208:211], v[56:59], a[140:143]\n"
  "s_waitcnt lgkmcnt(0)\n"
  "v_mfma_f32_16x16x32_bf16 v[0:3], a[144:147], v[128:131], v[192:195]\n"
  "v_exp_f32 v99, v99\n"
  "v_mfma_f32_16x16x32_bf16 v[0:3], a[148:151], v[132:135], v[0:3]\n"
  "ds_read_b64_tr_b16 a[176:177], v216 offset:16384\n"
  "v_cvt_pk_bf16_f32 v97, v98, v99\n"
  "v_mfma_f32_16x16x32_bf16 v[0:3], a[152:155], v[136:139], v[0:3]\n"
  "v_exp_f32 v100, v100\n"
  "v_mfma_f32_16x16x32_bf16 v[0:3], a[156:159], v[140:143], v[0:3]\n"
  "ds_read_b64_tr_b16 a[178:179], v216 offset:20480\n"
  "ds_read_b64_tr_b16 a[208:209], v216 offset:24576\n"
  "v_mfma_f32_16x16x32_bf16 v[8:11], a[144:147], v[144:147], v[196:199]\n"
  "v_exp_f32 v101, v101\n"
  "v_mfma_f32_16x16x32_bf16 v[8:11], a[148:151], v[148:151], v[8:11]\n"
  "ds_read_b64_tr_b16 a[210:211], v216 offset:28672\n"
  "v_cvt_pk_bf16_f32 v98, v100, v101\n"
  "v_mfma_f32_16x16x32_bf16 v[8:11], a[152:155], v[152:155], v[8:11]\n"
  "v_exp_f32 v102, v102\n"
  "v_mfma_f32_16x16x32_bf16 v[8:11], a[156:159], v[156:159], v[8:11]\n"
  "ds_read_b64_tr_b16 a[180:181], v217 offset:16384\n"
  "ds_read_b64_tr_b16 a[182:183], v217 offset:20480\n"
  "v_mfma_f32_16x16x32_bf16 v[16:19], a[144:147], v[160:163], v[200:203]\n"
  "v_exp_f32 v103, v103\n"
  "v_mfma_f32_16x16x32_bf16 v[16:19], a[148:151], v[164:167], v[16:19]\n"
  "ds_read_b64_tr_b16 a[212:213], v217 offset:24576\n"
  "v_cvt_pk_bf16_f32 v99, v102, v103\n"
  "v_mfma_f32_16x16x32_bf16 v[16:19], a[152:155], v[168:171], v[16:19]\n"
  "v_exp_f32 v104, v104\n"
  "v_mfma_f32_16x16x32_bf16 v[16:19], a[156:159], v[172:175], v[16:19]\n"
  "ds_read_b64_tr_b16 a[214:215], v217 offset:28672\n"
  "ds_read_b64_tr_b16 a[184:185], v218 offset:16384\n"
  "v_mfma_f32_16x16x32_bf16 v[24:27], a[144:147], v[176:179], v[204:207]\n"
  "v_exp_f32 v105, v105\n"
  "v_mfma_f32_16x16x32_bf16 v[24:27], a[148:151], v[180:183], v[24:27]\n"
  "ds_read_b64_tr_b16 a[186:187], v218 offset:20480\n"
  "v_cvt_pk_bf16_f32 v104, v104, v105\n"
  "v_mfma_f32_16x16x32_bf16 v[24:27], a[152:155], v[184:187], v[24:27]\n"
  "v_exp_f32 v106, v106\n"
  "v_mfma_f32_16x16x32_bf16 v[24:27], a[156:159], v[188:191], v[24:27]\n"
  "ds_read_b64_tr_b16 a[216:217], v218 offset:24576\n"
  "ds_read_b64_tr_b16 a[218:219], v218 offset:28672\n"
  "v_mfma_f32_16x16x32_bf16 v[4:7], a[160:163], v[128:131], v[192:195]\n"
  "v_exp_f32 v107, v107\n"
  "v_mfma_f32_16x16x32_bf16 v[4:7], a[164:167], v[132:135], v[4:7]\n"
  "ds_read_b128 a[144:147], v224 offset:40960\n"
  "v_cvt_pk_bf16_f32 v105, v106, v107\n"
  "v_mfma_f32_16x16x32_bf16 v[4:7], a[168:171], v[136:139], v[4:7]\n"
  "v_exp_f32 v108, v108\n"
  "v_mfma_f32_16x16x32_bf16 v[4:7], a[172:175], v[140:143], v[4:7]\n"
  "ds_read_b128 a[148:151], v225 offset:40960\n"
  "ds_read_b128 a[152:155], v226 offset:40960\n"
  "v_mfma_f32_16x16x32_bf16 v[12:15], a[160:163], v[144:147], v[196:199]\n"
  "v_exp_f32 v109, v109\n"
  "v_mfma_f32_16x16x32_bf16 v[12:15], a[164:167], v[148:151], v[12:15]\n"
  "ds_read_b128 a[156:159], v227 offset:40960\n"
  "v_cvt_pk_bf16_f32 v106, v108, v109\n"
  "v_mfma_f32_16x16x32_bf16 v[12:15], a[168:171], v[152:155], v[12:15]\n"
  "v_exp_f32 v110, v110\n"
  "v_mfma_f32_16x16x32_bf16 v[12:15], a[172:175], v[156:159], v[12:15]\n"
  "ds_read_b64_tr_b16 a[188:189], v219 offset:16384\n"
  "ds_read_b64_tr_b16 a[190:191], v219 offset:20480\n"
  "v_mfma_f32_16x16x32_bf16 v[20:23], a[160:163], v[160:163], v[200:203]\n"
  "v_exp_f32 v111, v111\n"
  "v_mfma_f32_16x16x32_bf16 v[20:23], a[164:167], v[164:167], v[20:23]\n"
  "ds_read_b64_tr_b16 a[220:221], v219 offset:24576\n"
  "v_cvt_pk_bf16_f32 v107, v110, v111\n"
  "v_mfma_f32_16x16x32_bf16 v[20:23], a[168:171], v[168:171], v[20:23]\n"
  "v_exp_f32 v112, v112\n"
  "v_mfma_f32_16x16x32_bf16 v[20:23], a[172:175], v[172:175], v[20:23]\n"
  "ds_read_b64_tr_b16 a[222:223], v219 offset:28672\n"
  "ds_read_b64_tr_b16 a[192:193], v220 offset:16384\n"
  "v_mfma_f32_16x16x32_bf16 v[28:31], a[160:163], v[176:179], v[204:207]\n"
  "v_exp_f32 v113, v113\n"
  "v_mfma_f32_16x16x32_bf16 v[28:31], a[164:167], v[180:183], v[28:31]\n"
  "ds_read_b64_tr_b16 a[194:195], v220 offset:20480\n"
  "v_cvt_pk_bf16_f32 v112, v112, v113\n"
  "v_mfma_f32_16x16x32_bf16 v[28:31], a[168:171], v[184:187], v[28:31]\n"
  "v_exp_f32 v114, v114\n"
  "v_mfma_f32_16x16x32_bf16 v[28:31], a[172:175], v[188:191], v[28:31]\n"
  "s_waitcnt lgkmcnt(6)\n"
  "ds_read_b64_tr_b16 a[224:225], v220 offset:24576\n"
  "ds_read_b64_tr_b16 a[226:227], v220 offset:28672\n"
  "v_mfma_f32_16x16x32_bf16 v[32:35], a[144:147], v[128:131], v[192:195]\n"
  "v_exp_f32 v115, v115\n"
  "v_mfma_f32_16x16x32_bf16 v[32:35], a[148:151], v[132:135], v[32:35]\n"
  "ds_read_b128 a[160:163], v224 offset:45056\n"
  "v_cvt_pk_bf16_f32 v113, v114, v115\n"
  "v_mfma_f32_16x16x32_bf16 v[32:35], a[152:155], v[136:139], v[32:35]\n"
  "v_exp_f32 v116, v116\n"
  "v_mfma_f32_16x16x32_bf16 v[32:35], a[156:159], v[140:143], v[32:35]\n"
  "ds_read_b128 a[164:167], v225 offset:45056\n"
  "ds_read_b128 a[168:171], v226 offset:45056\n"
  "v_mfma_f32_16x16x32_bf16 v[40:43], a[144:147], v[144:147], v[196:199]\n"
  "v_exp_f32 v117, v117\n"
  "v_mfma_f32_16x16x32_bf16 v[40:43], a[148:151], v[148:151], v[40:43]\n"
  "ds_read_b128 a[172:175], v227 offset:45056\n"
  "v_cvt_pk_bf16_f32 v114, v116, v117\n"
  "v_mfma_f32_16x16x32_bf16 v[40:43], a[152:155], v[152:155], v[40:43]\n"
  "v_exp_f32 v118, v118\n"
  "v_mfma_f32_16x16x32_bf16 v[40:43], a[156:159], v[156:159], v[40:43]\n"
  "ds_read_b64_tr_b16 a[196:197], v221 offset:16384\n"
  "ds_read_b64_tr_b16 a[198:199], v221 offset:20480\n"
  "v_mfma_f32_16x16x32_bf16 v[48:51], a[144:147], v[160:163], v[200:203]\n"
  "v_exp_f32 v119, v119\n"
  "v_mfma_f32_16x16x32_bf16 v[48:51], a[148:151], v[164:167], v[48:51]\n"
  "ds_read_b64_tr_b16 a[228:229], v221 offset:24576\n"
  "v_cvt_pk_bf16_f32 v115, v118, v119\n"
  "v_mfma_f32_16x16x32_bf16 v[48:51], a[152:155], v[168:171], v[48:51]\n"
  "v_exp_f32 v120, v120\n"
  "v_mfma_f32_16x16x32_bf16 v[48:51], a[156:159], v[172:175], v[48:51]\n"
  "ds_read_b64_tr_b16 a[230:231], v221 offset:28672\n"
  "ds_read_b64_tr_b16 a[200:201], v222 offset:16384\n"
  "v_mfma_f32_16x16x32_bf16 v[56:59], a[144:147], v[176:179], v[204:207]\n"
  "v_exp_f32 v121, v121\n"
  "v_mfma_f32_16x16x32_bf16 v[56:59], a[148:151], v[180:183], v[56:59]\n"
  "ds_read_b64_tr_b16 a[202:203], v222 offset:20480\n"
  "v_cvt_pk_bf16_f32 v120, v120, v121\n"
  "v_mfma_f32_16x16x32_bf16 v[56:59], a[152:155], v[184:187], v[56:59]\n"
  "v_exp_f32 v122, v122\n"
  "v_mfma_f32_16x16x32_bf16 v[56:59], a[156:159], v[188:191], v[56:59]\n"
  "s_waitcnt lgkmcnt(6)\n"
  "ds_read_b64_tr_b16 a[232:233], v222 offset:24576\n"
  "ds_read_b64_tr_b16 a[234:235], v222 offset:28672\n"
  "v_mfma_f32_16x16x32_bf16 v[36:39], a[160:163], v[128:131], v[192:195]\n"
  "v_exp_f32 v123, v123\n"
  "v_mfma_f32_16x16x32_bf16 v[36:39], a[164:167], v[132:135], v[36:39]\n"
  "v_cvt_pk_bf16_f32 v121, v122, v123\n"
  "v_mfma_f32_16x16x32_bf16 v[36:39], a[168:171], v[136:139], v[36:39]\n"
  "v_exp_f32 v124, v124\n"
  "v_mfma_f32_16x16x32_bf16 v[36:39], a[172:175], v[140:143], v[36:39]\n"
  "ds_read_b64_tr_b16 a[204:205], v223 offset:16384\n"
  "ds_read_b64_tr_b16 a[206:207], v223 offset:20480\n"
  "v_mfma_f32_16x16x32_bf16 v[44:47], a[160:163], v[144:147], v[196:199]\n"
  "v_exp_f32 v125, v125\n"
  "v_mfma_f32_16x16x32_bf16 v[44:47], a[164:167], v[148:151], v[44:47]\n"
  "v_cvt_pk_bf16_f32 v122, v124, v125\n"
  "v_mfma_f32_16x16x32_bf16 v[44:47], a[168:171], v[152:155], v[44:47]\n"
  "v_exp_f32 v126, v126\n"
  "v_mfma_f32_16x16x32_bf16 v[44:47], a[172:175], v[156:159], v[44:47]\n"
  "ds_read_b64_tr_b16 a[236:237], v223 offset:24576\n"
  "ds_read_b64_tr_b16 a[238:239], v223 offset:28672\n"
  "v_mfma_f32_16x16x32_bf16 v[52:55], a[160:163], v[160:163], v[200:203]\n"
  "v_exp_f32 v127, v127\n"
  "v_mfma_f32_16x16x32_bf16 v[52:55], a[164:167], v[164:167], v[52:55]\n"
  "v_cvt_pk_bf16_f32 v123, v126, v127\n"
  "v_mfma_f32_16x16x32_bf16 v[52:55], a[168:171], v[168:171], v[52:55]\n"
  "v_mfma_f32_16x16x32_bf16 v[52:55], a[172:175], v[172:175], v[52:55]\n"
  "v_mfma_f32_16x16x32_bf16 v[60:63], a[160:163], v[176:179], v[204:207]\n"
  "v_mfma_f32_16x16x32_bf16 v[60:63], a[164:167], v[180:183], v[60:63]\n"
  "v_mfma_f32_16x16x32_bf16 v[60:63], a[168:171], v[184:187], v[60:63]\n"
  "v_mfma_f32_16x16x32_bf16 v[60:63], a[172:175], v[188:191], v[60:63]\n"
  "s_waitcnt vmcnt(8) lgkmcnt(0)\n"
  "s_barrier\n"
  "s_mov_b32 s44, 6\n"
  ".p2align 6\n"
  "L_loop%=:\n"
  "v_mfma_f32_16x16x32_bf16 a[0:3], a[176:179], v[64:67], a[0:3]\n"
  "s_add_u32 m0, s42, 114688\n"
  "v_mfma_f32_16x16x32_bf16 a[0:3], a[208:211], v[96:99], a[0:3]\n"
  "buffer_load_dwordx4 v232, s[24:27], s41 offen offset:0 nt lds\n"
  "v_mfma_f32_16x16x32_bf16 a[16:19], a[180:183], v[64:67], a[16:19]\n"
  "v_exp_f32 v0, v0\n"
  "v_mfma_f32_16x16x32_bf16 a[16:19], a[212:215], v[96:99], a[16:19]\n"
  "buffer_load_dwordx4 v233, s[24:27], s41 offen offset:1024 nt lds\n"
  "v_mfma_f32_16x16x32_bf16 a[32:35], a[184:187], v[64:67], a[32:35]\n"
  "v_exp_f32 v1, v1\n"
  "v_mfma_f32_16x16x32_bf16 a[32:35], a[216:219], v[96:99], a[32:35]\n"
  "v_cvt_pk_bf16_f32 v0, v0, v1\n"
  "v_mfma_f32_16x16x32_bf16 a[48:51], a[188:191], v[64:67], a[48:51]\n"
  "v_exp_f32 v2, v2\n"
  "v_mfma_f32_16x16x32_bf16 a[48:51], a[220:223], v[96:99], a[48:51]\n"
  "ds_read_b128 a[144:147], v224 offset:49152\n"
  "v_mfma_f32_16x16x32_bf16 a[64:67], a[192:195], v[64:67], a[64:67]\n"
  "v_exp_f32 v3, v3\n"
  "v_mfma_f32_16x16x32_bf16 a[64:67], a[224:227], v[96:99], a[64:67]\n"
  "ds_read_b128 a[148:151], v225 offset:49152\n"
  "v_cvt_pk_bf16_f32 v1, v2, v3\n"
  "v_mfma_f32_16x16x32_bf16 a[80:83], a[196:199], v[64:67], a[80:83]\n"
  "v_exp_f32 v4, v4\n"
  "v_mfma_f32_16x16x32_bf16 a[80:83], a[228:231], v[96:99], a[80:83]\n"
  "ds_read_b128 a[152:155], v226 offset:49152\n"
  "v_mfma_f32_16x16x32_bf16 a[96:99], a[200:203], v[64:67], a[96:99]\n"
  "v_exp_f32 v5, v5\n"
  "v_mfma_f32_16x16x32_bf16 a[96:99], a[232:235], v[96:99], a[96:99]\n"
  "ds_read_b128 a[156:159], v227 offset:49152\n"
  "v_cvt_pk_bf16_f32 v2, v4, v5\n"
  "v_mfma_f32_16x16x32_bf16 a[112:115], a[204:207], v[64:67], a[112:115]\n"
  "v_exp_f32 v6, v6\n"
  "v_mfma_f32_16x16x32_bf16 a[112:115], a[236:239], v[96:99], a[112:115]\n"
  "ds_read_b128 a[160:163], v224 offset:53248\n"
  "v_mfma_f32_16x16x32_bf16 a[128:131], v[208:211], v[64:67], a[128:131]\n"
  "v_exp_f32 v7, v7\n"
  "v_mfma_f32_16x16x32_bf16 a[128:131], v[208:211], v[96:99], a[128:131]\n"
  "ds_read_b128 a[164:167], v225 offset:53248\n"
  "v_cvt_pk_bf16_f32 v3, v6, v7\n"
  "v_mfma_f32_16x16x32_bf16 a[4:7], a[176:179], v[72:75], a[4:7]\n"
  "v_exp_f32 v8, v8\n"
  "v_mfma_f32_16x16x32_bf16 a[4:7], a[208:211], v[104:107], a[4:7]\n"
  "ds_read_b128 a[168:171], v226 offset:53248\n"
  "v_mfma_f32_16x16x32_bf16 a[20:23], a[180:183], v[72:75], a[20:23]\n"
  "v_exp_f32 v9, v9\n"
  "v_mfma_f32_16x16x32_bf16 a[20:23], a[212:215], v[104:107], a[20:23]\n"
  "ds_read_b128 a[172:175], v227 offset:53248\n"
  "v_cvt_pk_bf16_f32 v8, v8, v9\n"
  "v_mfma_f32_16x16x32_bf16 a[36:39], a[184:187], v[72:75], a[36:39]\n"
  "v_exp_f32 v10, v10\n"
  "v_mfma_f32_16x16x32_bf16 a[36:39], a[216:219], v[104:107], a[36:39]\n"
  "buffer_load_dwordx4 v234, s[24:27], s41 offen offset:2048 nt lds\n"
  "v_mfma_f32_16x16x32_bf16 a[52:55], a[188:191], v[72:75], a[52:55]\n"
  "v_exp_f32 v11, v11\n"
  "v_mfma_f32_16x16x32_bf16 a[52:55], a[220:223], v[104:107], a[52:55]\n"
  "v_cvt_pk_bf16_f32 v9, v10, v11\n"
  "v_mfma_f32_16x16x32_bf16 a[68:71], a[192:195], v[72:75], a[68:71]\n"
  "v_exp_f32 v12, v12\n"
  "v_mfma_f32_16x16x32_bf16 a[68:71], a[224:227], v[104:107], a[68:71]\n"
  "buffer_load_dwordx4 v235, s[24:27], s41 offen offset:3072 nt lds\n"
  "v_mfma_f32_16x16x32_bf16 a[84:87], a[196:199], v[72:75], a[84:87]\n"
  "s_add_u32 s41, s41, 16384\n"
  "v_exp_f32 v13, v13\n"
  "v_mfma_f32_16x16x32_bf16 a[84:87], a[228:231], v[104:107], a[84:87]\n"
  "v_cvt_pk_bf16_f32 v10, v12, v13\n"
  "v_mfma_f32_16x16x32_bf16 a[100:103], a[200:203], v[72:75], a[100:103]\n"
  "v_exp_f32 v14, v14\n"
  "v_mfma_f32_16x16x32_bf16 a[100:103], a[232:235], v[104:107], a[100:103]\n"
  "v_mfma_f32_16x16x32_bf16 a[116:119], a[204:207], v[72:75], a[116:119]\n"
  "v_exp_f32 v15, v15\n"
  "v_mfma_f32_16x16x32_bf16 a[116:119], a[236:239], v[104:107], a[116:119]\n"
  "v_cvt_pk_bf16_f32 v11, v14, v15\n"
  "v_mfma_f32_16x16x32_bf16 a[132:135], v[208:211], v[72:75], a[132:135]\n"
  "v_exp_f32 v16, v16\n"
  "v_mfma_f32_16x16x32_bf16 a[132:135], v[208:211], v[104:107], a[132:135]\n"
  "v_mfma_f32_16x16x32_bf16 a[8:11], a[176:179], v[80:83], a[8:11]\n"
  "v_exp_f32 v17, v17\n"
  "v_mfma_f32_16x16x32_bf16 a[8:11], a[208:211], v[112:115], a[8:11]\n"
  "v_cvt_pk_bf16_f32 v16, v16, v17\n"
  "v_mfma_f32_16x16x32_bf16 a[24:27], a[180:183], v[80:83], a[24:27]\n"
  "s_add_u32 m0, s42, 16384\n"
  "v_exp_f32 v18, v18\n"
  "v_mfma_f32_16x16x32_bf16 a[24:27], a[212:215], v[112:115], a[24:27]\n"
  "buffer_load_dwordx4 v228, s[20:23], s40 offen offset:0 nt lds\n"
  "v_mfma_f32_16x16x32_bf16 a[40:43], a[184:187], v[80:83], a[40:43]\n"
  "v_exp_f32 v19, v19\n"
  "v_mfma_f32_16x16x32_bf16 a[40:43], a[216:219], v[112:115], a[40:43]\n"
  "v_cvt_pk_bf16_f32 v17, v18, v19\n"
  "v_mfma_f32_16x16x32_bf16 a[56:59], a[188:191], v[80:83], a[56:59]\n"
  "v_exp_f32 v20, v20\n"
  "v_mfma_f32_16x16x32_bf16 a[56:59], a[220:223], v[112:115], a[56:59]\n"
  "buffer_load_dwordx4 v229, s[20:23], s40 offen offset:1024 nt lds\n"
  "v_mfma_f32_16x16x32_bf16 a[72:75], a[192:195], v[80:83], a[72:75]\n"
  "v_exp_f32 v21, v21\n"
  "v_mfma_f32_16x16x32_bf16 a[72:75], a[224:227], v[112:115], a[72:75]\n"
  "v_cvt_pk_bf16_f32 v18, v20, v21\n"
  "v_mfma_f32_16x16x32_bf16 a[88:91], a[196:199], v[80:83], a[88:91]\n"
  "v_exp_f32 v22, v22\n"
  "v_mfma_f32_16x16x32_bf16 a[88:91], a[228:231], v[112:115], a[88:91]\n"
  "buffer_load_dwordx4 v230, s[20:23], s40 offen offset:2048 nt lds\n"
  "v_mfma_f32_16x16x32_bf16 a[104:107], a[200:203], v[80:83], a[104:107]\n"
  "v_exp_f32 v23, v23\n"
  "v_mfma_f32_16x16x32_bf16 a[104:107], a[232:235], v[112:115], a[104:107]\n"
  "v_cvt_pk_bf16_f32 v19, v22, v23\n"
  "v_mfma_f32_16x16x32_bf16 a[120:123], a[204:207], v[80:83], a[120:123]\n"
  "v_exp_f32 v24, v24\n"
  "v_mfma_f32_16x16x32_bf16 a[120:123], a[236:239], v[112:115], a[120:123]\n"
  "buffer_load_dwordx4 v231, s[20:23], s40 offen offset:3072 nt lds\n"
  "v_mfma_f32_16x16x32_bf16 a[136:139], v[208:211], v[80:83], a[136:139]\n"
  "s_add_u32 s40, s40, 16384\n"
  "v_exp_f32 v25, v25\n"
  "v_mfma_f32_16x16x32_bf16 a[136:139], v[208:211], v[112:115], a[136:139]\n"
  "v_cvt_pk_bf16_f32 v24, v24, v25\n"
  "v_mfma_f32_16x16x32_bf16 a[12:15], a[176:179], v[88:91], a[12:15]\n"
  "v_exp_f32 v26, v26\n"
  "v_mfma_f32_16x16x32_bf16 a[12:15], a[208:211], v[120:123], a[12:15]\n"
  "v_mfma_f32_16x16x32_bf16 a[28:31], a[180:183], v[88:91], a[28:31]\n"
  "v_exp_f32 v27, v27\n"
  "v_mfma_f32_16x16x32_bf16 a[28:31], a[212:215], v[120:123], a[28:31]\n"
  "v_cvt_pk_bf16_f32 v25, v26, v27\n"
  "v_mfma_f32_16x16x32_bf16 a[44:47], a[184:187], v[88:91], a[44:47]\n"
  "v_exp_f32 v28, v28\n"
  "v_mfma_f32_16x16x32_bf16 a[44:47], a[216:219], v[120:123], a[44:47]\n"
  "v_mfma_f32_16x16x32_bf16 a[60:63], a[188:191], v[88:91], a[60:63]\n"
  "v_exp_f32 v29, v29\n"
  "v_mfma_f32_16x16x32_bf16 a[60:63], a[220:223], v[120:123], a[60:63]\n"
  "v_cvt_pk_bf16_f32 v26, v28, v29\n"
  "v_mfma_f32_16x16x32_bf16 a[76:79], a[192:195], v[88:91], a[76:79]\n"
  "v_exp_f32 v30, v30\n"
  "v_mfma_f32_16x16x32_bf16 a[76:79], a[224:227], v[120:123], a[76:79]\n"
  "v_mfma_f32_16x16x32_bf16 a[92:95], a[196:199], v[88:91], a[92:95]\n"
  "v_exp_f32 v31, v31\n"
  "v_mfma_f32_16x16x32_bf16 a[92:95], a[228:231], v[120:123], a[92:95]\n"
  "v_cvt_pk_bf16_f32 v27, v30, v31\n"
  "v_mfma_f32_16x16x32_bf16 a[108:111], a[200:203], v[88:91], a[108:111]\n"
  "v_exp_f32 v32, v32\n"
  "v_mfma_f32_16x16x32_bf16 a[108:111], a[232:235], v[120:123], a[108:111]\n"
  "v_mfma_f32_16x16x32_bf16 a[124:127], a[204:207], v[88:91], a[124:127]\n"
  "v_exp_f32 v33, v33\n"
  "v_mfma_f32_16x16x32_bf16 a[124:127], a[236:239], v[120:123], a[124:127]\n"
  "v_cvt_pk_bf16_f32 v32, v32, v33\n"
  "v_mfma_f32_16x16x32_bf16 a[140:143], v[208:211], v[88:91], a[140:143]\n"
  "v_exp_f32 v34, v34\n"
  "v_mfma_f32_16x16x32_bf16 a[140:143], v[208:211], v[120:123], a[140:143]\n"
  "s_waitcnt lgkmcnt(0)\n"
  "v_mfma_f32_16x16x32_bf16 v[64:67], a[144:147], v[128:131], v[192:195]\n"
  "v_exp_f32 v35, v35\n"
  "v_mfma_f32_16x16x32_bf16 v[64:67], a[148:151], v[132:135], v[64:67]\n"
  "ds_read_b64_tr_b16 a[176:177], v216 offset:32768\n"
  "v_cvt_pk_bf16_f32 v33, v34, v35\n"
  "v_mfma_f32_16x16x32_bf16 v[64:67], a[152:155], v[136:139], v[64:67]\n"
  "v_exp_f32 v36, v36\n"
  "v_mfma_f32_16x16x32_bf16 v[64:67], a[156:159], v[140:143], v[64:67]\n"
  "ds_read_b64_tr_b16 a[178:179], v216 offset:36864\n"
  "ds_read_b64_tr_b16 a[208:209], v216 offset:40960\n"
  "v_mfma_f32_16x16x32_bf16 v[72:75], a[144:147], v[144:147], v[196:199]\n"
  "v_exp_f32 v37, v37\n"
  "v_mfma_f32_16x16x32_bf16 v[72:75], a[148:151], v[148:151], v[72:75]\n"
  "ds_read_b64_tr_b16 a[210:211], v216 offset:45056\n"
  "v_cvt_pk_bf16_f32 v34, v36, v37\n"
  "v_mfma_f32_16x16x32_bf16 v[72:75], a[152:155], v[152:155], v[72:75]\n"
  "v_exp_f32 v38, v38\n"
  "v_mfma_f32_16x16x32_bf16 v[72:75], a[156:159], v[156:159], v[72:75]\n"
  "ds_read_b64_tr_b16 a[180:181], v217 offset:32768\n"
  "ds_read_b64_tr_b16 a[182:183], v217 offset:36864\n"
  "v_mfma_f32_16x16x32_bf16 v[80:83], a[144:147], v[160:163], v[200:203]\n"
  "v_exp_f32 v39, v39\n"
  "v_mfma_f32_16x16x32_bf16 v[80:83], a[148:151], v[164:167], v[80:83]\n"
  "ds_read_b64_tr_b16 a[212:213], v217 offset:40960\n"
  "v_cvt_pk_bf16_f32 v35, v38, v39\n"
  "v_mfma_f32_16x16x32_bf16 v[80:83], a[152:155], v[168:171], v[80:83]\n"
  "v_exp_f32 v40, v40\n"
  "v_mfma_f32_16x16x32_bf16 v[80:83], a[156:159], v[172:175], v[80:83]\n"
  "ds_read_b64_tr_b16 a[214:215], v217 offset:45056\n"
  "ds_read_b64_tr_b16 a[184:185], v218 offset:32768\n"
  "v_mfma_f32_16x16x32_bf16 v[88:91], a[144:147], v[176:179], v[204:207]\n"
  "v_exp_f32 v41, v41\n"
  "v_mfma_f32_16x16x32_bf16 v[88:91], a[148:151], v[180:183], v[88:91]\n"
  "ds_read_b64_tr_b16 a[186:187], v218 offset:36864\n"
  "v_cvt_pk_bf16_f32 v40, v40, v41\n"
  "v_mfma_f32_16x16x32_bf16 v[88:91], a[152:155], v[184:187], v[88:91]\n"
  "v_exp_f32 v42, v42\n"
  "v_mfma_f32_16x16x32_bf16 v[88:91], a[156:159], v[188:191], v[88:91]\n"
  "ds_read_b64_tr_b16 a[216:217], v218 offset:40960\n"
  "ds_read_b64_tr_b16 a[218:219], v218 offset:45056\n"
  "v_mfma_f32_16x16x32_bf16 v[68:71], a[160:163], v[128:131], v[192:195]\n"
  "v_exp_f32 v43, v43\n"
  "v_mfma_f32_16x16x32_bf16 v[68:71], a[164:167], v[132:135], v[68:71]\n"
  "ds_read_b128 a[144:147], v224 offset:57344\n"
  "v_cvt_pk_bf16_f32 v41, v42, v43\n"
  "v_mfma_f32_16x16x32_bf16 v[68:71], a[168:171], v[136:139], v[68:71]\n"
  "v_exp_f32 v44, v44\n"
  "v_mfma_f32_16x16x32_bf16 v[68:71], a[172:175], v[140:143], v[68:71]\n"
  "ds_read_b128 a[148:151], v225 offset:57344\n"
  "ds_read_b128 a[152:155], v226 offset:57344\n"
  "v_mfma_f32_16x16x32_bf16 v[76:79], a[160:163], v[144:147], v[196:199]\n"
  "v_exp_f32 v45, v45\n"
  "v_mfma_f32_16x16x32_bf16 v[76:79], a[164:167], v[148:151], v[76:79]\n"
  "ds_read_b128 a[156:159], v227 offset:57344\n"
  "v_cvt_pk_bf16_f32 v42, v44, v45\n"
  "v_mfma_f32_16x16x32_bf16 v[76:79], a[168:171], v[152:155], v[76:79]\n"
  "v_exp_f32 v46, v46\n"
  "v_mfma_f32_16x16x32_bf16 v[76:79], a[172:175], v[156:159], v[76:79]\n"
  "ds_read_b64_tr_b16 a[188:189], v219 offset:32768\n"
  "ds_read_b64_tr_b16 a[190:191], v219 offset:36864\n"
  "v_mfma_f32_16x16x32_bf16 v[84:87], a[160:163], v[160:163], v[200:203]\n"
  "v_exp_f32 v47, v47\n"
  "v_mfma_f32_16x16x32_bf16 v[84:87], a[164:167], v[164:167], v[84:87]\n"
  "ds_read_b64_tr_b16 a[220:221], v219 offset:40960\n"
  "v_cvt_pk_bf16_f32 v43, v46, v47\n"
  "v_mfma_f32_16x16x32_bf16 v[84:87], a[168:171], v[168:171], v[84:87]\n"
  "v_exp_f32 v48, v48\n"
  "v_mfma_f32_16x16x32_bf16 v[84:87], a[172:175], v[172:175], v[84:87]\n"
  "ds_read_b64_tr_b16 a[222:223], v219 offset:45056\n"
  "ds_read_b64_tr_b16 a[192:193], v220 offset:32768\n"
  "v_mfma_f32_16x16x32_bf16 v[92:95], a[160:163], v[176:179], v[204:207]\n"
  "v_exp_f32 v49, v49\n"
  "v_mfma_f32_16x16x32_bf16 v[92:95], a[164:167], v[180:183], v[92:95]\n"
  "ds_read_b64_tr_b16 a[194:195], v220 offset:36864\n"
  "v_cvt_pk_bf16_f32 v48, v48, v49\n"
  "v_mfma_f32_16x16x32_bf16 v[92:95], a[168:171], v[184:187], v[92:95]\n"
  "v_exp_f32 v50, v50\n"
  "v_mfma_f32_16x16x32_bf16 v[92:95], a[172:175], v[188:191], v[92:95]\n"
  "s_waitcnt lgkmcnt(6)\n"
  "ds_read_b64_tr_b16 a[224:225], v220 offset:40960\n"
  "ds_read_b64_tr_b16 a[226:227], v220 offset:45056\n"
  "v_mfma_f32_16x16x32_bf16 v[96:99], a[144:147], v[128:131], v[192:195]\n"
  "v_exp_f32 v51, v51\n"
  "v_mfma_f32_16x16x32_bf16 v[96:99], a[148:151], v[132:135], v[96:99]\n"
  "ds_read_b128 a[160:163], v224 offset:61440\n"
  "v_cvt_pk_bf16_f32 v49, v50, v51\n"
  "v_mfma_f32_16x16x32_bf16 v[96:99], a[152:155], v[136:139], v[96:99]\n"
  "v_exp_f32 v52, v52\n"
  "v_mfma_f32_16x16x32_bf16 v[96:99], a[156:159], v[140:143], v[96:99]\n"
  "ds_read_b128 a[164:167], v225 offset:61440\n"
  "ds_read_b128 a[168:171], v226 offset:61440\n"
  "v_mfma_f32_16x16x32_bf16 v[104:107], a[144:147], v[144:147], v[196:199]\n"
  "v_exp_f32 v53, v53\n"
  "v_mfma_f32_16x16x32_bf16 v[104:107], a[148:151], v[148:151], v[104:107]\n"
  "ds_read_b128 a[172:175], v227 offset:61440\n"
  "v_cvt_pk_bf16_f32 v50, v52, v53\n"
  "v_mfma_f32_16x16x32_bf16 v[104:107], a[152:155], v[152:155], v[104:107]\n"
  "v_exp_f32 v54, v54\n"
  "v_mfma_f32_16x16x32_bf16 v[104:107], a[156:159], v[156:159], v[104:107]\n"
  "ds_read_b64_tr_b16 a[196:197], v221 offset:32768\n"
  "ds_read_b64_tr_b16 a[198:199], v221 offset:36864\n"
  "v_mfma_f32_16x16x32_bf16 v[112:115], a[144:147], v[160:163], v[200:203]\n"
  "v_exp_f32 v55, v55\n"
  "v_mfma_f32_16x16x32_bf16 v[112:115], a[148:151], v[164:167], v[112:115]\n"
  "ds_read_b64_tr_b16 a[228:229], v221 offset:40960\n"
  "v_cvt_pk_bf16_f32 v51, v54, v55\n"
  "v_mfma_f32_16x16x32_bf16 v[112:115], a[152:155], v[168:171], v[112:115]\n"
  "v_exp_f32 v56, v56\n"
  "v_mfma_f32_16x16x32_bf16 v[112:115], a[156:159], v[172:175], v[112:115]\n"
  "ds_read_b64_tr_b16 a[230:231], v221 offset:45056\n"
  "ds_read_b64_tr_b16 a[200:201], v222 offset:32768\n"
  "v_mfma_f32_16x16x32_bf16 v[120:123], a[144:147], v[176:179], v[204:207]\n"
  "v_exp_f32 v57, v57\n"
  "v_mfma_f32_16x16x32_bf16 v[120:123], a[148:151], v[180:183], v[120:123]\n"
  "ds_read_b64_tr_b16 a[202:203], v222 offset:36864\n"
  "v_cvt_pk_bf16_f32 v56, v56, v57\n"
  "v_mfma_f32_16x16x32_bf16 v[120:123], a[152:155], v[184:187], v[120:123]\n"
  "v_exp_f32 v58, v58\n"
  "v_mfma_f32_16x16x32_bf16 v[120:123], a[156:159], v[188:191], v[120:123]\n"
  "s_waitcnt lgkmcnt(6)\n"
  "ds_read_b64_tr_b16 a[232:233], v222 offset:40960\n"
  "ds_read_b64_tr_b16 a[234:235], v222 offset:45056\n"
  "v_mfma_f32_16x16x32_bf16 v[100:103], a[160:163], v[128:131], v[192:195]\n"
  "v_exp_f32 v59, v59\n"
  "v_mfma_f32_16x16x32_bf16 v[100:103], a[164:167], v[132:135], v[100:103]\n"
  "v_cvt_pk_bf16_f32 v57, v58, v59\n"
  "v_mfma_f32_16x16x32_bf16 v[100:103], a[168:171], v[136:139], v[100:103]\n"
  "v_exp_f32 v60, v60\n"
  "v_mfma_f32_16x16x32_bf16 v[100:103], a[172:175], v[140:143], v[100:103]\n"
  "ds_read_b64_tr_b16 a[204:205], v223 offset:32768\n"
  "ds_read_b64_tr_b16 a[206:207], v223 offset:36864\n"
  "v_mfma_f32_16x16x32_bf16 v[108:111], a[160:163], v[144:147], v[196:199]\n"
  "v_exp_f32 v61, v61\n"
  "v_mfma_f32_16x16x32_bf16 v[108:111], a[164:167], v[148:151], v[108:111]\n"
  "v_cvt_pk_bf16_f32 v58, v60, v61\n"
  "v_mfma_f32_16x16x32_bf16 v[108:111], a[168:171], v[152:155], v[108:111]\n"
  "v_exp_f32 v62, v62\n"
  "v_mfma_f32_16x16x32_bf16 v[108:111], a[172:175], v[156:159], v[108:111]\n"
  "ds_read_b64_tr_b16 a[236:237], v223 offset:40960\n"
  "ds_read_b64_tr_b16 a[238:239], v223 offset:45056\n"
  "v_mfma_f32_16x16x32_bf16 v[116:119], a[160:163], v[160:163], v[200:203]\n"
  "v_exp_f32 v63, v63\n"
  "v_mfma_f32_16x16x32_bf16 v[116:119], a[164:167], v[164:167], v[116:119]\n"
  "v_cvt_pk_bf16_f32 v59, v62, v63\n"
  "v_mfma_f32_16x16x32_bf16 v[116:119], a[168:171], v[168:171], v[116:119]\n"
  "v_mfma_f32_16x16x32_bf16 v[116:119], a[172:175], v[172:175], v[116:119]\n"
  "v_mfma_f32_16x16x32_bf16 v[124:127], a[160:163], v[176:179], v[204:207]\n"
  "v_mfma_f32_16x16x32_bf16 v[124:127], a[164:167], v[180:183], v[124:127]\n"
  "v_mfma_f32_16x16x32_bf16 v[124:127], a[168:171], v[184:187], v[124:127]\n"
  "v_mfma_f32_16x16x32_bf16 v[124:127], a[172:175], v[188:191], v[124:127]\n"
  "s_waitcnt vmcnt(4) lgkmcnt(0)\n"
  "s_barrier\n"
  "v_mfma_f32_16x16x32_bf16 a[0:3], a[176:179], v[0:3], a[0:3]\n"
  "s_add_u32 m0, s42, 65536\n"
  "v_mfma_f32_16x16x32_bf16 a[0:3], a[208:211], v[32:35], a[0:3]\n"
  "buffer_load_dwordx4 v232, s[24:27], s41 offen offset:0 nt lds\n"
  "v_mfma_f32_16x16x32_bf16 a[16:19], a[180:183], v[0:3], a[16:19]\n"
  "v_exp_f32 v64, v64\n"
  "v_mfma_f32_16x16x32_bf16 a[16:19], a[212:215], v[32:35], a[16:19]\n"
  "buffer_load_dwordx4 v233, s[24:27], s41 offen offset:1024 nt lds\n"
  "v_mfma_f32_16x16x32_bf16 a[32:35], a[184:187], v[0:3], a[32:35]\n"
  "v_exp_f32 v65, v65\n"
  "v_mfma_f32_16x16x32_bf16 a[32:35], a[216:219], v[32:35], a[32:35]\n"
  "v_cvt_pk_bf16_f32 v64, v64, v65\n"
  "v_mfma_f32_16x16x32_bf16 a[48:51], a[188:191], v[0:3], a[48:51]\n"
  "v_exp_f32 v66, v66\n"
  "v_mfma_f32_16x16x32_bf16 a[48:51], a[220:223], v[32:35], a[48:51]\n"
  "ds_read_b128 a[144:147], v224 offset:0\n"
  "v_mfma_f32_16x16x32_bf16 a[64:67], a[192:195], v[0:3], a[64:67]\n"
  "v_exp_f32 v67, v67\n"
  "v_mfma_f32_16x16x32_bf16 a[64:67], a[224:227], v[32:35], a[64:67]\n"
  "ds_read_b128 a[148:151], v225 offset:0\n"
  "v_cvt_pk_bf16_f32 v65, v66, v67\n"
  "v_mfma_f32_16x16x32_bf16 a[80:83], a[196:199], v[0:3], a[80:83]\n"
  "v_exp_f32 v68, v68\n"
  "v_mfma_f32_16x16x32_bf16 a[80:83], a[228:231], v[32:35], a[80:83]\n"
  "ds_read_b128 a[152:155], v226 offset:0\n"
  "v_mfma_f32_16x16x32_bf16 a[96:99], a[200:203], v[0:3], a[96:99]\n"
  "v_exp_f32 v69, v69\n"
  "v_mfma_f32_16x16x32_bf16 a[96:99], a[232:235], v[32:35], a[96:99]\n"
  "ds_read_b128 a[156:159], v227 offset:0\n"
  "v_cvt_pk_bf16_f32 v66, v68, v69\n"
  "v_mfma_f32_16x16x32_bf16 a[112:115], a[204:207], v[0:3], a[112:115]\n"
  "v_exp_f32 v70, v70\n"
  "v_mfma_f32_16x16x32_bf16 a[112:115], a[236:239], v[32:35], a[112:115]\n"
  "ds_read_b128 a[160:163], v224 offset:4096\n"
  "v_mfma_f32_16x16x32_bf16 a[128:131], v[208:211], v[0:3], a[128:131]\n"
  "v_exp_f32 v71, v71\n"
  "v_mfma_f32_16x16x32_bf16 a[128:131], v[208:211], v[32:35], a[128:131]\n"
  "ds_read_b128 a[164:167], v225 offset:4096\n"
  "v_cvt_pk_bf16_f32 v67, v70, v71\n"
  "v_mfma_f32_16x16x32_bf16 a[4:7], a[176:179], v[8:11], a[4:7]\n"
  "v_exp_f32 v72, v72\n"
  "v_mfma_f32_16x16x32_bf16 a[4:7], a[208:211], v[40:43], a[4:7]\n"
  "ds_read_b128 a[168:171], v226 offset:4096\n"
  "v_mfma_f32_16x16x32_bf16 a[20:23], a[180:183], v[8:11], a[20:23]\n"
  "v_exp_f32 v73, v73\n"
  "v_mfma_f32_16x16x32_bf16 a[20:23], a[212:215], v[40:43], a[20:23]\n"
  "ds_read_b128 a[172:175], v227 offset:4096\n"
  "v_cvt_pk_bf16_f32 v72, v72, v73\n"
  "v_mfma_f32_16x16x32_bf16 a[36:39], a[184:187], v[8:11], a[36:39]\n"
  "v_exp_f32 v74, v74\n"
  "v_mfma_f32_16x16x32_bf16 a[36:39], a[216:219], v[40:43], a[36:39]\n"
  "buffer_load_dwordx4 v234, s[24:27], s41 offen offset:2048 nt lds\n"
  "v_mfma_f32_16x16x32_bf16 a[52:55], a[188:191], v[8:11], a[52:55]\n"
  "v_exp_f32 v75, v75\n"
  "v_mfma_f32_16x16x32_bf16 a[52:55], a[220:223], v[40:43], a[52:55]\n"
  "v_cvt_pk_bf16_f32 v73, v74, v75\n"
  "v_mfma_f32_16x16x32_bf16 a[68:71], a[192:195], v[8:11], a[68:71]\n"
  "v_exp_f32 v76, v76\n"
  "v_mfma_f32_16x16x32_bf16 a[68:71], a[224:227], v[40:43], a[68:71]\n"
  "buffer_load_dwordx4 v235, s[24:27], s41 offen offset:3072 nt lds\n"
  "v_mfma_f32_16x16x32_bf16 a[84:87], a[196:199], v[8:11], a[84:87]\n"
  "s_add_u32 s41, s41, 16384\n"
  "v_exp_f32 v77, v77\n"
  "v_mfma_f32_16x16x32_bf16 a[84:87], a[228:231], v[40:43], a[84:87]\n"
  "v_cvt_pk_bf16_f32 v74, v76, v77\n"
  "v_mfma_f32_16x16x32_bf16 a[100:103], a[200:203], v[8:11], a[100:103]\n"
  "v_exp_f32 v78, v78\n"
  "v_mfma_f32_16x16x32_bf16 a[100:103], a[232:235], v[40:43], a[100:103]\n"
  "v_mfma_f32_16x16x32_bf16 a[116:119], a[204:207], v[8:11], a[116:119]\n"
  "v_exp_f32 v79, v79\n"
  "v_mfma_f32_16x16x32_bf16 a[116:119], a[236:239], v[40:43], a[116:119]\n"
  "v_cvt_pk_bf16_f32 v75, v78, v79\n"
  "v_mfma_f32_16x16x32_bf16 a[132:135], v[208:211], v[8:11], a[132:135]\n"
  "v_exp_f32 v80, v80\n"
  "v_mfma_f32_16x16x32_bf16 a[132:135], v[208:211], v[40:43], a[132:135]\n"
  "v_mfma_f32_16x16x32_bf16 a[8:11], a[176:179], v[16:19], a[8:11]\n"
  "v_exp_f32 v81, v81\n"
  "v_mfma_f32_16x16x32_bf16 a[8:11], a[208:211], v[48:51], a[8:11]\n"
  "v_cvt_pk_bf16_f32 v80, v80, v81\n"
  "v_mfma_f32_16x16x32_bf16 a[24:27], a[180:183], v[16:19], a[24:27]\n"
  "s_add_u32 m0, s42, 32768\n"
  "v_exp_f32 v82, v82\n"
  "v_mfma_f32_16x16x32_bf16 a[24:27], a[212:215], v[48:51], a[24:27]\n"
  "buffer_load_dwordx4 v228, s[20:23], s40 offen offset:0 nt lds\n"
  "v_mfma_f32_16x16x32_bf16 a[40:43], a[184:187], v[16:19], a[40:43]\n"
  "v_exp_f32 v83, v83\n"
  "v_mfma_f32_16x16x32_bf16 a[40:43], a[216:219], v[48:51], a[40:43]\n"
  "v_cvt_pk_bf16_f32 v81, v82, v83\n"
  "v_mfma_f32_16x16x32_bf16 a[56:59], a[188:191], v[16:19], a[56:59]\n"
  "v_exp_f32 v84, v84\n"
  "v_mfma_f32_16x16x32_bf16 a[56:59], a[220:223], v[48:51], a[56:59]\n"
  "buffer_load_dwordx4 v229, s[20:23], s40 offen offset:1024 nt lds\n"
  "v_mfma_f32_16x16x32_bf16 a[72:75], a[192:195], v[16:19], a[72:75]\n"
  "v_exp_f32 v85, v85\n"
  "v_mfma_f32_16x16x32_bf16 a[72:75], a[224:227], v[48:51], a[72:75]\n"
  "v_cvt_pk_bf16_f32 v82, v84, v85\n"
  "v_mfma_f32_16x16x32_bf16 a[88:91], a[196:199], v[16:19], a[88:91]\n"
  "v_exp_f32 v86, v86\n"
  "v_mfma_f32_16x16x32_bf16 a[88:91], a[228:231], v[48:51], a[88:91]\n"
  "buffer_load_dwordx4 v230, s[20:23], s40 offen offset:2048 nt lds\n"
  "v_mfma_f32_16x16x32_bf16 a[104:107], a[200:203], v[16:19], a[104:107]\n"
  "v_exp_f32 v87, v87\n"
  "v_mfma_f32_16x16x32_bf16 a[104:107], a[232:235], v[48:51], a[104:107]\n"
  "v_cvt_pk_bf16_f32 v83, v86, v87\n"
  "v_mfma_f32_16x16x32_bf16 a[120:123], a[204:207], v[16:19], a[120:123]\n"
  "v_exp_f32 v88, v88\n"
  "v_mfma_f32_16x16x32_bf16 a[120:123], a[236:239], v[48:51], a[120:123]\n"
  "buffer_load_dwordx4 v231, s[20:23], s40 offen offset:3072 nt lds\n"
  "v_mfma_f32_16x16x32_bf16 a[136:139], v[208:211], v[16:19], a[136:139]\n"
  "s_add_u32 s40, s40, 16384\n"
  "v_exp_f32 v89, v89\n"
  "v_mfma_f32_16x16x32_bf16 a[136:139], v[208:211], v[48:51], a[136:139]\n"
  "v_cvt_pk_bf16_f32 v88, v88, v89\n"
  "v_mfma_f32_16x16x32_bf16 a[12:15], a[176:179], v[24:27], a[12:15]\n"
  "v_exp_f32 v90, v90\n"
  "v_mfma_f32_16x16x32_bf16 a[12:15], a[208:211], v[56:59], a[12:15]\n"
  "v_mfma_f32_16x16x32_bf16 a[28:31], a[180:183], v[24:27], a[28:31]\n"
  "v_exp_f32 v91, v91\n"
  "v_mfma_f32_16x16x32_bf16 a[28:31], a[212:215], v[56:59], a[28:31]\n"
  "v_cvt_pk_bf16_f32 v89, v90, v91\n"
  "v_mfma_f32_16x16x32_bf16 a[44:47], a[184:187], v[24:27], a[44:47]\n"
  "v_exp_f32 v92, v92\n"
  "v_mfma_f32_16x16x32_bf16 a[44:47], a[216:219], v[56:59], a[44:47]\n"
  "v_mfma_f32_16x16x32_bf16 a[60:63], a[188:191], v[24:27], a[60:63]\n"
  "v_exp_f32 v93, v93\n"
  "v_mfma_f32_16x16x32_bf16 a[60:63], a[220:223], v[56:59], a[60:63]\n"
  "v_cvt_pk_bf16_f32 v90, v92, v93\n"
  "v_mfma_f32_16x16x32_bf16 a[76:79], a[192:195], v[24:27], a[76:79]\n"
  "v_exp_f32 v94, v94\n"
  "v_mfma_f32_16x16x32_bf16 a[76:79], a[224:227], v[56:59], a[76:79]\n"
  "v_mfma_f32_16x16x32_bf16 a[92:95], a[196:199], v[24:27], a[92:95]\n"
  "v_exp_f32 v95, v95\n"
  "v_mfma_f32_16x16x32_bf16 a[92:95], a[228:231], v[56:59], a[92:95]\n"
  "v_cvt_pk_bf16_f32 v91, v94, v95\n"
  "v_mfma_f32_16x16x32_bf16 a[108:111], a[200:203], v[24:27], a[108:111]\n"
  "v_exp_f32 v96, v96\n"
  "v_mfma_f32_16x16x32_bf16 a[108:111], a[232:235], v[56:59], a[108:111]\n"
  "v_mfma_f32_16x16x32_bf16 a[124:127], a[204:207], v[24:27], a[124:127]\n"
  "v_exp_f32 v97, v97\n"
  "v_mfma_f32_16x16x32_bf16 a[124:127], a[236:239], v[56:59], a[124:127]\n"
  "v_cvt_pk_bf16_f32 v96, v96, v97\n"
  "v_mfma_f32_16x16x32_bf16 a[140:143], v[208:211], v[24:27], a[140:143]\n"
  "v_exp_f32 v98, v98\n"
  "v_mfma_f32_16x16x32_bf16 a[140:143], v[208:211], v[56:59], a[140:143]\n"
  "s_waitcnt lgkmcnt(0)\n"
  "v_mfma_f32_16x16x32_bf16 v[0:3], a[144:147], v[128:131], v[192:195]\n"
  "v_exp_f32 v99, v99\n"
  "v_mfma_f32_16x16x32_bf16 v[0:3], a[148:151], v[132:135], v[0:3]\n"
  "ds_read_b64_tr_b16 a[176:177], v216 offset:49152\n"
  "v_cvt_pk_bf16_f32 v97, v98, v99\n"
  "v_mfma_f32_16x16x32_bf16 v[0:3], a[152:155], v[136:139], v[0:3]\n"
  "v_exp_f32 v100, v100\n"
  "v_mfma_f32_16x16x32_bf16 v[0:3], a[156:159], v[140:143], v[0:3]\n"
  "ds_read_b64_tr_b16 a[178:179], v216 offset:53248\n"
  "ds_read_b64_tr_b16 a[208:209], v216 offset:57344\n"
  "v_mfma_f32_16x16x32_bf16 v[8:11], a[144:147], v[144:147], v[196:199]\n"
  "v_exp_f32 v101, v101\n"
  "v_mfma_f32_16x16x32_bf16 v[8:11], a[148:151], v[148:151], v[8:11]\n"
  "ds_read_b64_tr_b16 a[210:211], v216 offset:61440\n"
  "v_cvt_pk_bf16_f32 v98, v100, v101\n"
  "v_mfma_f32_16x16x32_bf16 v[8:11], a[152:155], v[152:155], v[8:11]\n"
  "v_exp_f32 v102, v102\n"
  "v_mfma_f32_16x16x32_bf16 v[8:11], a[156:159], v[156:159], v[8:11]\n"
  "ds_read_b64_tr_b16 a[180:181], v217 offset:49152\n"
  "ds_read_b64_tr_b16 a[182:183], v217 offset:53248\n"
  "v_mfma_f32_16x16x32_bf16 v[16:19], a[144:147], v[160:163], v[200:203]\n"
  "v_exp_f32 v103, v103\n"
  "v_mfma_f32_16x16x32_bf16 v[16:19], a[148:151], v[164:167], v[16:19]\n"
  "ds_read_b64_tr_b16 a[212:213], v217 offset:57344\n"
  "v_cvt_pk_bf16_f32 v99, v102, v103\n"
  "v_mfma_f32_16x16x32_bf16 v[16:19], a[152:155], v[168:171], v[16:19]\n"
  "v_exp_f32 v104, v104\n"
  "v_mfma_f32_16x16x32_bf16 v[16:19], a[156:159], v[172:175], v[16:19]\n"
  "ds_read_b64_tr_b16 a[214:215], v217 offset:61440\n"
  "ds_read_b64_tr_b16 a[184:185], v218 offset:49152\n"
  "v_mfma_f32_16x16x32_bf16 v[24:27], a[144:147], v[176:179], v[204:207]\n"
  "v_exp_f32 v105, v105\n"
  "v_mfma_f32_16x16x32_bf16 v[24:27], a[148:151], v[180:183], v[24:27]\n"
  "ds_read_b64_tr_b16 a[186:187], v218 offset:53248\n"
  "v_cvt_pk_bf16_f32 v104, v104, v105\n"
  "v_mfma_f32_16x16x32_bf16 v[24:27], a[152:155], v[184:187], v[24:27]\n"
  "v_exp_f32 v106, v106\n"
  "v_mfma_f32_16x16x32_bf16 v[24:27], a[156:159], v[188:191], v[24:27]\n"
  "ds_read_b64_tr_b16 a[216:217], v218 offset:57344\n"
  "ds_read_b64_tr_b16 a[218:219], v218 offset:61440\n"
  "v_mfma_f32_16x16x32_bf16 v[4:7], a[160:163], v[128:131], v[192:195]\n"
  "v_exp_f32 v107, v107\n"
  "v_mfma_f32_16x16x32_bf16 v[4:7], a[164:167], v[132:135], v[4:7]\n"
  "ds_read_b128 a[144:147], v224 offset:8192\n"
  "v_cvt_pk_bf16_f32 v105, v106, v107\n"
  "v_mfma_f32_16x16x32_bf16 v[4:7], a[168:171], v[136:139], v[4:7]\n"
  "v_exp_f32 v108, v108\n"
  "v_mfma_f32_16x16x32_bf16 v[4:7], a[172:175], v[140:143], v[4:7]\n"
  "ds_read_b128 a[148:151], v225 offset:8192\n"
  "ds_read_b128 a[152:155], v226 offset:8192\n"
  "v_mfma_f32_16x16x32_bf16 v[12:15], a[160:163], v[144:147], v[196:199]\n"
  "v_exp_f32 v109, v109\n"
  "v_mfma_f32_16x16x32_bf16 v[12:15], a[164:167], v[148:151], v[12:15]\n"
  "ds_read_b128 a[156:159], v227 offset:8192\n"
  "v_cvt_pk_bf16_f32 v106, v108, v109\n"
  "v_mfma_f32_16x16x32_bf16 v[12:15], a[168:171], v[152:155], v[12:15]\n"
  "v_exp_f32 v110, v110\n"
  "v_mfma_f32_16x16x32_bf16 v[12:15], a[172:175], v[156:159], v[12:15]\n"
  "ds_read_b64_tr_b16 a[188:189], v219 offset:49152\n"
  "ds_read_b64_tr_b16 a[190:191], v219 offset:53248\n"
  "v_mfma_f32_16x16x32_bf16 v[20:23], a[160:163], v[160:163], v[200:203]\n"
  "v_exp_f32 v111, v111\n"
  "v_mfma_f32_16x16x32_bf16 v[20:23], a[164:167], v[164:167], v[20:23]\n"
  "ds_read_b64_tr_b16 a[220:221], v219 offset:57344\n"
  "v_cvt_pk_bf16_f32 v107, v110, v111\n"
  "v_mfma_f32_16x16x32_bf16 v[20:23], a[168:171], v[168:171], v[20:23]\n"
  "v_exp_f32 v112, v112\n"
  "v_mfma_f32_16x16x32_bf16 v[20:23], a[172:175], v[172:175], v[20:23]\n"
  "ds_read_b64_tr_b16 a[222:223], v219 offset:61440\n"
  "ds_read_b64_tr_b16 a[192:193], v220 offset:49152\n"
  "v_mfma_f32_16x16x32_bf16 v[28:31], a[160:163], v[176:179], v[204:207]\n"
  "v_exp_f32 v113, v113\n"
  "v_mfma_f32_16x16x32_bf16 v[28:31], a[164:167], v[180:183], v[28:31]\n"
  "ds_read_b64_tr_b16 a[194:195], v220 offset:53248\n"
  "v_cvt_pk_bf16_f32 v112, v112, v113\n"
  "v_mfma_f32_16x16x32_bf16 v[28:31], a[168:171], v[184:187], v[28:31]\n"
  "v_exp_f32 v114, v114\n"
  "v_mfma_f32_16x16x32_bf16 v[28:31], a[172:175], v[188:191], v[28:31]\n"
  "s_waitcnt lgkmcnt(6)\n"
  "ds_read_b64_tr_b16 a[224:225], v220 offset:57344\n"
  "ds_read_b64_tr_b16 a[226:227], v220 offset:61440\n"
  "v_mfma_f32_16x16x32_bf16 v[32:35], a[144:147], v[128:131], v[192:195]\n"
  "v_exp_f32 v115, v115\n"
  "v_mfma_f32_16x16x32_bf16 v[32:35], a[148:151], v[132:135], v[32:35]\n"
  "ds_read_b128 a[160:163], v224 offset:12288\n"
  "v_cvt_pk_bf16_f32 v113, v114, v115\n"
  "v_mfma_f32_16x16x32_bf16 v[32:35], a[152:155], v[136:139], v[32:35]\n"
  "v_exp_f32 v116, v116\n"
  "v_mfma_f32_16x16x32_bf16 v[32:35], a[156:159], v[140:143], v[32:35]\n"
  "ds_read_b128 a[164:167], v225 offset:12288\n"
  "ds_read_b128 a[168:171], v226 offset:12288\n"
  "v_mfma_f32_16x16x32_bf16 v[40:43], a[144:147], v[144:147], v[196:199]\n"
  "v_exp_f32 v117, v117\n"
  "v_mfma_f32_16x16x32_bf16 v[40:43], a[148:151], v[148:151], v[40:43]\n"
  "ds_read_b128 a[172:175], v227 offset:12288\n"
  "v_cvt_pk_bf16_f32 v114, v116, v117\n"
  "v_mfma_f32_16x16x32_bf16 v[40:43], a[152:155], v[152:155], v[40:43]\n"
  "v_exp_f32 v118, v118\n"
  "v_mfma_f32_16x16x32_bf16 v[40:43], a[156:159], v[156:159], v[40:43]\n"
  "ds_read_b64_tr_b16 a[196:197], v221 offset:49152\n"
  "ds_read_b64_tr_b16 a[198:199], v221 offset:53248\n"
  "v_mfma_f32_16x16x32_bf16 v[48:51], a[144:147], v[160:163], v[200:203]\n"
  "v_exp_f32 v119, v119\n"
  "v_mfma_f32_16x16x32_bf16 v[48:51], a[148:151], v[164:167], v[48:51]\n"
  "ds_read_b64_tr_b16 a[228:229], v221 offset:57344\n"
  "v_cvt_pk_bf16_f32 v115, v118, v119\n"
  "v_mfma_f32_16x16x32_bf16 v[48:51], a[152:155], v[168:171], v[48:51]\n"
  "v_exp_f32 v120, v120\n"
  "v_mfma_f32_16x16x32_bf16 v[48:51], a[156:159], v[172:175], v[48:51]\n"
  "ds_read_b64_tr_b16 a[230:231], v221 offset:61440\n"
  "ds_read_b64_tr_b16 a[200:201], v222 offset:49152\n"
  "v_mfma_f32_16x16x32_bf16 v[56:59], a[144:147], v[176:179], v[204:207]\n"
  "v_exp_f32 v121, v121\n"
  "v_mfma_f32_16x16x32_bf16 v[56:59], a[148:151], v[180:183], v[56:59]\n"
  "ds_read_b64_tr_b16 a[202:203], v222 offset:53248\n"
  "v_cvt_pk_bf16_f32 v120, v120, v121\n"
  "v_mfma_f32_16x16x32_bf16 v[56:59], a[152:155], v[184:187], v[56:59]\n"
  "v_exp_f32 v122, v122\n"
  "v_mfma_f32_16x16x32_bf16 v[56:59], a[156:159], v[188:191], v[56:59]\n"
  "s_waitcnt lgkmcnt(6)\n"
  "ds_read_b64_tr_b16 a[232:233], v222 offset:57344\n"
  "ds_read_b64_tr_b16 a[234:235], v222 offset:61440\n"
  "v_mfma_f32_16x16x32_bf16 v[36:39], a[160:163], v[128:131], v[192:195]\n"
  "v_exp_f32 v123, v123\n"
  "v_mfma_f32_16x16x32_bf16 v[36:39], a[164:167], v[132:135], v[36:39]\n"
  "v_cvt_pk_bf16_f32 v121, v122, v123\n"
  "v_mfma_f32_16x16x32_bf16 v[36:39], a[168:171], v[136:139], v[36:39]\n"
  "v_exp_f32 v124, v124\n"
  "v_mfma_f32_16x16x32_bf16 v[36:39], a[172:175], v[140:143], v[36:39]\n"
  "ds_read_b64_tr_b16 a[204:205], v223 offset:49152\n"
  "ds_read_b64_tr_b16 a[206:207], v223 offset:53248\n"
  "v_mfma_f32_16x16x32_bf16 v[44:47], a[160:163], v[144:147], v[196:199]\n"
  "v_exp_f32 v125, v125\n"
  "v_mfma_f32_16x16x32_bf16 v[44:47], a[164:167], v[148:151], v[44:47]\n"
  "v_cvt_pk_bf16_f32 v122, v124, v125\n"
  "v_mfma_f32_16x16x32_bf16 v[44:47], a[168:171], v[152:155], v[44:47]\n"
  "v_exp_f32 v126, v126\n"
  "v_mfma_f32_16x16x32_bf16 v[44:47], a[172:175], v[156:159], v[44:47]\n"
  "ds_read_b64_tr_b16 a[236:237], v223 offset:57344\n"
  "ds_read_b64_tr_b16 a[238:239], v223 offset:61440\n"
  "v_mfma_f32_16x16x32_bf16 v[52:55], a[160:163], v[160:163], v[200:203]\n"
  "v_exp_f32 v127, v127\n"
  "v_mfma_f32_16x16x32_bf16 v[52:55], a[164:167], v[164:167], v[52:55]\n"
  "v_cvt_pk_bf16_f32 v123, v126, v127\n"
  "v_mfma_f32_16x16x32_bf16 v[52:55], a[168:171], v[168:171], v[52:55]\n"
  "v_mfma_f32_16x16x32_bf16 v[52:55], a[172:175], v[172:175], v[52:55]\n"
  "v_mfma_f32_16x16x32_bf16 v[60:63], a[160:163], v[176:179], v[204:207]\n"
  "v_mfma_f32_16x16x32_bf16 v[60:63], a[164:167], v[180:183], v[60:63]\n"
  "v_mfma_f32_16x16x32_bf16 v[60:63], a[168:171], v[184:187], v[60:63]\n"
  "v_mfma_f32_16x16x32_bf16 v[60:63], a[172:175], v[188:191], v[60:63]\n"
  "s_waitcnt vmcnt(4) lgkmcnt(0)\n"
  "s_barrier\n"
  "v_mfma_f32_16x16x32_bf16 a[0:3], a[176:179], v[64:67], a[0:3]\n"
  "s_add_u32 m0, s42, 81920\n"
  "v_mfma_f32_16x16x32_bf16 a[0:3], a[208:211], v[96:99], a[0:3]\n"
  "buffer_load_dwordx4 v232, s[24:27], s41 offen offset:0 nt lds\n"
  "v_mfma_f32_16x16x32_bf16 a[16:19], a[180:183], v[64:67], a[16:19]\n"
  "v_exp_f32 v0, v0\n"
  "v_mfma_f32_16x16x32_bf16 a[16:19], a[212:215], v[96:99], a[16:19]\n"
  "buffer_load_dwordx4 v233, s[24:27], s41 offen offset:1024 nt lds\n"
  "v_mfma_f32_16x16x32_bf16 a[32:35], a[184:187], v[64:67], a[32:35]\n"
  "v_exp_f32 v1, v1\n"
  "v_mfma_f32_16x16x32_bf16 a[32:35], a[216:219], v[96:99], a[32:35]\n"
  "v_cvt_pk_bf16_f32 v0, v0, v1\n"
  "v_mfma_f32_16x16x32_bf16 a[48:51], a[188:191], v[64:67], a[48:51]\n"
  "v_exp_f32 v2, v2\n"
  "v_mfma_f32_16x16x32_bf16 a[48:51], a[220:223], v[96:99], a[48:51]\n"
  "ds_read_b128 a[144:147], v224 offset:16384\n"
  "v_mfma_f32_16x16x32_bf16 a[64:67], a[192:195], v[64:67], a[64:67]\n"
  "v_exp_f32 v3, v3\n"
  "v_mfma_f32_16x16x32_bf16 a[64:67], a[224:227], v[96:99], a[64:67]\n"
  "ds_read_b128 a[148:151], v225 offset:16384\n"
  "v_cvt_pk_bf16_f32 v1, v2, v3\n"
  "v_mfma_f32_16x16x32_bf16 a[80:83], a[196:199], v[64:67], a[80:83]\n"
  "v_exp_f32 v4, v4\n"
  "v_mfma_f32_16x16x32_bf16 a[80:83], a[228:231], v[96:99], a[80:83]\n"
  "ds_read_b128 a[152:155], v226 offset:16384\n"
  "v_mfma_f32_16x16x32_bf16 a[96:99], a[200:203], v[64:67], a[96:99]\n"
  "v_exp_f32 v5, v5\n"
  "v_mfma_f32_16x16x32_bf16 a[96:99], a[232:235], v[96:99], a[96:99]\n"
  "ds_read_b128 a[156:159], v227 offset:16384\n"
  "v_cvt_pk_bf16_f32 v2, v4, v5\n"
  "v_mfma_f32_16x16x32_bf16 a[112:115], a[204:207], v[64:67], a[112:115]\n"
  "v_exp_f32 v6, v6\n"
  "v_mfma_f32_16x16x32_bf16 a[112:115], a[236:239], v[96:99], a[112:115]\n"
  "ds_read_b128 a[160:163], v224 offset:20480\n"
  "v_mfma_f32_16x16x32_bf16 a[128:131], v[208:211], v[64:67], a[128:131]\n"
  "v_exp_f32 v7, v7\n"
  "v_mfma_f32_16x16x32_bf16 a[128:131], v[208:211], v[96:99], a[128:131]\n"
  "ds_read_b128 a[164:167], v225 offset:20480\n"
  "v_cvt_pk_bf16_f32 v3, v6, v7\n"
  "v_mfma_f32_16x16x32_bf16 a[4:7], a[176:179], v[72:75], a[4:7]\n"
  "v_exp_f32 v8, v8\n"
  "v_mfma_f32_16x16x32_bf16 a[4:7], a[208:211], v[104:107], a[4:7]\n"
  "ds_read_b128 a[168:171], v226 offset:20480\n"
  "v_mfma_f32_16x16x32_bf16 a[20:23], a[180:183], v[72:75], a[20:23]\n"
  "v_exp_f32 v9, v9\n"
  "v_mfma_f32_16x16x32_bf16 a[20:23], a[212:215], v[104:107], a[20:23]\n"
  "ds_read_b128 a[172:175], v227 offset:20480\n"
  "v_cvt_pk_bf16_f32 v8, v8, v9\n"
  "v_mfma_f32_16x16x32_bf16 a[36:39], a[184:187], v[72:75], a[36:39]\n"
  "v_exp_f32 v10, v10\n"
  "v_mfma_f32_16x16x32_bf16 a[36:39], a[216:219], v[104:107], a[36:39]\n"
  "buffer_load_dwordx4 v234, s[24:27], s41 offen offset:2048 nt lds\n"
  "v_mfma_f32_16x16x32_bf16 a[52:55], a[188:191], v[72:75], a[52:55]\n"
  "v_exp_f32 v11, v11\n"
  "v_mfma_f32_16x16x32_bf16 a[52:55], a[220:223], v[104:107], a[52:55]\n"
  "v_cvt_pk_bf16_f32 v9, v10, v11\n"
  "v_mfma_f32_16x16x32_bf16 a[68:71], a[192:195], v[72:75], a[68:71]\n"
  "v_exp_f32 v12, v12\n"
  "v_mfma_f32_16x16x32_bf16 a[68:71], a[224:227], v[104:107], a[68:71]\n"
  "buffer_load_dwordx4 v235, s[24:27], s41 offen offset:3072 nt lds\n"
  "v_mfma_f32_16x16x32_bf16 a[84:87], a[196:199], v[72:75], a[84:87]\n"
  "s_add_u32 s41, s41, 16384\n"
  "v_exp_f32 v13, v13\n"
  "v_mfma_f32_16x16x32_bf16 a[84:87], a[228:231], v[104:107], a[84:87]\n"
  "v_cvt_pk_bf16_f32 v10, v12, v13\n"
  "v_mfma_f32_16x16x32_bf16 a[100:103], a[200:203], v[72:75], a[100:103]\n"
  "v_exp_f32 v14, v14\n"
  "v_mfma_f32_16x16x32_bf16 a[100:103], a[232:235], v[104:107], a[100:103]\n"
  "v_mfma_f32_16x16x32_bf16 a[116:119], a[204:207], v[72:75], a[116:119]\n"
  "v_exp_f32 v15, v15\n"
  "v_mfma_f32_16x16x32_bf16 a[116:119], a[236:239], v[104:107], a[116:119]\n"
  "v_cvt_pk_bf16_f32 v11, v14, v15\n"
  "v_mfma_f32_16x16x32_bf16 a[132:135], v[208:211], v[72:75], a[132:135]\n"
  "v_exp_f32 v16, v16\n"
  "v_mfma_f32_16x16x32_bf16 a[132:135], v[208:211], v[104:107], a[132:135]\n"
  "v_mfma_f32_16x16x32_bf16 a[8:11], a[176:179], v[80:83], a[8:11]\n"
  "v_exp_f32 v17, v17\n"
  "v_mfma_f32_16x16x32_bf16 a[8:11], a[208:211], v[112:115], a[8:11]\n"
  "v_cvt_pk_bf16_f32 v16, v16, v17\n"
  "v_mfma_f32_16x16x32_bf16 a[24:27], a[180:183], v[80:83], a[24:27]\n"
  "s_add_u32 m0, s42, 49152\n"
  "v_exp_f32 v18, v18\n"
  "v_mfma_f32_16x16x32_bf16 a[24:27], a[212:215], v[112:115], a[24:27]\n"
  "buffer_load_dwordx4 v228, s[20:23], s40 offen offset:0 nt lds\n"
  "v_mfma_f32_16x16x32_bf16 a[40:43], a[184:187], v[80:83], a[40:43]\n"
  "v_exp_f32 v19, v19\n"
  "v_mfma_f32_16x16x32_bf16 a[40:43], a[216:219], v[112:115], a[40:43]\n"
  "v_cvt_pk_bf16_f32 v17, v18, v19\n"
  "v_mfma_f32_16x16x32_bf16 a[56:59], a[188:191], v[80:83], a[56:59]\n"
  "v_exp_f32 v20, v20\n"
  "v_mfma_f32_16x16x32_bf16 a[56:59], a[220:223], v[112:115], a[56:59]\n"
  "buffer_load_dwordx4 v229, s[20:23], s40 offen offset:1024 nt lds\n"
  "v_mfma_f32_16x16x32_bf16 a[72:75], a[192:195], v[80:83], a[72:75]\n"
  "v_exp_f32 v21, v21\n"
  "v_mfma_f32_16x16x32_bf16 a[72:75], a[224:227], v[112:115], a[72:75]\n"
  "v_cvt_pk_bf16_f32 v18, v20, v21\n"
  "v_mfma_f32_16x16x32_bf16 a[88:91], a[196:199], v[80:83], a[88:91]\n"
  "v_exp_f32 v22, v22\n"
  "v_mfma_f32_16x16x32_bf16 a[88:91], a[228:231], v[112:115], a[88:91]\n"
  "buffer_load_dwordx4 v230, s[20:23], s40 offen offset:2048 nt lds\n"
  "v_mfma_f32_16x16x32_bf16 a[104:107], a[200:203], v[80:83], a[104:107]\n"
  "v_exp_f32 v23, v23\n"
  "v_mfma_f32_16x16x32_bf16 a[104:107], a[232:235], v[112:115], a[104:107]\n"
  "v_cvt_pk_bf16_f32 v19, v22, v23\n"
  "v_mfma_f32_16x16x32_bf16 a[120:123], a[204:207], v[80:83], a[120:123]\n"
  "v_exp_f32 v24, v24\n"
  "v_mfma_f32_16x16x32_bf16 a[120:123], a[236:239], v[112:115], a[120:123]\n"
  "buffer_load_dwordx4 v231, s[20:23], s40 offen offset:3072 nt lds\n"
  "v_mfma_f32_16x16x32_bf16 a[136:139], v[208:211], v[80:83], a[136:139]\n"
  "s_add_u32 s40, s40, 16384\n"
  "v_exp_f32 v25, v25\n"
  "v_mfma_f32_16x16x32_bf16 a[136:139], v[208:211], v[112:115], a[136:139]\n"
  "v_cvt_pk_bf16_f32 v24, v24, v25\n"
  "v_mfma_f32_16x16x32_bf16 a[12:15], a[176:179], v[88:91], a[12:15]\n"
  "v_exp_f32 v26, v26\n"
  "v_mfma_f32_16x16x32_bf16 a[12:15], a[208:211], v[120:123], a[12:15]\n"
  "v_mfma_f32_16x16x32_bf16 a[28:31], a[180:183], v[88:91], a[28:31]\n"
  "v_exp_f32 v27, v27\n"
  "v_mfma_f32_16x16x32_bf16 a[28:31], a[212:215], v[120:123], a[28:31]\n"
  "v_cvt_pk_bf16_f32 v25, v26, v27\n"
  "v_mfma_f32_16x16x32_bf16 a[44:47], a[184:187], v[88:91], a[44:47]\n"
  "v_exp_f32 v28, v28\n"
  "v_mfma_f32_16x16x32_bf16 a[44:47], a[216:219], v[120:123], a[44:47]\n"
  "v_mfma_f32_16x16x32_bf16 a[60:63], a[188:191], v[88:91], a[60:63]\n"
  "v_exp_f32 v29, v29\n"
  "v_mfma_f32_16x16x32_bf16 a[60:63], a[220:223], v[120:123], a[60:63]\n"
  "v_cvt_pk_bf16_f32 v26, v28, v29\n"
  "v_mfma_f32_16x16x32_bf16 a[76:79], a[192:195], v[88:91], a[76:79]\n"
  "v_exp_f32 v30, v30\n"
  "v_mfma_f32_16x16x32_bf16 a[76:79], a[224:227], v[120:123], a[76:79]\n"
  "v_mfma_f32_16x16x32_bf16 a[92:95], a[196:199], v[88:91], a[92:95]\n"
  "v_exp_f32 v31, v31\n"
  "v_mfma_f32_16x16x32_bf16 a[92:95], a[228:231], v[120:123], a[92:95]\n"
  "v_cvt_pk_bf16_f32 v27, v30, v31\n"
  "v_mfma_f32_16x16x32_bf16 a[108:111], a[200:203], v[88:91], a[108:111]\n"
  "v_exp_f32 v32, v32\n"
  "v_mfma_f32_16x16x32_bf16 a[108:111], a[232:235], v[120:123], a[108:111]\n"
  "v_mfma_f32_16x16x32_bf16 a[124:127], a[204:207], v[88:91], a[124:127]\n"
  "v_exp_f32 v33, v33\n"
  "v_mfma_f32_16x16x32_bf16 a[124:127], a[236:239], v[120:123], a[124:127]\n"
  "v_cvt_pk_bf16_f32 v32, v32, v33\n"
  "v_mfma_f32_16x16x32_bf16 a[140:143], v[208:211], v[88:91], a[140:143]\n"
  "v_exp_f32 v34, v34\n"
  "v_mfma_f32_16x16x32_bf16 a[140:143], v[208:211], v[120:123], a[140:143]\n"
  "s_waitcnt lgkmcnt(0)\n"
  "v_mfma_f32_16x16x32_bf16 v[64:67], a[144:147], v[128:131], v[192:195]\n"
  "v_exp_f32 v35, v35\n"
  "v_mfma_f32_16x16x32_bf16 v[64:67], a[148:151], v[132:135], v[64:67]\n"
  "ds_read_b64_tr_b16 a[176:177], v216 offset:0\n"
  "v_cvt_pk_bf16_f32 v33, v34, v35\n"
  "v_mfma_f32_16x16x32_bf16 v[64:67], a[152:155], v[136:139], v[64:67]\n"
  "v_exp_f32 v36, v36\n"
  "v_mfma_f32_16x16x32_bf16 v[64:67], a[156:159], v[140:143], v[64:67]\n"
  "ds_read_b64_tr_b16 a[178:179], v216 offset:4096\n"
  "ds_read_b64_tr_b16 a[208:209], v216 offset:8192\n"
  "v_mfma_f32_16x16x32_bf16 v[72:75], a[144:147], v[144:147], v[196:199]\n"
  "v_exp_f32 v37, v37\n"
  "v_mfma_f32_16x16x32_bf16 v[72:75], a[148:151], v[148:151], v[72:75]\n"
  "ds_read_b64_tr_b16 a[210:211], v216 offset:12288\n"
  "v_cvt_pk_bf16_f32 v34, v36, v37\n"
  "v_mfma_f32_16x16x32_bf16 v[72:75], a[152:155], v[152:155], v[72:75]\n"
  "v_exp_f32 v38, v38\n"
  "v_mfma_f32_16x16x32_bf16 v[72:75], a[156:159], v[156:159], v[72:75]\n"
  "ds_read_b64_tr_b16 a[180:181], v217 offset:0\n"
  "ds_read_b64_tr_b16 a[182:183], v217 offset:4096\n"
  "v_mfma_f32_16x16x32_bf16 v[80:83], a[144:147], v[160:163], v[200:203]\n"
  "v_exp_f32 v39, v39\n"
  "v_mfma_f32_16x16x32_bf16 v[80:83], a[148:151], v[164:167], v[80:83]\n"
  "ds_read_b64_tr_b16 a[212:213], v217 offset:8192\n"
  "v_cvt_pk_bf16_f32 v35, v38, v39\n"
  "v_mfma_f32_16x16x32_bf16 v[80:83], a[152:155], v[168:171], v[80:83]\n"
  "v_exp_f32 v40, v40\n"
  "v_mfma_f32_16x16x32_bf16 v[80:83], a[156:159], v[172:175], v[80:83]\n"
  "ds_read_b64_tr_b16 a[214:215], v217 offset:12288\n"
  "ds_read_b64_tr_b16 a[184:185], v218 offset:0\n"
  "v_mfma_f32_16x16x32_bf16 v[88:91], a[144:147], v[176:179], v[204:207]\n"
  "v_exp_f32 v41, v41\n"
  "v_mfma_f32_16x16x32_bf16 v[88:91], a[148:151], v[180:183], v[88:91]\n"
  "ds_read_b64_tr_b16 a[186:187], v218 offset:4096\n"
  "v_cvt_pk_bf16_f32 v40, v40, v41\n"
  "v_mfma_f32_16x16x32_bf16 v[88:91], a[152:155], v[184:187], v[88:91]\n"
  "v_exp_f32 v42, v42\n"
  "v_mfma_f32_16x16x32_bf16 v[88:91], a[156:159], v[188:191], v[88:91]\n"
  "ds_read_b64_tr_b16 a[216:217], v218 offset:8192\n"
  "ds_read_b64_tr_b16 a[218:219], v218 offset:12288\n"
  "v_mfma_f32_16x16x32_bf16 v[68:71], a[160:163], v[128:131], v[192:195]\n"
  "v_exp_f32 v43, v43\n"
  "v_mfma_f32_16x16x32_bf16 v[68:71], a[164:167], v[132:135], v[68:71]\n"
  "ds_read_b128 a[144:147], v224 offset:24576\n"
  "v_cvt_pk_bf16_f32 v41, v42, v43\n"
  "v_mfma_f32_16x16x32_bf16 v[68:71], a[168:171], v[136:139], v[68:71]\n"
  "v_exp_f32 v44, v44\n"
  "v_mfma_f32_16x16x32_bf16 v[68:71], a[172:175], v[140:143], v[68:71]\n"
  "ds_read_b128 a[148:151], v225 offset:24576\n"
  "ds_read_b128 a[152:155], v226 offset:24576\n"
  "v_mfma_f32_16x16x32_bf16 v[76:79], a[160:163], v[144:147], v[196:199]\n"
  "v_exp_f32 v45, v45\n"
  "v_mfma_f32_16x16x32_bf16 v[76:79], a[164:167], v[148:151], v[76:79]\n"
  "ds_read_b128 a[156:159], v227 offset:24576\n"
  "v_cvt_pk_bf16_f32 v42, v44, v45\n"
  "v_mfma_f32_16x16x32_bf16 v[76:79], a[168:171], v[152:155], v[76:79]\n"
  "v_exp_f32 v46, v46\n"
  "v_mfma_f32_16x16x32_bf16 v[76:79], a[172:175], v[156:159], v[76:79]\n"
  "ds_read_b64_tr_b16 a[188:189], v219 offset:0\n"
  "ds_read_b64_tr_b16 a[190:191], v219 offset:4096\n"
  "v_mfma_f32_16x16x32_bf16 v[84:87], a[160:163], v[160:163], v[200:203]\n"
  "v_exp_f32 v47, v47\n"
  "v_mfma_f32_16x16x32_bf16 v[84:87], a[164:167], v[164:167], v[84:87]\n"
  "ds_read_b64_tr_b16 a[220:221], v219 offset:8192\n"
  "v_cvt_pk_bf16_f32 v43, v46, v47\n"
  "v_mfma_f32_16x16x32_bf16 v[84:87], a[168:171], v[168:171], v[84:87]\n"
  "v_exp_f32 v48, v48\n"
  "v_mfma_f32_16x16x32_bf16 v[84:87], a[172:175], v[172:175], v[84:87]\n"
  "ds_read_b64_tr_b16 a[222:223], v219 offset:12288\n"
  "ds_read_b64_tr_b16 a[192:193], v220 offset:0\n"
  "v_mfma_f32_16x16x32_bf16 v[92:95], a[160:163], v[176:179], v[204:207]\n"
  "v_exp_f32 v49, v49\n"
  "v_mfma_f32_16x16x32_bf16 v[92:95], a[164:167], v[180:183], v[92:95]\n"
  "ds_read_b64_tr_b16 a[194:195], v220 offset:4096\n"
  "v_cvt_pk_bf16_f32 v48, v48, v49\n"
  "v_mfma_f32_16x16x32_bf16 v[92:95], a[168:171], v[184:187], v[92:95]\n"
  "v_exp_f32 v50, v50\n"
  "v_mfma_f32_16x16x32_bf16 v[92:95], a[172:175], v[188:191], v[92:95]\n"
  "s_waitcnt lgkmcnt(6)\n"
  "ds_read_b64_tr_b16 a[224:225], v220 offset:8192\n"
  "ds_read_b64_tr_b16 a[226:227], v220 offset:12288\n"
  "v_mfma_f32_16x16x32_bf16 v[96:99], a[144:147], v[128:131], v[192:195]\n"
  "v_exp_f32 v51, v51\n"
  "v_mfma_f32_16x16x32_bf16 v[96:99], a[148:151], v[132:135], v[96:99]\n"
  "ds_read_b128 a[160:163], v224 offset:28672\n"
  "v_cvt_pk_bf16_f32 v49, v50, v51\n"
  "v_mfma_f32_16x16x32_bf16 v[96:99], a[152:155], v[136:139], v[96:99]\n"
  "v_exp_f32 v52, v52\n"
  "v_mfma_f32_16x16x32_bf16 v[96:99], a[156:159], v[140:143], v[96:99]\n"
  "ds_read_b128 a[164:167], v225 offset:28672\n"
  "ds_read_b128 a[168:171], v226 offset:28672\n"
  "v_mfma_f32_16x16x32_bf16 v[104:107], a[144:147], v[144:147], v[196:199]\n"
  "v_exp_f32 v53, v53\n"
  "v_mfma_f32_16x16x32_bf16 v[104:107], a[148:151], v[148:151], v[104:107]\n"
  "ds_read_b128 a[172:175], v227 offset:28672\n"
  "v_cvt_pk_bf16_f32 v50, v52, v53\n"
  "v_mfma_f32_16x16x32_bf16 v[104:107], a[152:155], v[152:155], v[104:107]\n"
  "v_exp_f32 v54, v54\n"
  "v_mfma_f32_16x16x32_bf16 v[104:107], a[156:159], v[156:159], v[104:107]\n"
  "ds_read_b64_tr_b16 a[196:197], v221 offset:0\n"
  "ds_read_b64_tr_b16 a[198:199], v221 offset:4096\n"
  "v_mfma_f32_16x16x32_bf16 v[112:115], a[144:147], v[160:163], v[200:203]\n"
  "v_exp_f32 v55, v55\n"
  "v_mfma_f32_16x16x32_bf16 v[112:115], a[148:151], v[164:167], v[112:115]\n"
  "ds_read_b64_tr_b16 a[228:229], v221 offset:8192\n"
  "v_cvt_pk_bf16_f32 v51, v54, v55\n"
  "v_mfma_f32_16x16x32_bf16 v[112:115], a[152:155], v[168:171], v[112:115]\n"
  "v_exp_f32 v56, v56\n"
  "v_mfma_f32_16x16x32_bf16 v[112:115], a[156:159], v[172:175], v[112:115]\n"
  "ds_read_b64_tr_b16 a[230:231], v221 offset:12288\n"
  "ds_read_b64_tr_b16 a[200:201], v222 offset:0\n"
  "v_mfma_f32_16x16x32_bf16 v[120:123], a[144:147], v[176:179], v[204:207]\n"
  "v_exp_f32 v57, v57\n"
  "v_mfma_f32_16x16x32_bf16 v[120:123], a[148:151], v[180:183], v[120:123]\n"
  "ds_read_b64_tr_b16 a[202:203], v222 offset:4096\n"
  "v_cvt_pk_bf16_f32 v56, v56, v57\n"
  "v_mfma_f32_16x16x32_bf16 v[120:123], a[152:155], v[184:187], v[120:123]\n"
  "v_exp_f32 v58, v58\n"
  "v_mfma_f32_16x16x32_bf16 v[120:123], a[156:159], v[188:191], v[120:123]\n"
  "s_waitcnt lgkmcnt(6)\n"
  "ds_read_b64_tr_b16 a[232:233], v222 offset:8192\n"
  "ds_read_b64_tr_b16 a[234:235], v222 offset:12288\n"
  "v_mfma_f32_16x16x32_bf16 v[100:103], a[160:163], v[128:131], v[192:195]\n"
  "v_exp_f32 v59, v59\n"
  "v_mfma_f32_16x16x32_bf16 v[100:103], a[164:167], v[132:135], v[100:103]\n"
  "v_cvt_pk_bf16_f32 v57, v58, v59\n"
  "v_mfma_f32_16x16x32_bf16 v[100:103], a[168:171], v[136:139], v[100:103]\n"
  "v_exp_f32 v60, v60\n"
  "v_mfma_f32_16x16x32_bf16 v[100:103], a[172:175], v[140:143], v[100:103]\n"
  "ds_read_b64_tr_b16 a[204:205], v223 offset:0\n"
  "ds_read_b64_tr_b16 a[206:207], v223 offset:4096\n"
  "v_mfma_f32_16x16x32_bf16 v[108:111], a[160:163], v[144:147], v[196:199]\n"
  "v_exp_f32 v61, v61\n"
  "v_mfma_f32_16x16x32_bf16 v[108:111], a[164:167], v[148:151], v[108:111]\n"
  "v_cvt_pk_bf16_f32 v58, v60, v61\n"
  "v_mfma_f32_16x16x32_bf16 v[108:111], a[168:171], v[152:155], v[108:111]\n"
  "v_exp_f32 v62, v62\n"
  "v_mfma_f32_16x16x32_bf16 v[108:111], a[172:175], v[156:159], v[108:111]\n"
  "ds_read_b64_tr_b16 a[236:237], v223 offset:8192\n"
  "ds_read_b64_tr_b16 a[238:239], v223 offset:12288\n"
  "v_mfma_f32_16x16x32_bf16 v[116:119], a[160:163], v[160:163], v[200:203]\n"
  "v_exp_f32 v63, v63\n"
  "v_mfma_f32_16x16x32_bf16 v[116:119], a[164:167], v[164:167], v[116:119]\n"
  "v_cvt_pk_bf16_f32 v59, v62, v63\n"
  "v_mfma_f32_16x16x32_bf16 v[116:119], a[168:171], v[168:171], v[116:119]\n"
  "v_mfma_f32_16x16x32_bf16 v[116:119], a[172:175], v[172:175], v[116:119]\n"
  "v_mfma_f32_16x16x32_bf16 v[124:127], a[160:163], v[176:179], v[204:207]\n"
  "v_mfma_f32_16x16x32_bf16 v[124:127], a[164:167], v[180:183], v[124:127]\n"
  "v_mfma_f32_16x16x32_bf16 v[124:127], a[168:171], v[184:187], v[124:127]\n"
  "v_mfma_f32_16x16x32_bf16 v[124:127], a[172:175], v[188:191], v[124:127]\n"
  "s_waitcnt vmcnt(4) lgkmcnt(0)\n"
  "s_barrier\n"
  "v_mfma_f32_16x16x32_bf16 a[0:3], a[176:179], v[0:3], a[0:3]\n"
  "s_add_u32 m0, s42, 98304\n"
  "v_mfma_f32_16x16x32_bf16 a[0:3], a[208:211], v[32:35], a[0:3]\n"
  "buffer_load_dwordx4 v232, s[24:27], s41 offen offset:0 nt lds\n"
  "v_mfma_f32_16x16x32_bf16 a[16:19], a[180:183], v[0:3], a[16:19]\n"
  "v_exp_f32 v64, v64\n"
  "v_mfma_f32_16x16x32_bf16 a[16:19], a[212:215], v[32:35], a[16:19]\n"
  "buffer_load_dwordx4 v233, s[24:27], s41 offen offset:1024 nt lds\n"
  "v_mfma_f32_16x16x32_bf16 a[32:35], a[184:187], v[0:3], a[32:35]\n"
  "v_exp_f32 v65, v65\n"
  "v_mfma_f32_16x16x32_bf16 a[32:35], a[216:219], v[32:35], a[32:35]\n"
  "v_cvt_pk_bf16_f32 v64, v64, v65\n"
  "v_mfma_f32_16x16x32_bf16 a[48:51], a[188:191], v[0:3], a[48:51]\n"
  "v_exp_f32 v66, v66\n"
  "v_mfma_f32_16x16x32_bf16 a[48:51], a[220:223], v[32:35], a[48:51]\n"
  "ds_read_b128 a[144:147], v224 offset:32768\n"
  "v_mfma_f32_16x16x32_bf16 a[64:67], a[192:195], v[0:3], a[64:67]\n"
  "v_exp_f32 v67, v67\n"
  "v_mfma_f32_16x16x32_bf16 a[64:67], a[224:227], v[32:35], a[64:67]\n"
  "ds_read_b128 a[148:151], v225 offset:32768\n"
  "v_cvt_pk_bf16_f32 v65, v66, v67\n"
  "v_mfma_f32_16x16x32_bf16 a[80:83], a[196:199], v[0:3], a[80:83]\n"
  "v_exp_f32 v68, v68\n"
  "v_mfma_f32_16x16x32_bf16 a[80:83], a[228:231], v[32:35], a[80:83]\n"
  "ds_read_b128 a[152:155], v226 offset:32768\n"
  "v_mfma_f32_16x16x32_bf16 a[96:99], a[200:203], v[0:3], a[96:99]\n"
  "v_exp_f32 v69, v69\n"
  "v_mfma_f32_16x16x32_bf16 a[96:99], a[232:235], v[32:35], a[96:99]\n"
  "ds_read_b128 a[156:159], v227 offset:32768\n"
  "v_cvt_pk_bf16_f32 v66, v68, v69\n"
  "v_mfma_f32_16x16x32_bf16 a[112:115], a[204:207], v[0:3], a[112:115]\n"
  "v_exp_f32 v70, v70\n"
  "v_mfma_f32_16x16x32_bf16 a[112:115], a[236:239], v[32:35], a[112:115]\n"
  "ds_read_b128 a[160:163], v224 offset:36864\n"
  "v_mfma_f32_16x16x32_bf16 a[128:131], v[208:211], v[0:3], a[128:131]\n"
  "v_exp_f32 v71, v71\n"
  "v_mfma_f32_16x16x32_bf16 a[128:131], v[208:211], v[32:35], a[128:131]\n"
  "ds_read_b128 a[164:167], v225 offset:36864\n"
  "v_cvt_pk_bf16_f32 v67, v70, v71\n"
  "v_mfma_f32_16x16x32_bf16 a[4:7], a[176:179], v[8:11], a[4:7]\n"
  "v_exp_f32 v72, v72\n"
  "v_mfma_f32_16x16x32_bf16 a[4:7], a[208:211], v[40:43], a[4:7]\n"
  "ds_read_b128 a[168:171], v226 offset:36864\n"
  "v_mfma_f32_16x16x32_bf16 a[20:23], a[180:183], v[8:11], a[20:23]\n"
  "v_exp_f32 v73, v73\n"
  "v_mfma_f32_16x16x32_bf16 a[20:23], a[212:215], v[40:43], a[20:23]\n"
  "ds_read_b128 a[172:175], v227 offset:36864\n"
  "v_cvt_pk_bf16_f32 v72, v72, v73\n"
  "v_mfma_f32_16x16x32_bf16 a[36:39], a[184:187], v[8:11], a[36:39]\n"
  "v_exp_f32 v74, v74\n"
  "v_mfma_f32_16x16x32_bf16 a[36:39], a[216:219], v[40:43], a[36:39]\n"
  "buffer_load_dwordx4 v234, s[24:27], s41 offen offset:2048 nt lds\n"
  "v_mfma_f32_16x16x32_bf16 a[52:55], a[188:191], v[8:11], a[52:55]\n"
  "v_exp_f32 v75, v75\n"
  "v_mfma_f32_16x16x32_bf16 a[52:55], a[220:223], v[40:43], a[52:55]\n"
  "v_cvt_pk_bf16_f32 v73, v74, v75\n"
  "v_mfma_f32_16x16x32_bf16 a[68:71], a[192:195], v[8:11], a[68:71]\n"
  "v_exp_f32 v76, v76\n"
  "v_mfma_f32_16x16x32_bf16 a[68:71], a[224:227], v[40:43], a[68:71]\n"
  "buffer_load_dwordx4 v235, s[24:27], s41 offen offset:3072 nt lds\n"
  "v_mfma_f32_16x16x32_bf16 a[84:87], a[196:199], v[8:11], a[84:87]\n"
  "s_add_u32 s41, s41, 16384\n"
  "v_exp_f32 v77, v77\n"
  "v_mfma_f32_16x16x32_bf16 a[84:87], a[228:231], v[40:43], a[84:87]\n"
  "v_cvt_pk_bf16_f32 v74, v76, v77\n"
  "v_mfma_f32_16x16x32_bf16 a[100:103], a[200:203], v[8:11], a[100:103]\n"
  "v_exp_f32 v78, v78\n"
  "v_mfma_f32_16x16x32_bf16 a[100:103], a[232:235], v[40:43], a[100:103]\n"
  "v_mfma_f32_16x16x32_bf16 a[116:119], a[204:207], v[8:11], a[116:119]\n"
  "v_exp_f32 v79, v79\n"
  "v_mfma_f32_16x16x32_bf16 a[116:119], a[236:239], v[40:43], a[116:119]\n"
  "v_cvt_pk_bf16_f32 v75, v78, v79\n"
  "v_mfma_f32_16x16x32_bf16 a[132:135], v[208:211], v[8:11], a[132:135]\n"
  "v_exp_f32 v80, v80\n"
  "v_mfma_f32_16x16x32_bf16 a[132:135], v[208:211], v[40:43], a[132:135]\n"
  "v_mfma_f32_16x16x32_bf16 a[8:11], a[176:179], v[16:19], a[8:11]\n"
  "v_exp_f32 v81, v81\n"
  "v_mfma_f32_16x16x32_bf16 a[8:11], a[208:211], v[48:51], a[8:11]\n"
  "v_cvt_pk_bf16_f32 v80, v80, v81\n"
  "v_mfma_f32_16x16x32_bf16 a[24:27], a[180:183], v[16:19], a[24:27]\n"
  "s_add_u32 m0, s42, 0\n"
  "v_exp_f32 v82, v82\n"
  "v_mfma_f32_16x16x32_bf16 a[24:27], a[212:215], v[48:51], a[24:27]\n"
  "buffer_load_dwordx4 v228, s[20:23], s40 offen offset:0 nt lds\n"
  "v_mfma_f32_16x16x32_bf16 a[40:43], a[184:187], v[16:19], a[40:43]\n"
  "v_exp_f32 v83, v83\n"
  "v_mfma_f32_16x16x32_bf16 a[40:43], a[216:219], v[48:51], a[40:43]\n"
  "v_cvt_pk_bf16_f32 v81, v82, v83\n"
  "v_mfma_f32_16x16x32_bf16 a[56:59], a[188:191], v[16:19], a[56:59]\n"
  "v_exp_f32 v84, v84\n"
  "v_mfma_f32_16x16x32_bf16 a[56:59], a[220:223], v[48:51], a[56:59]\n"
  "buffer_load_dwordx4 v229, s[20:23], s40 offen offset:1024 nt lds\n"
  "v_mfma_f32_16x16x32_bf16 a[72:75], a[192:195], v[16:19], a[72:75]\n"
  "v_exp_f32 v85, v85\n"
  "v_mfma_f32_16x16x32_bf16 a[72:75], a[224:227], v[48:51], a[72:75]\n"
  "v_cvt_pk_bf16_f32 v82, v84, v85\n"
  "v_mfma_f32_16x16x32_bf16 a[88:91], a[196:199], v[16:19], a[88:91]\n"
  "v_exp_f32 v86, v86\n"
  "v_mfma_f32_16x16x32_bf16 a[88:91], a[228:231], v[48:51], a[88:91]\n"
  "buffer_load_dwordx4 v230, s[20:23], s40 offen offset:2048 nt lds\n"
  "v_mfma_f32_16x16x32_bf16 a[104:107], a[200:203], v[16:19], a[104:107]\n"
  "v_exp_f32 v87, v87\n"
  "v_mfma_f32_16x16x32_bf16 a[104:107], a[232:235], v[48:51], a[104:107]\n"
  "v_cvt_pk_bf16_f32 v83, v86, v87\n"
  "v_mfma_f32_16x16x32_bf16 a[120:123], a[204:207], v[16:19], a[120:123]\n"
  "v_exp_f32 v88, v88\n"
  "v_mfma_f32_16x16x32_bf16 a[120:123], a[236:239], v[48:51], a[120:123]\n"
  "buffer_load_dwordx4 v231, s[20:23], s40 offen offset:3072 nt lds\n"
  "v_mfma_f32_16x16x32_bf16 a[136:139], v[208:211], v[16:19], a[136:139]\n"
  "s_add_u32 s40, s40, 16384\n"
  "v_exp_f32 v89, v89\n"
  "v_mfma_f32_16x16x32_bf16 a[136:139], v[208:211], v[48:51], a[136:139]\n"
  "v_cvt_pk_bf16_f32 v88, v88, v89\n"
  "v_mfma_f32_16x16x32_bf16 a[12:15], a[176:179], v[24:27], a[12:15]\n"
  "v_exp_f32 v90, v90\n"
  "v_mfma_f32_16x16x32_bf16 a[12:15], a[208:211], v[56:59], a[12:15]\n"
  "v_mfma_f32_16x16x32_bf16 a[28:31], a[180:183], v[24:27], a[28:31]\n"
  "v_exp_f32 v91, v91\n"
  "v_mfma_f32_16x16x32_bf16 a[28:31], a[212:215], v[56:59], a[28:31]\n"
  "v_cvt_pk_bf16_f32 v89, v90, v91\n"
  "v_mfma_f32_16x16x32_bf16 a[44:47], a[184:187], v[24:27], a[44:47]\n"
  "v_exp_f32 v92, v92\n"
  "v_mfma_f32_16x16x32_bf16 a[44:47], a[216:219], v[56:59], a[44:47]\n"
  "v_mfma_f32_16x16x32_bf16 a[60:63], a[188:191], v[24:27], a[60:63]\n"
  "v_exp_f32 v93, v93\n"
  "v_mfma_f32_16x16x32_bf16 a[60:63], a[220:223], v[56:59], a[60:63]\n"
  "v_cvt_pk_bf16_f32 v90, v92, v93\n"
  "v_mfma_f32_16x16x32_bf16 a[76:79], a[192:195], v[24:27], a[76:79]\n"
  "v_exp_f32 v94, v94\n"
  "v_mfma_f32_16x16x32_bf16 a[76:79], a[224:227], v[56:59], a[76:79]\n"
  "v_mfma_f32_16x16x32_bf16 a[92:95], a[196:199], v[24:27], a[92:95]\n"
  "v_exp_f32 v95, v95\n"
  "v_mfma_f32_16x16x32_bf16 a[92:95], a[228:231], v[56:59], a[92:95]\n"
  "v_cvt_pk_bf16_f32 v91, v94, v95\n"
  "v_mfma_f32_16x16x32_bf16 a[108:111], a[200:203], v[24:27], a[108:111]\n"
  "v_exp_f32 v96, v96\n"
  "v_mfma_f32_16x16x32_bf16 a[108:111], a[232:235], v[56:59], a[108:111]\n"
  "v_mfma_f32_16x16x32_bf16 a[124:127], a[204:207], v[24:27], a[124:127]\n"
  "v_exp_f32 v97, v97\n"
  "v_mfma_f32_16x16x32_bf16 a[124:127], a[236:239], v[56:59], a[124:127]\n"
  "v_cvt_pk_bf16_f32 v96, v96, v97\n"
  "v_mfma_f32_16x16x32_bf16 a[140:143], v[208:211], v[24:27], a[140:143]\n"
  "v_exp_f32 v98, v98\n"
  "v_mfma_f32_16x16x32_bf16 a[140:143], v[208:211], v[56:59], a[140:143]\n"
  "s_waitcnt lgkmcnt(0)\n"
  "v_mfma_f32_16x16x32_bf16 v[0:3], a[144:147], v[128:131], v[192:195]\n"
  "v_exp_f32 v99, v99\n"
  "v_mfma_f32_16x16x32_bf16 v[0:3], a[148:151], v[132:135], v[0:3]\n"
  "ds_read_b64_tr_b16 a[176:177], v216 offset:16384\n"
  "v_cvt_pk_bf16_f32 v97, v98, v99\n"
  "v_mfma_f32_16x16x32_bf16 v[0:3], a[152:155], v[136:139], v[0:3]\n"
  "v_exp_f32 v100, v100\n"
  "v_mfma_f32_16x16x32_bf16 v[0:3], a[156:159], v[140:143], v[0:3]\n"
  "ds_read_b64_tr_b16 a[178:179], v216 offset:20480\n"
  "ds_read_b64_tr_b16 a[208:209], v216 offset:24576\n"
  "v_mfma_f32_16x16x32_bf16 v[8:11], a[144:147], v[144:147], v[196:199]\n"
  "v_exp_f32 v101, v101\n"
  "v_mfma_f32_16x16x32_bf16 v[8:11], a[148:151], v[148:151], v[8:11]\n"
  "ds_read_b64_tr_b16 a[210:211], v216 offset:28672\n"
  "v_cvt_pk_bf16_f32 v98, v100, v101\n"
  "v_mfma_f32_16x16x32_bf16 v[8:11], a[152:155], v[152:155], v[8:11]\n"
  "v_exp_f32 v102, v102\n"
  "v_mfma_f32_16x16x32_bf16 v[8:11], a[156:159], v[156:159], v[8:11]\n"
  "ds_read_b64_tr_b16 a[180:181], v217 offset:16384\n"
  "ds_read_b64_tr_b16 a[182:183], v217 offset:20480\n"
  "v_mfma_f32_16x16x32_bf16 v[16:19], a[144:147], v[160:163], v[200:203]\n"
  "v_exp_f32 v103, v103\n"
  "v_mfma_f32_16x16x32_bf16 v[16:19], a[148:151], v[164:167], v[16:19]\n"
  "ds_read_b64_tr_b16 a[212:213], v217 offset:24576\n"
  "v_cvt_pk_bf16_f32 v99, v102, v103\n"
  "v_mfma_f32_16x16x32_bf16 v[16:19], a[152:155], v[168:171], v[16:19]\n"
  "v_exp_f32 v104, v104\n"
  "v_mfma_f32_16x16x32_bf16 v[16:19], a[156:159], v[172:175], v[16:19]\n"
  "ds_read_b64_tr_b16 a[214:215], v217 offset:28672\n"
  "ds_read_b64_tr_b16 a[184:185], v218 offset:16384\n"
  "v_mfma_f32_16x16x32_bf16 v[24:27], a[144:147], v[176:179], v[204:207]\n"
  "v_exp_f32 v105, v105\n"
  "v_mfma_f32_16x16x32_bf16 v[24:27], a[148:151], v[180:183], v[24:27]\n"
  "ds_read_b64_tr_b16 a[186:187], v218 offset:20480\n"
  "v_cvt_pk_bf16_f32 v104, v104, v105\n"
  "v_mfma_f32_16x16x32_bf16 v[24:27], a[152:155], v[184:187], v[24:27]\n"
  "v_exp_f32 v106, v106\n"
  "v_mfma_f32_16x16x32_bf16 v[24:27], a[156:159], v[188:191], v[24:27]\n"
  "ds_read_b64_tr_b16 a[216:217], v218 offset:24576\n"
  "ds_read_b64_tr_b16 a[218:219], v218 offset:28672\n"
  "v_mfma_f32_16x16x32_bf16 v[4:7], a[160:163], v[128:131], v[192:195]\n"
  "v_exp_f32 v107, v107\n"
  "v_mfma_f32_16x16x32_bf16 v[4:7], a[164:167], v[132:135], v[4:7]\n"
  "ds_read_b128 a[144:147], v224 offset:40960\n"
  "v_cvt_pk_bf16_f32 v105, v106, v107\n"
  "v_mfma_f32_16x16x32_bf16 v[4:7], a[168:171], v[136:139], v[4:7]\n"
  "v_exp_f32 v108, v108\n"
  "v_mfma_f32_16x16x32_bf16 v[4:7], a[172:175], v[140:143], v[4:7]\n"
  "ds_read_b128 a[148:151], v225 offset:40960\n"
  "ds_read_b128 a[152:155], v226 offset:40960\n"
  "v_mfma_f32_16x16x32_bf16 v[12:15], a[160:163], v[144:147], v[196:199]\n"
  "v_exp_f32 v109, v109\n"
  "v_mfma_f32_16x16x32_bf16 v[12:15], a[164:167], v[148:151], v[12:15]\n"
  "ds_read_b128 a[156:159], v227 offset:40960\n"
  "v_cvt_pk_bf16_f32 v106, v108, v109\n"
  "v_mfma_f32_16x16x32_bf16 v[12:15], a[168:171], v[152:155], v[12:15]\n"
  "v_exp_f32 v110, v110\n"
  "v_mfma_f32_16x16x32_bf16 v[12:15], a[172:175], v[156:159], v[12:15]\n"
  "ds_read_b64_tr_b16 a[188:189], v219 offset:16384\n"
  "ds_read_b64_tr_b16 a[190:191], v219 offset:20480\n"
  "v_mfma_f32_16x16x32_bf16 v[20:23], a[160:163], v[160:163], v[200:203]\n"
  "v_exp_f32 v111, v111\n"
  "v_mfma_f32_16x16x32_bf16 v[20:23], a[164:167], v[164:167], v[20:23]\n"
  "ds_read_b64_tr_b16 a[220:221], v219 offset:24576\n"
  "v_cvt_pk_bf16_f32 v107, v110, v111\n"
  "v_mfma_f32_16x16x32_bf16 v[20:23], a[168:171], v[168:171], v[20:23]\n"
  "v_exp_f32 v112, v112\n"
  "v_mfma_f32_16x16x32_bf16 v[20:23], a[172:175], v[172:175], v[20:23]\n"
  "ds_read_b64_tr_b16 a[222:223], v219 offset:28672\n"
  "ds_read_b64_tr_b16 a[192:193], v220 offset:16384\n"
  "v_mfma_f32_16x16x32_bf16 v[28:31], a[160:163], v[176:179], v[204:207]\n"
  "v_exp_f32 v113, v113\n"
  "v_mfma_f32_16x16x32_bf16 v[28:31], a[164:167], v[180:183], v[28:31]\n"
  "ds_read_b64_tr_b16 a[194:195], v220 offset:20480\n"
  "v_cvt_pk_bf16_f32 v112, v112, v113\n"
  "v_mfma_f32_16x16x32_bf16 v[28:31], a[168:171], v[184:187], v[28:31]\n"
  "v_exp_f32 v114, v114\n"
  "v_mfma_f32_16x16x32_bf16 v[28:31], a[172:175], v[188:191], v[28:31]\n"
  "s_waitcnt lgkmcnt(6)\n"
  "ds_read_b64_tr_b16 a[224:225], v220 offset:24576\n"
  "ds_read_b64_tr_b16 a[226:227], v220 offset:28672\n"
  "v_mfma_f32_16x16x32_bf16 v[32:35], a[144:147], v[128:131], v[192:195]\n"
  "v_exp_f32 v115, v115\n"
  "v_mfma_f32_16x16x32_bf16 v[32:35], a[148:151], v[132:135], v[32:35]\n"
  "ds_read_b128 a[160:163], v224 offset:45056\n"
  "v_cvt_pk_bf16_f32 v113, v114, v115\n"
  "v_mfma_f32_16x16x32_bf16 v[32:35], a[152:155], v[136:139], v[32:35]\n"
  "v_exp_f32 v116, v116\n"
  "v_mfma_f32_16x16x32_bf16 v[32:35], a[156:159], v[140:143], v[32:35]\n"
  "ds_read_b128 a[164:167], v225 offset:45056\n"
  "ds_read_b128 a[168:171], v226 offset:45056\n"
  "v_mfma_f32_16x16x32_bf16 v[40:43], a[144:147], v[144:147], v[196:199]\n"
  "v_exp_f32 v117, v117\n"
  "v_mfma_f32_16x16x32_bf16 v[40:43], a[148:151], v[148:151], v[40:43]\n"
  "ds_read_b128 a[172:175], v227 offset:45056\n"
  "v_cvt_pk_bf16_f32 v114, v116, v117\n"
  "v_mfma_f32_16x16x32_bf16 v[40:43], a[152:155], v[152:155], v[40:43]\n"
  "v_exp_f32 v118, v118\n"
  "v_mfma_f32_16x16x32_bf16 v[40:43], a[156:159], v[156:159], v[40:43]\n"
  "ds_read_b64_tr_b16 a[196:197], v221 offset:16384\n"
  "ds_read_b64_tr_b16 a[198:199], v221 offset:20480\n"
  "v_mfma_f32_16x16x32_bf16 v[48:51], a[144:147], v[160:163], v[200:203]\n"
  "v_exp_f32 v119, v119\n"
  "v_mfma_f32_16x16x32_bf16 v[48:51], a[148:151], v[164:167], v[48:51]\n"
  "ds_read_b64_tr_b16 a[228:229], v221 offset:24576\n"
  "v_cvt_pk_bf16_f32 v115, v118, v119\n"
  "v_mfma_f32_16x16x32_bf16 v[48:51], a[152:155], v[168:171], v[48:51]\n"
  "v_exp_f32 v120, v120\n"
  "v_mfma_f32_16x16x32_bf16 v[48:51], a[156:159], v[172:175], v[48:51]\n"
  "ds_read_b64_tr_b16 a[230:231], v221 offset:28672\n"
  "ds_read_b64_tr_b16 a[200:201], v222 offset:16384\n"
  "v_mfma_f32_16x16x32_bf16 v[56:59], a[144:147], v[176:179], v[204:207]\n"
  "v_exp_f32 v121, v121\n"
  "v_mfma_f32_16x16x32_bf16 v[56:59], a[148:151], v[180:183], v[56:59]\n"
  "ds_read_b64_tr_b16 a[202:203], v222 offset:20480\n"
  "v_cvt_pk_bf16_f32 v120, v120, v121\n"
  "v_mfma_f32_16x16x32_bf16 v[56:59], a[152:155], v[184:187], v[56:59]\n"
  "v_exp_f32 v122, v122\n"
  "v_mfma_f32_16x16x32_bf16 v[56:59], a[156:159], v[188:191], v[56:59]\n"
  "s_waitcnt lgkmcnt(6)\n"
  "ds_read_b64_tr_b16 a[232:233], v222 offset:24576\n"
  "ds_read_b64_tr_b16 a[234:235], v222 offset:28672\n"
  "v_mfma_f32_16x16x32_bf16 v[36:39], a[160:163], v[128:131], v[192:195]\n"
  "v_exp_f32 v123, v123\n"
  "v_mfma_f32_16x16x32_bf16 v[36:39], a[164:167], v[132:135], v[36:39]\n"
  "v_cvt_pk_bf16_f32 v121, v122, v123\n"
  "v_mfma_f32_16x16x32_bf16 v[36:39], a[168:171], v[136:139], v[36:39]\n"
  "v_exp_f32 v124, v124\n"
  "v_mfma_f32_16x16x32_bf16 v[36:39], a[172:175], v[140:143], v[36:39]\n"
  "ds_read_b64_tr_b16 a[204:205], v223 offset:16384\n"
  "ds_read_b64_tr_b16 a[206:207], v223 offset:20480\n"
  "v_mfma_f32_16x16x32_bf16 v[44:47], a[160:163], v[144:147], v[196:199]\n"
  "v_exp_f32 v125, v125\n"
  "v_mfma_f32_16x16x32_bf16 v[44:47], a[164:167], v[148:151], v[44:47]\n"
  "v_cvt_pk_bf16_f32 v122, v124, v125\n"
  "v_mfma_f32_16x16x32_bf16 v[44:47], a[168:171], v[152:155], v[44:47]\n"
  "v_exp_f32 v126, v126\n"
  "v_mfma_f32_16x16x32_bf16 v[44:47], a[172:175], v[156:159], v[44:47]\n"
  "ds_read_b64_tr_b16 a[236:237], v223 offset:24576\n"
  "ds_read_b64_tr_b16 a[238:239], v223 offset:28672\n"
  "v_mfma_f32_16x16x32_bf16 v[52:55], a[160:163], v[160:163], v[200:203]\n"
  "v_exp_f32 v127, v127\n"
  "v_mfma_f32_16x16x32_bf16 v[52:55], a[164:167], v[164:167], v[52:55]\n"
  "v_cvt_pk_bf16_f32 v123, v126, v127\n"
  "v_mfma_f32_16x16x32_bf16 v[52:55], a[168:171], v[168:171], v[52:55]\n"
  "v_mfma_f32_16x16x32_bf16 v[52:55], a[172:175], v[172:175], v[52:55]\n"
  "v_mfma_f32_16x16x32_bf16 v[60:63], a[160:163], v[176:179], v[204:207]\n"
  "v_mfma_f32_16x16x32_bf16 v[60:63], a[164:167], v[180:183], v[60:63]\n"
  "v_mfma_f32_16x16x32_bf16 v[60:63], a[168:171], v[184:187], v[60:63]\n"
  "v_mfma_f32_16x16x32_bf16 v[60:63], a[172:175], v[188:191], v[60:63]\n"
  "s_waitcnt vmcnt(4) lgkmcnt(0)\n"
  "s_barrier\n"
  "s_sub_u32 s44, s44, 1\n"
  "s_cmp_lg_u32 s44, 0\n"
  "s_cbranch_scc1 L_loop%=\n"
  "v_mfma_f32_16x16x32_bf16 a[0:3], a[176:179], v[64:67], a[0:3]\n"
  "s_add_u32 m0, s42, 114688\n"
  "v_mfma_f32_16x16x32_bf16 a[0:3], a[208:211], v[96:99], a[0:3]\n"
  "buffer_load_dwordx4 v232, s[24:27], s41 offen offset:0 nt lds\n"
  "v_mfma_f32_16x16x32_bf16 a[16:19], a[180:183], v[64:67], a[16:19]\n"
  "v_exp_f32 v0, v0\n"
  "v_mfma_f32_16x16x32_bf16 a[16:19], a[212:215], v[96:99], a[16:19]\n"
  "buffer_load_dwordx4 v233, s[24:27], s41 offen offset:1024 nt lds\n"
  "v_mfma_f32_16x16x32_bf16 a[32:35], a[184:187], v[64:67], a[32:35]\n"
  "v_exp_f32 v1, v1\n"
  "v_mfma_f32_16x16x32_bf16 a[32:35], a[216:219], v[96:99], a[32:35]\n"
  "v_cvt_pk_bf16_f32 v0, v0, v1\n"
  "v_mfma_f32_16x16x32_bf16 a[48:51], a[188:191], v[64:67], a[48:51]\n"
  "v_exp_f32 v2, v2\n"
  "v_mfma_f32_16x16x32_bf16 a[48:51], a[220:223], v[96:99], a[48:51]\n"
  "ds_read_b128 a[144:147], v224 offset:49152\n"
  "v_mfma_f32_16x16x32_bf16 a[64:67], a[192:195], v[64:67], a[64:67]\n"
  "v_exp_f32 v3, v3\n"
  "v_mfma_f32_16x16x32_bf16 a[64:67], a[224:227], v[96:99], a[64:67]\n"
  "ds_read_b128 a[148:151], v225 offset:49152\n"
  "v_cvt_pk_bf16_f32 v1, v2, v3\n"
  "v_mfma_f32_16x16x32_bf16 a[80:83], a[196:199], v[64:67], a[80:83]\n"
  "v_exp_f32 v4, v4\n"
  "v_mfma_f32_16x16x32_bf16 a[80:83], a[228:231], v[96:99], a[80:83]\n"
  "ds_read_b128 a[152:155], v226 offset:49152\n"
  "v_mfma_f32_16x16x32_bf16 a[96:99], a[200:203], v[64:67], a[96:99]\n"
  "v_exp_f32 v5, v5\n"
  "v_mfma_f32_16x16x32_bf16 a[96:99], a[232:235], v[96:99], a[96:99]\n"
  "ds_read_b128 a[156:159], v227 offset:49152\n"
  "v_cvt_pk_bf16_f32 v2, v4, v5\n"
  "v_mfma_f32_16x16x32_bf16 a[112:115], a[204:207], v[64:67], a[112:115]\n"
  "v_exp_f32 v6, v6\n"
  "v_mfma_f32_16x16x32_bf16 a[112:115], a[236:239], v[96:99], a[112:115]\n"
  "ds_read_b128 a[160:163], v224 offset:53248\n"
  "v_mfma_f32_16x16x32_bf16 a[128:131], v[208:211], v[64:67], a[128:131]\n"
  "v_exp_f32 v7, v7\n"
  "v_mfma_f32_16x16x32_bf16 a[128:131], v[208:211], v[96:99], a[128:131]\n"
  "ds_read_b128 a[164:167], v225 offset:53248\n"
  "v_cvt_pk_bf16_f32 v3, v6, v7\n"
  "v_mfma_f32_16x16x32_bf16 a[4:7], a[176:179], v[72:75], a[4:7]\n"
  "v_exp_f32 v8, v8\n"
  "v_mfma_f32_16x16x32_bf16 a[4:7], a[208:211], v[104:107], a[4:7]\n"
  "ds_read_b128 a[168:171], v226 offset:53248\n"
  "v_mfma_f32_16x16x32_bf16 a[20:23], a[180:183], v[72:75], a[20:23]\n"
  "v_exp_f32 v9, v9\n"
  "v_mfma_f32_16x16x32_bf16 a[20:23], a[212:215], v[104:107], a[20:23]\n"
  "ds_read_b128 a[172:175], v227 offset:53248\n"
  "v_cvt_pk_bf16_f32 v8, v8, v9\n"
  "v_mfma_f32_16x16x32_bf16 a[36:39], a[184:187], v[72:75], a[36:39]\n"
  "v_exp_f32 v10, v10\n"
  "v_mfma_f32_16x16x32_bf16 a[36:39], a[216:219], v[104:107], a[36:39]\n"
  "buffer_load_dwordx4 v234, s[24:27], s41 offen offset:2048 nt lds\n"
  "v_mfma_f32_16x16x32_bf16 a[52:55], a[188:191], v[72:75], a[52:55]\n"
  "v_exp_f32 v11, v11\n"
  "v_mfma_f32_16x16x32_bf16 a[52:55], a[220:223], v[104:107], a[52:55]\n"
  "v_cvt_pk_bf16_f32 v9, v10, v11\n"
  "v_mfma_f32_16x16x32_bf16 a[68:71], a[192:195], v[72:75], a[68:71]\n"
  "v_exp_f32 v12, v12\n"
  "v_mfma_f32_16x16x32_bf16 a[68:71], a[224:227], v[104:107], a[68:71]\n"
  "buffer_load_dwordx4 v235, s[24:27], s41 offen offset:3072 nt lds\n"
  "v_mfma_f32_16x16x32_bf16 a[84:87], a[196:199], v[72:75], a[84:87]\n"
  "s_add_u32 s41, s41, 16384\n"
  "v_exp_f32 v13, v13\n"
  "v_mfma_f32_16x16x32_bf16 a[84:87], a[228:231], v[104:107], a[84:87]\n"
  "v_cvt_pk_bf16_f32 v10, v12, v13\n"
  "v_mfma_f32_16x16x32_bf16 a[100:103], a[200:203], v[72:75], a[100:103]\n"
  "v_exp_f32 v14, v14\n"
  "v_mfma_f32_16x16x32_bf16 a[100:103], a[232:235], v[104:107], a[100:103]\n"
  "v_mfma_f32_16x16x32_bf16 a[116:119], a[204:207], v[72:75], a[116:119]\n"
  "v_exp_f32 v15, v15\n"
  "v_mfma_f32_16x16x32_bf16 a[116:119], a[236:239], v[104:107], a[116:119]\n"
  "v_cvt_pk_bf16_f32 v11, v14, v15\n"
  "v_mfma_f32_16x16x32_bf16 a[132:135], v[208:211], v[72:75], a[132:135]\n"
  "v_exp_f32 v16, v16\n"
  "v_mfma_f32_16x16x32_bf16 a[132:135], v[208:211], v[104:107], a[132:135]\n"
  "v_mfma_f32_16x16x32_bf16 a[8:11], a[176:179], v[80:83], a[8:11]\n"
  "v_exp_f32 v17, v17\n"
  "v_mfma_f32_16x16x32_bf16 a[8:11], a[208:211], v[112:115], a[8:11]\n"
  "v_cvt_pk_bf16_f32 v16, v16, v17\n"
  "v_mfma_f32_16x16x32_bf16 a[24:27], a[180:183], v[80:83], a[24:27]\n"
  "s_add_u32 m0, s42, 16384\n"
  "v_exp_f32 v18, v18\n"
  "v_mfma_f32_16x16x32_bf16 a[24:27], a[212:215], v[112:115], a[24:27]\n"
  "buffer_load_dwordx4 v228, s[20:23], s40 offen offset:0 nt lds\n"
  "v_mfma_f32_16x16x32_bf16 a[40:43], a[184:187], v[80:83], a[40:43]\n"
  "v_exp_f32 v19, v19\n"
  "v_mfma_f32_16x16x32_bf16 a[40:43], a[216:219], v[112:115], a[40:43]\n"
  "v_cvt_pk_bf16_f32 v17, v18, v19\n"
  "v_mfma_f32_16x16x32_bf16 a[56:59], a[188:191], v[80:83], a[56:59]\n"
  "v_exp_f32 v20, v20\n"
  "v_mfma_f32_16x16x32_bf16 a[56:59], a[220:223], v[112:115], a[56:59]\n"
  "buffer_load_dwordx4 v229, s[20:23], s40 offen offset:1024 nt lds\n"
  "v_mfma_f32_16x16x32_bf16 a[72:75], a[192:195], v[80:83], a[72:75]\n"
  "v_exp_f32 v21, v21\n"
  "v_mfma_f32_16x16x32_bf16 a[72:75], a[224:227], v[112:115], a[72:75]\n"
  "v_cvt_pk_bf16_f32 v18, v20, v21\n"
  "v_mfma_f32_16x16x32_bf16 a[88:91], a[196:199], v[80:83], a[88:91]\n"
  "v_exp_f32 v22, v22\n"
  "v_mfma_f32_16x16x32_bf16 a[88:91], a[228:231], v[112:115], a[88:91]\n"
  "buffer_load_dwordx4 v230, s[20:23], s40 offen offset:2048 nt lds\n"
  "v_mfma_f32_16x16x32_bf16 a[104:107], a[200:203], v[80:83], a[104:107]\n"
  "v_exp_f32 v23, v23\n"
  "v_mfma_f32_16x16x32_bf16 a[104:107], a[232:235], v[112:115], a[104:107]\n"
  "v_cvt_pk_bf16_f32 v19, v22, v23\n"
  "v_mfma_f32_16x16x32_bf16 a[120:123], a[204:207], v[80:83], a[120:123]\n"
  "v_exp_f32 v24, v24\n"
  "v_mfma_f32_16x16x32_bf16 a[120:123], a[236:239], v[112:115], a[120:123]\n"
  "buffer_load_dwordx4 v231, s[20:23], s40 offen offset:3072 nt lds\n"
  "v_mfma_f32_16x16x32_bf16 a[136:139], v[208:211], v[80:83], a[136:139]\n"
  "s_add_u32 s40, s40, 16384\n"
  "v_exp_f32 v25, v25\n"
  "v_mfma_f32_16x16x32_bf16 a[136:139], v[208:211], v[112:115], a[136:139]\n"
  "v_cvt_pk_bf16_f32 v24, v24, v25\n"
  "v_mfma_f32_16x16x32_bf16 a[12:15], a[176:179], v[88:91], a[12:15]\n"
  "v_exp_f32 v26, v26\n"
  "v_mfma_f32_16x16x32_bf16 a[12:15], a[208:211], v[120:123], a[12:15]\n"
  "v_mfma_f32_16x16x32_bf16 a[28:31], a[180:183], v[88:91], a[28:31]\n"
  "v_exp_f32 v27, v27\n"
  "v_mfma_f32_16x16x32_bf16 a[28:31], a[212:215], v[120:123], a[28:31]\n"
  "v_cvt_pk_bf16_f32 v25, v26, v27\n"
  "v_mfma_f32_16x16x32_bf16 a[44:47], a[184:187], v[88:91], a[44:47]\n"
  "v_exp_f32 v28, v28\n"
  "v_mfma_f32_16x16x32_bf16 a[44:47], a[216:219], v[120:123], a[44:47]\n"
  "v_mfma_f32_16x16x32_bf16 a[60:63], a[188:191], v[88:91], a[60:63]\n"
  "v_exp_f32 v29, v29\n"
  "v_mfma_f32_16x16x32_bf16 a[60:63], a[220:223], v[120:123], a[60:63]\n"
  "v_cvt_pk_bf16_f32 v26, v28, v29\n"
  "v_mfma_f32_16x16x32_bf16 a[76:79], a[192:195], v[88:91], a[76:79]\n"
  "v_exp_f32 v30, v30\n"
  "v_mfma_f32_16x16x32_bf16 a[76:79], a[224:227], v[120:123], a[76:79]\n"
  "v_mfma_f32_16x16x32_bf16 a[92:95], a[196:199], v[88:91], a[92:95]\n"
  "v_exp_f32 v31, v31\n"
  "v_mfma_f32_16x16x32_bf16 a[92:95], a[228:231], v[120:123], a[92:95]\n"
  "v_cvt_pk_bf16_f32 v27, v30, v31\n"
  "v_mfma_f32_16x16x32_bf16 a[108:111], a[200:203], v[88:91], a[108:111]\n"
  "v_exp_f32 v32, v32\n"
  "v_mfma_f32_16x16x32_bf16 a[108:111], a[232:235], v[120:123], a[108:111]\n"
  "v_mfma_f32_16x16x32_bf16 a[124:127], a[204:207], v[88:91], a[124:127]\n"
  "v_exp_f32 v33, v33\n"
  "v_mfma_f32_16x16x32_bf16 a[124:127], a[236:239], v[120:123], a[124:127]\n"
  "v_cvt_pk_bf16_f32 v32, v32, v33\n"
  "v_mfma_f32_16x16x32_bf16 a[140:143], v[208:211], v[88:91], a[140:143]\n"
  "v_exp_f32 v34, v34\n"
  "v_mfma_f32_16x16x32_bf16 a[140:143], v[208:211], v[120:123], a[140:143]\n"
  "s_waitcnt lgkmcnt(0)\n"
  "v_mfma_f32_16x16x32_bf16 v[64:67], a[144:147], v[128:131], v[192:195]\n"
  "v_exp_f32 v35, v35\n"
  "v_mfma_f32_16x16x32_bf16 v[64:67], a[148:151], v[132:135], v[64:67]\n"
  "ds_read_b64_tr_b16 a[176:177], v216 offset:32768\n"
  "v_cvt_pk_bf16_f32 v33, v34, v35\n"
  "v_mfma_f32_16x16x32_bf16 v[64:67], a[152:155], v[136:139], v[64:67]\n"
  "v_exp_f32 v36, v36\n"
  "v_mfma_f32_16x16x32_bf16 v[64:67], a[156:159], v[140:143], v[64:67]\n"
  "ds_read_b64_tr_b16 a[178:179], v216 offset:36864\n"
  "ds_read_b64_tr_b16 a[208:209], v216 offset:40960\n"
  "v_mfma_f32_16x16x32_bf16 v[72:75], a[144:147], v[144:147], v[196:199]\n"
  "v_exp_f32 v37, v37\n"
  "v_mfma_f32_16x16x32_bf16 v[72:75], a[148:151], v[148:151], v[72:75]\n"
  "ds_read_b64_tr_b16 a[210:211], v216 offset:45056\n"
  "v_cvt_pk_bf16_f32 v34, v36, v37\n"
  "v_mfma_f32_16x16x32_bf16 v[72:75], a[152:155], v[152:155], v[72:75]\n"
  "v_exp_f32 v38, v38\n"
  "v_mfma_f32_16x16x32_bf16 v[72:75], a[156:159], v[156:159], v[72:75]\n"
  "ds_read_b64_tr_b16 a[180:181], v217 offset:32768\n"
  "ds_read_b64_tr_b16 a[182:183], v217 offset:36864\n"
  "v_mfma_f32_16x16x32_bf16 v[80:83], a[144:147], v[160:163], v[200:203]\n"
  "v_exp_f32 v39, v39\n"
  "v_mfma_f32_16x16x32_bf16 v[80:83], a[148:151], v[164:167], v[80:83]\n"
  "ds_read_b64_tr_b16 a[212:213], v217 offset:40960\n"
  "v_cvt_pk_bf16_f32 v35, v38, v39\n"
  "v_mfma_f32_16x16x32_bf16 v[80:83], a[152:155], v[168:171], v[80:83]\n"
  "v_exp_f32 v40, v40\n"
  "v_mfma_f32_16x16x32_bf16 v[80:83], a[156:159], v[172:175], v[80:83]\n"
  "ds_read_b64_tr_b16 a[214:215], v217 offset:45056\n"
  "ds_read_b64_tr_b16 a[184:185], v218 offset:32768\n"
  "v_mfma_f32_16x16x32_bf16 v[88:91], a[144:147], v[176:179], v[204:207]\n"
  "v_exp_f32 v41, v41\n"
  "v_mfma_f32_16x16x32_bf16 v[88:91], a[148:151], v[180:183], v[88:91]\n"
  "ds_read_b64_tr_b16 a[186:187], v218 offset:36864\n"
  "v_cvt_pk_bf16_f32 v40, v40, v41\n"
  "v_mfma_f32_16x16x32_bf16 v[88:91], a[152:155], v[184:187], v[88:91]\n"
  "v_exp_f32 v42, v42\n"
  "v_mfma_f32_16x16x32_bf16 v[88:91], a[156:159], v[188:191], v[88:91]\n"
  "ds_read_b64_tr_b16 a[216:217], v218 offset:40960\n"
  "ds_read_b64_tr_b16 a[218:219], v218 offset:45056\n"
  "v_mfma_f32_16x16x32_bf16 v[68:71], a[160:163], v[128:131], v[192:195]\n"
  "v_exp_f32 v43, v43\n"
  "v_mfma_f32_16x16x32_bf16 v[68:71], a[164:167], v[132:135], v[68:71]\n"
  "ds_read_b128 a[144:147], v224 offset:57344\n"
  "v_cvt_pk_bf16_f32 v41, v42, v43\n"
  "v_mfma_f32_16x16x32_bf16 v[68:71], a[168:171], v[136:139], v[68:71]\n"
  "v_exp_f32 v44, v44\n"
  "v_mfma_f32_16x16x32_bf16 v[68:71], a[172:175], v[140:143], v[68:71]\n"
  "ds_read_b128 a[148:151], v225 offset:57344\n"
  "ds_read_b128 a[152:155], v226 offset:57344\n"
  "v_mfma_f32_16x16x32_bf16 v[76:79], a[160:163], v[144:147], v[196:199]\n"
  "v_exp_f32 v45, v45\n"
  "v_mfma_f32_16x16x32_bf16 v[76:79], a[164:167], v[148:151], v[76:79]\n"
  "ds_read_b128 a[156:159], v227 offset:57344\n"
  "v_cvt_pk_bf16_f32 v42, v44, v45\n"
  "v_mfma_f32_16x16x32_bf16 v[76:79], a[168:171], v[152:155], v[76:79]\n"
  "v_exp_f32 v46, v46\n"
  "v_mfma_f32_16x16x32_bf16 v[76:79], a[172:175], v[156:159], v[76:79]\n"
  "ds_read_b64_tr_b16 a[188:189], v219 offset:32768\n"
  "ds_read_b64_tr_b16 a[190:191], v219 offset:36864\n"
  "v_mfma_f32_16x16x32_bf16 v[84:87], a[160:163], v[160:163], v[200:203]\n"
  "v_exp_f32 v47, v47\n"
  "v_mfma_f32_16x16x32_bf16 v[84:87], a[164:167], v[164:167], v[84:87]\n"
  "ds_read_b64_tr_b16 a[220:221], v219 offset:40960\n"
  "v_cvt_pk_bf16_f32 v43, v46, v47\n"
  "v_mfma_f32_16x16x32_bf16 v[84:87], a[168:171], v[168:171], v[84:87]\n"
  "v_exp_f32 v48, v48\n"
  "v_mfma_f32_16x16x32_bf16 v[84:87], a[172:175], v[172:175], v[84:87]\n"
  "ds_read_b64_tr_b16 a[222:223], v219 offset:45056\n"
  "ds_read_b64_tr_b16 a[192:193], v220 offset:32768\n"
  "v_mfma_f32_16x16x32_bf16 v[92:95], a[160:163], v[176:179], v[204:207]\n"
  "v_exp_f32 v49, v49\n"
  "v_mfma_f32_16x16x32_bf16 v[92:95], a[164:167], v[180:183], v[92:95]\n"
  "ds_read_b64_tr_b16 a[194:195], v220 offset:36864\n"
  "v_cvt_pk_bf16_f32 v48, v48, v49\n"
  "v_mfma_f32_16x16x32_bf16 v[92:95], a[168:171], v[184:187], v[92:95]\n"
  "v_exp_f32 v50, v50\n"
  "v_mfma_f32_16x16x32_bf16 v[92:95], a[172:175], v[188:191], v[92:95]\n"
  "s_waitcnt lgkmcnt(6)\n"
  "ds_read_b64_tr_b16 a[224:225], v220 offset:40960\n"
  "ds_read_b64_tr_b16 a[226:227], v220 offset:45056\n"
  "v_mfma_f32_16x16x32_bf16 v[96:99], a[144:147], v[128:131], v[192:195]\n"
  "v_exp_f32 v51, v51\n"
  "v_mfma_f32_16x16x32_bf16 v[96:99], a[148:151], v[132:135], v[96:99]\n"
  "ds_read_b128 a[160:163], v224 offset:61440\n"
  "v_cvt_pk_bf16_f32 v49, v50, v51\n"
  "v_mfma_f32_16x16x32_bf16 v[96:99], a[152:155], v[136:139], v[96:99]\n"
  "v_exp_f32 v52, v52\n"
  "v_mfma_f32_16x16x32_bf16 v[96:99], a[156:159], v[140:143], v[96:99]\n"
  "ds_read_b128 a[164:167], v225 offset:61440\n"
  "ds_read_b128 a[168:171], v226 offset:61440\n"
  "v_mfma_f32_16x16x32_bf16 v[104:107], a[144:147], v[144:147], v[196:199]\n"
  "v_exp_f32 v53, v53\n"
  "v_mfma_f32_16x16x32_bf16 v[104:107], a[148:151], v[148:151], v[104:107]\n"
  "ds_read_b128 a[172:175], v227 offset:61440\n"
  "v_cvt_pk_bf16_f32 v50, v52, v53\n"
  "v_mfma_f32_16x16x32_bf16 v[104:107], a[152:155], v[152:155], v[104:107]\n"
  "v_exp_f32 v54, v54\n"
  "v_mfma_f32_16x16x32_bf16 v[104:107], a[156:159], v[156:159], v[104:107]\n"
  "ds_read_b64_tr_b16 a[196:197], v221 offset:32768\n"
  "ds_read_b64_tr_b16 a[198:199], v221 offset:36864\n"
  "v_mfma_f32_16x16x32_bf16 v[112:115], a[144:147], v[160:163], v[200:203]\n"
  "v_exp_f32 v55, v55\n"
  "v_mfma_f32_16x16x32_bf16 v[112:115], a[148:151], v[164:167], v[112:115]\n"
  "ds_read_b64_tr_b16 a[228:229], v221 offset:40960\n"
  "v_cvt_pk_bf16_f32 v51, v54, v55\n"
  "v_mfma_f32_16x16x32_bf16 v[112:115], a[152:155], v[168:171], v[112:115]\n"
  "v_exp_f32 v56, v56\n"
  "v_mfma_f32_16x16x32_bf16 v[112:115], a[156:159], v[172:175], v[112:115]\n"
  "ds_read_b64_tr_b16 a[230:231], v221 offset:45056\n"
  "ds_read_b64_tr_b16 a[200:201], v222 offset:32768\n"
  "v_mfma_f32_16x16x32_bf16 v[120:123], a[144:147], v[176:179], v[204:207]\n"
  "v_exp_f32 v57, v57\n"
  "v_mfma_f32_16x16x32_bf16 v[120:123], a[148:151], v[180:183], v[120:123]\n"
  "ds_read_b64_tr_b16 a[202:203], v222 offset:36864\n"
  "v_cvt_pk_bf16_f32 v56, v56, v57\n"
  "v_mfma_f32_16x16x32_bf16 v[120:123], a[152:155], v[184:187], v[120:123]\n"
  "v_exp_f32 v58, v58\n"
  "v_mfma_f32_16x16x32_bf16 v[120:123], a[156:159], v[188:191], v[120:123]\n"
  "s_waitcnt lgkmcnt(6)\n"
  "ds_read_b64_tr_b16 a[232:233], v222 offset:40960\n"
  "ds_read_b64_tr_b16 a[234:235], v222 offset:45056\n"
  "v_mfma_f32_16x16x32_bf16 v[100:103], a[160:163], v[128:131], v[192:195]\n"
  "v_exp_f32 v59, v59\n"
  "v_mfma_f32_16x16x32_bf16 v[100:103], a[164:167], v[132:135], v[100:103]\n"
  "v_cvt_pk_bf16_f32 v57, v58, v59\n"
  "v_mfma_f32_16x16x32_bf16 v[100:103], a[168:171], v[136:139], v[100:103]\n"
  "v_exp_f32 v60, v60\n"
  "v_mfma_f32_16x16x32_bf16 v[100:103], a[172:175], v[140:143], v[100:103]\n"
  "ds_read_b64_tr_b16 a[204:205], v223 offset:32768\n"
  "ds_read_b64_tr_b16 a[206:207], v223 offset:36864\n"
  "v_mfma_f32_16x16x32_bf16 v[108:111], a[160:163], v[144:147], v[196:199]\n"
  "v_exp_f32 v61, v61\n"
  "v_mfma_f32_16x16x32_bf16 v[108:111], a[164:167], v[148:151], v[108:111]\n"
  "v_cvt_pk_bf16_f32 v58, v60, v61\n"
  "v_mfma_f32_16x16x32_bf16 v[108:111], a[168:171], v[152:155], v[108:111]\n"
  "v_exp_f32 v62, v62\n"
  "v_mfma_f32_16x16x32_bf16 v[108:111], a[172:175], v[156:159], v[108:111]\n"
  "ds_read_b64_tr_b16 a[236:237], v223 offset:40960\n"
  "ds_read_b64_tr_b16 a[238:239], v223 offset:45056\n"
  "v_mfma_f32_16x16x32_bf16 v[116:119], a[160:163], v[160:163], v[200:203]\n"
  "v_exp_f32 v63, v63\n"
  "v_mfma_f32_16x16x32_bf16 v[116:119], a[164:167], v[164:167], v[116:119]\n"
  "v_cvt_pk_bf16_f32 v59, v62, v63\n"
  "v_mfma_f32_16x16x32_bf16 v[116:119], a[168:171], v[168:171], v[116:119]\n"
  "v_mfma_f32_16x16x32_bf16 v[116:119], a[172:175], v[172:175], v[116:119]\n"
  "v_mfma_f32_16x16x32_bf16 v[124:127], a[160:163], v[176:179], v[204:207]\n"
  "v_mfma_f32_16x16x32_bf16 v[124:127], a[164:167], v[180:183], v[124:127]\n"
  "v_mfma_f32_16x16x32_bf16 v[124:127], a[168:171], v[184:187], v[124:127]\n"
  "v_mfma_f32_16x16x32_bf16 v[124:127], a[172:175], v[188:191], v[124:127]\n"
  "s_waitcnt vmcnt(4) lgkmcnt(0)\n"
  "s_barrier\n"
  "v_mfma_f32_16x16x32_bf16 a[0:3], a[176:179], v[0:3], a[0:3]\n"
  "s_add_u32 m0, s42, 65536\n"
  "v_mfma_f32_16x16x32_bf16 a[0:3], a[208:211], v[32:35], a[0:3]\n"
  "buffer_load_dwordx4 v232, s[24:27], s41 offen offset:0 nt lds\n"
  "v_mfma_f32_16x16x32_bf16 a[16:19], a[180:183], v[0:3], a[16:19]\n"
  "v_exp_f32 v64, v64\n"
  "v_mfma_f32_16x16x32_bf16 a[16:19], a[212:215], v[32:35], a[16:19]\n"
  "buffer_load_dwordx4 v233, s[24:27], s41 offen offset:1024 nt lds\n"
  "v_mfma_f32_16x16x32_bf16 a[32:35], a[184:187], v[0:3], a[32:35]\n"
  "v_exp_f32 v65, v65\n"
  "v_mfma_f32_16x16x32_bf16 a[32:35], a[216:219], v[32:35], a[32:35]\n"
  "v_cvt_pk_bf16_f32 v64, v64, v65\n"
  "v_mfma_f32_16x16x32_bf16 a[48:51], a[188:191], v[0:3], a[48:51]\n"
  "v_exp_f32 v66, v66\n"
  "v_mfma_f32_16x16x32_bf16 a[48:51], a[220:223], v[32:35], a[48:51]\n"
  "ds_read_b128 a[144:147], v224 offset:0\n"
  "v_mfma_f32_16x16x32_bf16 a[64:67], a[192:195], v[0:3], a[64:67]\n"
  "v_exp_f32 v67, v67\n"
  "v_mfma_f32_16x16x32_bf16 a[64:67], a[224:227], v[32:35], a[64:67]\n"
  "ds_read_b128 a[148:151], v225 offset:0\n"
  "v_cvt_pk_bf16_f32 v65, v66, v67\n"
  "v_mfma_f32_16x16x32_bf16 a[80:83], a[196:199], v[0:3], a[80:83]\n"
  "v_exp_f32 v68, v68\n"
  "v_mfma_f32_16x16x32_bf16 a[80:83], a[228:231], v[32:35], a[80:83]\n"
  "ds_read_b128 a[152:155], v226 offset:0\n"
  "v_mfma_f32_16x16x32_bf16 a[96:99], a[200:203], v[0:3], a[96:99]\n"
  "v_exp_f32 v69, v69\n"
  "v_mfma_f32_16x16x32_bf16 a[96:99], a[232:235], v[32:35], a[96:99]\n"
  "ds_read_b128 a[156:159], v227 offset:0\n"
  "v_cvt_pk_bf16_f32 v66, v68, v69\n"
  "v_mfma_f32_16x16x32_bf16 a[112:115], a[204:207], v[0:3], a[112:115]\n"
  "v_exp_f32 v70, v70\n"
  "v_mfma_f32_16x16x32_bf16 a[112:115], a[236:239], v[32:35], a[112:115]\n"
  "ds_read_b128 a[160:163], v224 offset:4096\n"
  "v_mfma_f32_16x16x32_bf16 a[128:131], v[208:211], v[0:3], a[128:131]\n"
  "v_exp_f32 v71, v71\n"
  "v_mfma_f32_16x16x32_bf16 a[128:131], v[208:211], v[32:35], a[128:131]\n"
  "ds_read_b128 a[164:167], v225 offset:4096\n"
  "v_cvt_pk_bf16_f32 v67, v70, v71\n"
  "v_mfma_f32_16x16x32_bf16 a[4:7], a[176:179], v[8:11], a[4:7]\n"
  "v_exp_f32 v72, v72\n"
  "v_mfma_f32_16x16x32_bf16 a[4:7], a[208:211], v[40:43], a[4:7]\n"
  "ds_read_b128 a[168:171], v226 offset:4096\n"
  "v_mfma_f32_16x16x32_bf16 a[20:23], a[180:183], v[8:11], a[20:23]\n"
  "v_exp_f32 v73, v73\n"
  "v_mfma_f32_16x16x32_bf16 a[20:23], a[212:215], v[40:43], a[20:23]\n"
  "ds_read_b128 a[172:175], v227 offset:4096\n"
  "v_cvt_pk_bf16_f32 v72, v72, v73\n"
  "v_mfma_f32_16x16x32_bf16 a[36:39], a[184:187], v[8:11], a[36:39]\n"
  "v_exp_f32 v74, v74\n"
  "v_mfma_f32_16x16x32_bf16 a[36:39], a[216:219], v[40:43], a[36:39]\n"
  "buffer_load_dwordx4 v234, s[24:27], s41 offen offset:2048 nt lds\n"
  "v_mfma_f32_16x16x32_bf16 a[52:55], a[188:191], v[8:11], a[52:55]\n"
  "v_exp_f32 v75, v75\n"
  "v_mfma_f32_16x16x32_bf16 a[52:55], a[220:223], v[40:43], a[52:55]\n"
  "v_cvt_pk_bf16_f32 v73, v74, v75\n"
  "v_mfma_f32_16x16x32_bf16 a[68:71], a[192:195], v[8:11], a[68:71]\n"
  "v_exp_f32 v76, v76\n"
  "v_mfma_f32_16x16x32_bf16 a[68:71], a[224:227], v[40:43], a[68:71]\n"
  "buffer_load_dwordx4 v235, s[24:27], s41 offen offset:3072 nt lds\n"
  "v_mfma_f32_16x16x32_bf16 a[84:87], a[196:199], v[8:11], a[84:87]\n"
  "s_add_u32 s41, s41, 16384\n"
  "v_exp_f32 v77, v77\n"
  "v_mfma_f32_16x16x32_bf16 a[84:87], a[228:231], v[40:43], a[84:87]\n"
  "v_cvt_pk_bf16_f32 v74, v76, v77\n"
  "v_mfma_f32_16x16x32_bf16 a[100:103], a[200:203], v[8:11], a[100:103]\n"
  "v_exp_f32 v78, v78\n"
  "v_mfma_f32_16x16x32_bf16 a[100:103], a[232:235], v[40:43], a[100:103]\n"
  "v_mfma_f32_16x16x32_bf16 a[116:119], a[204:207], v[8:11], a[116:119]\n"
  "v_exp_f32 v79, v79\n"
  "v_mfma_f32_16x16x32_bf16 a[116:119], a[236:239], v[40:43], a[116:119]\n"
  "v_cvt_pk_bf16_f32 v75, v78, v79\n"
  "v_mfma_f32_16x16x32_bf16 a[132:135], v[208:211], v[8:11], a[132:135]\n"
  "v_exp_f32 v80, v80\n"
  "v_mfma_f32_16x16x32_bf16 a[132:135], v[208:211], v[40:43], a[132:135]\n"
  "v_mfma_f32_16x16x32_bf16 a[8:11], a[176:179], v[16:19], a[8:11]\n"
  "v_exp_f32 v81, v81\n"
  "v_mfma_f32_16x16x32_bf16 a[8:11], a[208:211], v[48:51], a[8:11]\n"
  "v_cvt_pk_bf16_f32 v80, v80, v81\n"
  "v_mfma_f32_16x16x32_bf16 a[24:27], a[180:183], v[16:19], a[24:27]\n"
  "s_add_u32 m0, s42, 32768\n"
  "v_exp_f32 v82, v82\n"
  "v_mfma_f32_16x16x32_bf16 a[24:27], a[212:215], v[48:51], a[24:27]\n"
  "buffer_load_dwordx4 v228, s[20:23], s40 offen offset:0 nt lds\n"
  "v_mfma_f32_16x16x32_bf16 a[40:43], a[184:187], v[16:19], a[40:43]\n"
  "v_exp_f32 v83, v83\n"
  "v_mfma_f32_16x16x32_bf16 a[40:43], a[216:219], v[48:51], a[40:43]\n"
  "v_cvt_pk_bf16_f32 v81, v82, v83\n"
  "v_mfma_f32_16x16x32_bf16 a[56:59], a[188:191], v[16:19], a[56:59]\n"
  "v_exp_f32 v84, v84\n"
  "v_mfma_f32_16x16x32_bf16 a[56:59], a[220:223], v[48:51], a[56:59]\n"
  "buffer_load_dwordx4 v229, s[20:23], s40 offen offset:1024 nt lds\n"
  "v_mfma_f32_16x16x32_bf16 a[72:75], a[192:195], v[16:19], a[72:75]\n"
  "v_exp_f32 v85, v85\n"
  "v_mfma_f32_16x16x32_bf16 a[72:75], a[224:227], v[48:51], a[72:75]\n"
  "v_cvt_pk_bf16_f32 v82, v84, v85\n"
  "v_mfma_f32_16x16x32_bf16 a[88:91], a[196:199], v[16:19], a[88:91]\n"
  "v_exp_f32 v86, v86\n"
  "v_mfma_f32_16x16x32_bf16 a[88:91], a[228:231], v[48:51], a[88:91]\n"
  "buffer_load_dwordx4 v230, s[20:23], s40 offen offset:2048 nt lds\n"
  "v_mfma_f32_16x16x32_bf16 a[104:107], a[200:203], v[16:19], a[104:107]\n"
  "v_exp_f32 v87, v87\n"
  "v_mfma_f32_16x16x32_bf16 a[104:107], a[232:235], v[48:51], a[104:107]\n"
  "v_cvt_pk_bf16_f32 v83, v86, v87\n"
  "v_mfma_f32_16x16x32_bf16 a[120:123], a[204:207], v[16:19], a[120:123]\n"
  "v_exp_f32 v88, v88\n"
  "v_mfma_f32_16x16x32_bf16 a[120:123], a[236:239], v[48:51], a[120:123]\n"
  "buffer_load_dwordx4 v231, s[20:23], s40 offen offset:3072 nt lds\n"
  "v_mfma_f32_16x16x32_bf16 a[136:139], v[208:211], v[16:19], a[136:139]\n"
  "s_add_u32 s40, s40, 16384\n"
  "v_exp_f32 v89, v89\n"
  "v_mfma_f32_16x16x32_bf16 a[136:139], v[208:211], v[48:51], a[136:139]\n"
  "v_cvt_pk_bf16_f32 v88, v88, v89\n"
  "v_mfma_f32_16x16x32_bf16 a[12:15], a[176:179], v[24:27], a[12:15]\n"
  "v_exp_f32 v90, v90\n"
  "v_mfma_f32_16x16x32_bf16 a[12:15], a[208:211], v[56:59], a[12:15]\n"
  "v_mfma_f32_16x16x32_bf16 a[28:31], a[180:183], v[24:27], a[28:31]\n"
  "v_exp_f32 v91, v91\n"
  "v_mfma_f32_16x16x32_bf16 a[28:31], a[212:215], v[56:59], a[28:31]\n"
  "v_cvt_pk_bf16_f32 v89, v90, v91\n"
  "v_mfma_f32_16x16x32_bf16 a[44:47], a[184:187], v[24:27], a[44:47]\n"
  "v_exp_f32 v92, v92\n"
  "v_mfma_f32_16x16x32_bf16 a[44:47], a[216:219], v[56:59], a[44:47]\n"
  "v_mfma_f32_16x16x32_bf16 a[60:63], a[188:191], v[24:27], a[60:63]\n"
  "v_exp_f32 v93, v93\n"
  "v_mfma_f32_16x16x32_bf16 a[60:63], a[220:223], v[56:59], a[60:63]\n"
  "v_cvt_pk_bf16_f32 v90, v92, v93\n"
  "v_mfma_f32_16x16x32_bf16 a[76:79], a[192:195], v[24:27], a[76:79]\n"
  "v_exp_f32 v94, v94\n"
  "v_mfma_f32_16x16x32_bf16 a[76:79], a[224:227], v[56:59], a[76:79]\n"
  "v_mfma_f32_16x16x32_bf16 a[92:95], a[196:199], v[24:27], a[92:95]\n"
  "v_exp_f32 v95, v95\n"
  "v_mfma_f32_16x16x32_bf16 a[92:95], a[228:231], v[56:59], a[92:95]\n"
  "v_cvt_pk_bf16_f32 v91, v94, v95\n"
  "v_mfma_f32_16x16x32_bf16 a[108:111], a[200:203], v[24:27], a[108:111]\n"
  "v_exp_f32 v96, v96\n"
  "v_mfma_f32_16x16x32_bf16 a[108:111], a[232:235], v[56:59], a[108:111]\n"
  "v_mfma_f32_16x16x32_bf16 a[124:127], a[204:207], v[24:27], a[124:127]\n"
  "v_exp_f32 v97, v97\n"
  "v_mfma_f32_16x16x32_bf16 a[124:127], a[236:239], v[56:59], a[124:127]\n"
  "v_cvt_pk_bf16_f32 v96, v96, v97\n"
  "v_mfma_f32_16x16x32_bf16 a[140:143], v[208:211], v[24:27], a[140:143]\n"
  "v_exp_f32 v98, v98\n"
  "v_mfma_f32_16x16x32_bf16 a[140:143], v[208:211], v[56:59], a[140:143]\n"
  "s_waitcnt lgkmcnt(0)\n"
  "v_mfma_f32_16x16x32_bf16 v[0:3], a[144:147], v[128:131], v[192:195]\n"
  "v_exp_f32 v99, v99\n"
  "v_mfma_f32_16x16x32_bf16 v[0:3], a[148:151], v[132:135], v[0:3]\n"
  "ds_read_b64_tr_b16 a[176:177], v216 offset:49152\n"
  "v_cvt_pk_bf16_f32 v97, v98, v99\n"
  "v_mfma_f32_16x16x32_bf16 v[0:3], a[152:155], v[136:139], v[0:3]\n"
  "v_exp_f32 v100, v100\n"
  "v_mfma_f32_16x16x32_bf16 v[0:3], a[156:159], v[140:143], v[0:3]\n"
  "ds_read_b64_tr_b16 a[178:179], v216 offset:53248\n"
  "ds_read_b64_tr_b16 a[208:209], v216 offset:57344\n"
  "v_mfma_f32_16x16x32_bf16 v[8:11], a[144:147], v[144:147], v[196:199]\n"
  "v_exp_f32 v101, v101\n"
  "v_mfma_f32_16x16x32_bf16 v[8:11], a[148:151], v[148:151], v[8:11]\n"
  "ds_read_b64_tr_b16 a[210:211], v216 offset:61440\n"
  "v_cvt_pk_bf16_f32 v98, v100, v101\n"
  "v_mfma_f32_16x16x32_bf16 v[8:11], a[152:155], v[152:155], v[8:11]\n"
  "v_exp_f32 v102, v102\n"
  "v_mfma_f32_16x16x32_bf16 v[8:11], a[156:159], v[156:159], v[8:11]\n"
  "ds_read_b64_tr_b16 a[180:181], v217 offset:49152\n"
  "ds_read_b64_tr_b16 a[182:183], v217 offset:53248\n"
  "v_mfma_f32_16x16x32_bf16 v[16:19], a[144:147], v[160:163], v[200:203]\n"
  "v_exp_f32 v103, v103\n"
  "v_mfma_f32_16x16x32_bf16 v[16:19], a[148:151], v[164:167], v[16:19]\n"
  "ds_read_b64_tr_b16 a[212:213], v217 offset:57344\n"
  "v_cvt_pk_bf16_f32 v99, v102, v103\n"
  "v_mfma_f32_16x16x32_bf16 v[16:19], a[152:155], v[168:171], v[16:19]\n"
  "v_exp_f32 v104, v104\n"
  "v_mfma_f32_16x16x32_bf16 v[16:19], a[156:159], v[172:175], v[16:19]\n"
  "ds_read_b64_tr_b16 a[214:215], v217 offset:61440\n"
  "ds_read_b64_tr_b16 a[184:185], v218 offset:49152\n"
  "v_mfma_f32_16x16x32_bf16 v[24:27], a[144:147], v[176:179], v[204:207]\n"
  "v_exp_f32 v105, v105\n"
  "v_mfma_f32_16x16x32_bf16 v[24:27], a[148:151], v[180:183], v[24:27]\n"
  "ds_read_b64_tr_b16 a[186:187], v218 offset:53248\n"
  "v_cvt_pk_bf16_f32 v104, v104, v105\n"
  "v_mfma_f32_16x16x32_bf16 v[24:27], a[152:155], v[184:187], v[24:27]\n"
  "v_exp_f32 v106, v106\n"
  "v_mfma_f32_16x16x32_bf16 v[24:27], a[156:159], v[188:191], v[24:27]\n"
  "ds_read_b64_tr_b16 a[216:217], v218 offset:57344\n"
  "ds_read_b64_tr_b16 a[218:219], v218 offset:61440\n"
  "v_mfma_f32_16x16x32_bf16 v[4:7], a[160:163], v[128:131], v[192:195]\n"
  "v_exp_f32 v107, v107\n"
  "v_mfma_f32_16x16x32_bf16 v[4:7], a[164:167], v[132:135], v[4:7]\n"
  "ds_read_b128 a[144:147], v224 offset:8192\n"
  "v_cvt_pk_bf16_f32 v105, v106, v107\n"
  "v_mfma_f32_16x16x32_bf16 v[4:7], a[168:171], v[136:139], v[4:7]\n"
  "v_exp_f32 v108, v108\n"
  "v_mfma_f32_16x16x32_bf16 v[4:7], a[172:175], v[140:143], v[4:7]\n"
  "ds_read_b128 a[148:151], v225 offset:8192\n"
  "ds_read_b128 a[152:155], v226 offset:8192\n"
  "v_mfma_f32_16x16x32_bf16 v[12:15], a[160:163], v[144:147], v[196:199]\n"
  "v_exp_f32 v109, v109\n"
  "v_mfma_f32_16x16x32_bf16 v[12:15], a[164:167], v[148:151], v[12:15]\n"
  "ds_read_b128 a[156:159], v227 offset:8192\n"
  "v_cvt_pk_bf16_f32 v106, v108, v109\n"
  "v_mfma_f32_16x16x32_bf16 v[12:15], a[168:171], v[152:155], v[12:15]\n"
  "v_exp_f32 v110, v110\n"
  "v_mfma_f32_16x16x32_bf16 v[12:15], a[172:175], v[156:159], v[12:15]\n"
  "ds_read_b64_tr_b16 a[188:189], v219 offset:49152\n"
  "ds_read_b64_tr_b16 a[190:191], v219 offset:53248\n"
  "v_mfma_f32_16x16x32_bf16 v[20:23], a[160:163], v[160:163], v[200:203]\n"
  "v_exp_f32 v111, v111\n"
  "v_mfma_f32_16x16x32_bf16 v[20:23], a[164:167], v[164:167], v[20:23]\n"
  "ds_read_b64_tr_b16 a[220:221], v219 offset:57344\n"
  "v_cvt_pk_bf16_f32 v107, v110, v111\n"
  "v_mfma_f32_16x16x32_bf16 v[20:23], a[168:171], v[168:171], v[20:23]\n"
  "v_exp_f32 v112, v112\n"
  "v_mfma_f32_16x16x32_bf16 v[20:23], a[172:175], v[172:175], v[20:23]\n"
  "ds_read_b64_tr_b16 a[222:223], v219 offset:61440\n"
  "ds_read_b64_tr_b16 a[192:193], v220 offset:49152\n"
  "v_mfma_f32_16x16x32_bf16 v[28:31], a[160:163], v[176:179], v[204:207]\n"
  "v_exp_f32 v113, v113\n"
  "v_mfma_f32_16x16x32_bf16 v[28:31], a[164:167], v[180:183], v[28:31]\n"
  "ds_read_b64_tr_b16 a[194:195], v220 offset:53248\n"
  "v_cvt_pk_bf16_f32 v112, v112, v113\n"
  "v_mfma_f32_16x16x32_bf16 v[28:31], a[168:171], v[184:187], v[28:31]\n"
  "v_exp_f32 v114, v114\n"
  "v_mfma_f32_16x16x32_bf16 v[28:31], a[172:175], v[188:191], v[28:31]\n"
  "s_waitcnt lgkmcnt(6)\n"
  "ds_read_b64_tr_b16 a[224:225], v220 offset:57344\n"
  "ds_read_b64_tr_b16 a[226:227], v220 offset:61440\n"
  "v_mfma_f32_16x16x32_bf16 v[32:35], a[144:147], v[128:131], v[192:195]\n"
  "v_exp_f32 v115, v115\n"
  "v_mfma_f32_16x16x32_bf16 v[32:35], a[148:151], v[132:135], v[32:35]\n"
  "ds_read_b128 a[160:163], v224 offset:12288\n"
  "v_cvt_pk_bf16_f32 v113, v114, v115\n"
  "v_mfma_f32_16x16x32_bf16 v[32:35], a[152:155], v[136:139], v[32:35]\n"
  "v_exp_f32 v116, v116\n"
  "v_mfma_f32_16x16x32_bf16 v[32:35], a[156:159], v[140:143], v[32:35]\n"
  "ds_read_b128 a[164:167], v225 offset:12288\n"
  "ds_read_b128 a[168:171], v226 offset:12288\n"
  "v_mfma_f32_16x16x32_bf16 v[40:43], a[144:147], v[144:147], v[196:199]\n"
  "v_exp_f32 v117, v117\n"
  "v_mfma_f32_16x16x32_bf16 v[40:43], a[148:151], v[148:151], v[40:43]\n"
  "ds_read_b128 a[172:175], v227 offset:12288\n"
  "v_cvt_pk_bf16_f32 v114, v116, v117\n"
  "v_mfma_f32_16x16x32_bf16 v[40:43], a[152:155], v[152:155], v[40:43]\n"
  "v_exp_f32 v118, v118\n"
  "v_mfma_f32_16x16x32_bf16 v[40:43], a[156:159], v[156:159], v[40:43]\n"
  "ds_read_b64_tr_b16 a[196:197], v221 offset:49152\n"
  "ds_read_b64_tr_b16 a[198:199], v221 offset:53248\n"
  "v_mfma_f32_16x16x32_bf16 v[48:51], a[144:147], v[160:163], v[200:203]\n"
  "v_exp_f32 v119, v119\n"
  "v_mfma_f32_16x16x32_bf16 v[48:51], a[148:151], v[164:167], v[48:51]\n"
  "ds_read_b64_tr_b16 a[228:229], v221 offset:57344\n"
  "v_cvt_pk_bf16_f32 v115, v118, v119\n"
  "v_mfma_f32_16x16x32_bf16 v[48:51], a[152:155], v[168:171], v[48:51]\n"
  "v_exp_f32 v120, v120\n"
  "v_mfma_f32_16x16x32_bf16 v[48:51], a[156:159], v[172:175], v[48:51]\n"
  "ds_read_b64_tr_b16 a[230:231], v221 offset:61440\n"
  "ds_read_b64_tr_b16 a[200:201], v222 offset:49152\n"
  "v_mfma_f32_16x16x32_bf16 v[56:59], a[144:147], v[176:179], v[204:207]\n"
  "v_exp_f32 v121, v121\n"
  "v_mfma_f32_16x16x32_bf16 v[56:59], a[148:151], v[180:183], v[56:59]\n"
  "ds_read_b64_tr_b16 a[202:203], v222 offset:53248\n"
  "v_cvt_pk_bf16_f32 v120, v120, v121\n"
  "v_mfma_f32_16x16x32_bf16 v[56:59], a[152:155], v[184:187], v[56:59]\n"
  "v_exp_f32 v122, v122\n"
  "v_mfma_f32_16x16x32_bf16 v[56:59], a[156:159], v[188:191], v[56:59]\n"
  "s_waitcnt lgkmcnt(6)\n"
  "ds_read_b64_tr_b16 a[232:233], v222 offset:57344\n"
  "ds_read_b64_tr_b16 a[234:235], v222 offset:61440\n"
  "v_mfma_f32_16x16x32_bf16 v[36:39], a[160:163], v[128:131], v[192:195]\n"
  "v_exp_f32 v123, v123\n"
  "v_mfma_f32_16x16x32_bf16 v[36:39], a[164:167], v[132:135], v[36:39]\n"
  "v_cvt_pk_bf16_f32 v121, v122, v123\n"
  "v_mfma_f32_16x16x32_bf16 v[36:39], a[168:171], v[136:139], v[36:39]\n"
  "v_exp_f32 v124, v124\n"
  "v_mfma_f32_16x16x32_bf16 v[36:39], a[172:175], v[140:143], v[36:39]\n"
  "ds_read_b64_tr_b16 a[204:205], v223 offset:49152\n"
  "ds_read_b64_tr_b16 a[206:207], v223 offset:53248\n"
  "v_mfma_f32_16x16x32_bf16 v[44:47], a[160:163], v[144:147], v[196:199]\n"
  "v_exp_f32 v125, v125\n"
  "v_mfma_f32_16x16x32_bf16 v[44:47], a[164:167], v[148:151], v[44:47]\n"
  "v_cvt_pk_bf16_f32 v122, v124, v125\n"
  "v_mfma_f32_16x16x32_bf16 v[44:47], a[168:171], v[152:155], v[44:47]\n"
  "v_exp_f32 v126, v126\n"
  "v_mfma_f32_16x16x32_bf16 v[44:47], a[172:175], v[156:159], v[44:47]\n"
  "ds_read_b64_tr_b16 a[236:237], v223 offset:57344\n"
  "ds_read_b64_tr_b16 a[238:239], v223 offset:61440\n"
  "v_mfma_f32_16x16x32_bf16 v[52:55], a[160:163], v[160:163], v[200:203]\n"
  "v_exp_f32 v127, v127\n"
  "v_mfma_f32_16x16x32_bf16 v[52:55], a[164:167], v[164:167], v[52:55]\n"
  "v_cvt_pk_bf16_f32 v123, v126, v127\n"
  "v_mfma_f32_16x16x32_bf16 v[52:55], a[168:171], v[168:171], v[52:55]\n"
  "v_mfma_f32_16x16x32_bf16 v[52:55], a[172:175], v[172:175], v[52:55]\n"
  "v_mfma_f32_16x16x32_bf16 v[60:63], a[160:163], v[176:179], v[204:207]\n"
  "v_mfma_f32_16x16x32_bf16 v[60:63], a[164:167], v[180:183], v[60:63]\n"
  "v_mfma_f32_16x16x32_bf16 v[60:63], a[168:171], v[184:187], v[60:63]\n"
  "v_mfma_f32_16x16x32_bf16 v[60:63], a[172:175], v[188:191], v[60:63]\n"
  "s_waitcnt vmcnt(4) lgkmcnt(0)\n"
  "s_barrier\n"
  "v_mfma_f32_16x16x32_bf16 a[0:3], a[176:179], v[64:67], a[0:3]\n"
  "s_add_u32 m0, s42, 81920\n"
  "v_mfma_f32_16x16x32_bf16 a[0:3], a[208:211], v[96:99], a[0:3]\n"
  "buffer_load_dwordx4 v232, s[24:27], s41 offen offset:0 nt lds\n"
  "v_mfma_f32_16x16x32_bf16 a[16:19], a[180:183], v[64:67], a[16:19]\n"
  "v_exp_f32 v0, v0\n"
  "v_mfma_f32_16x16x32_bf16 a[16:19], a[212:215], v[96:99], a[16:19]\n"
  "buffer_load_dwordx4 v233, s[24:27], s41 offen offset:1024 nt lds\n"
  "v_mfma_f32_16x16x32_bf16 a[32:35], a[184:187], v[64:67], a[32:35]\n"
  "v_exp_f32 v1, v1\n"
  "v_mfma_f32_16x16x32_bf16 a[32:35], a[216:219], v[96:99], a[32:35]\n"
  "v_cvt_pk_bf16_f32 v0, v0, v1\n"
  "v_mfma_f32_16x16x32_bf16 a[48:51], a[188:191], v[64:67], a[48:51]\n"
  "v_exp_f32 v2, v2\n"
  "v_mfma_f32_16x16x32_bf16 a[48:51], a[220:223], v[96:99], a[48:51]\n"
  "ds_read_b128 a[144:147], v224 offset:16384\n"
  "v_mfma_f32_16x16x32_bf16 a[64:67], a[192:195], v[64:67], a[64:67]\n"
  "v_exp_f32 v3, v3\n"
  "v_mfma_f32_16x16x32_bf16 a[64:67], a[224:227], v[96:99], a[64:67]\n"
  "ds_read_b128 a[148:151], v225 offset:16384\n"
  "v_cvt_pk_bf16_f32 v1, v2, v3\n"
  "v_mfma_f32_16x16x32_bf16 a[80:83], a[196:199], v[64:67], a[80:83]\n"
  "v_exp_f32 v4, v4\n"
  "v_mfma_f32_16x16x32_bf16 a[80:83], a[228:231], v[96:99], a[80:83]\n"
  "ds_read_b128 a[152:155], v226 offset:16384\n"
  "v_mfma_f32_16x16x32_bf16 a[96:99], a[200:203], v[64:67], a[96:99]\n"
  "v_exp_f32 v5, v5\n"
  "v_mfma_f32_16x16x32_bf16 a[96:99], a[232:235], v[96:99], a[96:99]\n"
  "ds_read_b128 a[156:159], v227 offset:16384\n"
  "v_cvt_pk_bf16_f32 v2, v4, v5\n"
  "v_mfma_f32_16x16x32_bf16 a[112:115], a[204:207], v[64:67], a[112:115]\n"
  "v_exp_f32 v6, v6\n"
  "v_mfma_f32_16x16x32_bf16 a[112:115], a[236:239], v[96:99], a[112:115]\n"
  "ds_read_b128 a[160:163], v224 offset:20480\n"
  "v_mfma_f32_16x16x32_bf16 a[128:131], v[208:211], v[64:67], a[128:131]\n"
  "v_exp_f32 v7, v7\n"
  "v_mfma_f32_16x16x32_bf16 a[128:131], v[208:211], v[96:99], a[128:131]\n"
  "ds_read_b128 a[164:167], v225 offset:20480\n"
  "v_cvt_pk_bf16_f32 v3, v6, v7\n"
  "v_mfma_f32_16x16x32_bf16 a[4:7], a[176:179], v[72:75], a[4:7]\n"
  "v_exp_f32 v8, v8\n"
  "v_mfma_f32_16x16x32_bf16 a[4:7], a[208:211], v[104:107], a[4:7]\n"
  "ds_read_b128 a[168:171], v226 offset:20480\n"
  "v_mfma_f32_16x16x32_bf16 a[20:23], a[180:183], v[72:75], a[20:23]\n"
  "v_exp_f32 v9, v9\n"
  "v_mfma_f32_16x16x32_bf16 a[20:23], a[212:215], v[104:107], a[20:23]\n"
  "ds_read_b128 a[172:175], v227 offset:20480\n"
  "v_cvt_pk_bf16_f32 v8, v8, v9\n"
  "v_mfma_f32_16x16x32_bf16 a[36:39], a[184:187], v[72:75], a[36:39]\n"
  "v_exp_f32 v10, v10\n"
  "v_mfma_f32_16x16x32_bf16 a[36:39], a[216:219], v[104:107], a[36:39]\n"
  "buffer_load_dwordx4 v234, s[24:27], s41 offen offset:2048 nt lds\n"
  "v_mfma_f32_16x16x32_bf16 a[52:55], a[188:191], v[72:75], a[52:55]\n"
  "v_exp_f32 v11, v11\n"
  "v_mfma_f32_16x16x32_bf16 a[52:55], a[220:223], v[104:107], a[52:55]\n"
  "v_cvt_pk_bf16_f32 v9, v10, v11\n"
  "v_mfma_f32_16x16x32_bf16 a[68:71], a[192:195], v[72:75], a[68:71]\n"
  "v_exp_f32 v12, v12\n"
  "v_mfma_f32_16x16x32_bf16 a[68:71], a[224:227], v[104:107], a[68:71]\n"
  "buffer_load_dwordx4 v235, s[24:27], s41 offen offset:3072 nt lds\n"
  "v_mfma_f32_16x16x32_bf16 a[84:87], a[196:199], v[72:75], a[84:87]\n"
  "s_add_u32 s41, s41, 16384\n"
  "v_exp_f32 v13, v13\n"
  "v_mfma_f32_16x16x32_bf16 a[84:87], a[228:231], v[104:107], a[84:87]\n"
  "v_cvt_pk_bf16_f32 v10, v12, v13\n"
  "v_mfma_f32_16x16x32_bf16 a[100:103], a[200:203], v[72:75], a[100:103]\n"
  "v_exp_f32 v14, v14\n"
  "v_mfma_f32_16x16x32_bf16 a[100:103], a[232:235], v[104:107], a[100:103]\n"
  "v_mfma_f32_16x16x32_bf16 a[116:119], a[204:207], v[72:75], a[116:119]\n"
  "v_exp_f32 v15, v15\n"
  "v_mfma_f32_16x16x32_bf16 a[116:119], a[236:239], v[104:107], a[116:119]\n"
  "v_cvt_pk_bf16_f32 v11, v14, v15\n"
  "v_mfma_f32_16x16x32_bf16 a[132:135], v[208:211], v[72:75], a[132:135]\n"
  "v_exp_f32 v16, v16\n"
  "v_mfma_f32_16x16x32_bf16 a[132:135], v[208:211], v[104:107], a[132:135]\n"
  "v_mfma_f32_16x16x32_bf16 a[8:11], a[176:179], v[80:83], a[8:11]\n"
  "v_exp_f32 v17, v17\n"
  "v_mfma_f32_16x16x32_bf16 a[8:11], a[208:211], v[112:115], a[8:11]\n"
  "v_cvt_pk_bf16_f32 v16, v16, v17\n"
  "v_mfma_f32_16x16x32_bf16 a[24:27], a[180:183], v[80:83], a[24:27]\n"
  "s_add_u32 m0, s42, 49152\n"
  "v_exp_f32 v18, v18\n"
  "v_mfma_f32_16x16x32_bf16 a[24:27], a[212:215], v[112:115], a[24:27]\n"
  "buffer_load_dwordx4 v228, s[20:23], s40 offen offset:0 nt lds\n"
  "v_mfma_f32_16x16x32_bf16 a[40:43], a[184:187], v[80:83], a[40:43]\n"
  "v_exp_f32 v19, v19\n"
  "v_mfma_f32_16x16x32_bf16 a[40:43], a[216:219], v[112:115], a[40:43]\n"
  "v_cvt_pk_bf16_f32 v17, v18, v19\n"
  "v_mfma_f32_16x16x32_bf16 a[56:59], a[188:191], v[80:83], a[56:59]\n"
  "v_exp_f32 v20, v20\n"
  "v_mfma_f32_16x16x32_bf16 a[56:59], a[220:223], v[112:115], a[56:59]\n"
  "buffer_load_dwordx4 v229, s[20:23], s40 offen offset:1024 nt lds\n"
  "v_mfma_f32_16x16x32_bf16 a[72:75], a[192:195], v[80:83], a[72:75]\n"
  "v_exp_f32 v21, v21\n"
  "v_mfma_f32_16x16x32_bf16 a[72:75], a[224:227], v[112:115], a[72:75]\n"
  "v_cvt_pk_bf16_f32 v18, v20, v21\n"
  "v_mfma_f32_16x16x32_bf16 a[88:91], a[196:199], v[80:83], a[88:91]\n"
  "v_exp_f32 v22, v22\n"
  "v_mfma_f32_16x16x32_bf16 a[88:91], a[228:231], v[112:115], a[88:91]\n"
  "buffer_load_dwordx4 v230, s[20:23], s40 offen offset:2048 nt lds\n"
  "v_mfma_f32_16x16x32_bf16 a[104:107], a[200:203], v[80:83], a[104:107]\n"
  "v_exp_f32 v23, v23\n"
  "v_mfma_f32_16x16x32_bf16 a[104:107], a[232:235], v[112:115], a[104:107]\n"
  "v_cvt_pk_bf16_f32 v19, v22, v23\n"
  "v_mfma_f32_16x16x32_bf16 a[120:123], a[204:207], v[80:83], a[120:123]\n"
  "v_exp_f32 v24, v24\n"
  "v_mfma_f32_16x16x32_bf16 a[120:123], a[236:239], v[112:115], a[120:123]\n"
  "buffer_load_dwordx4 v231, s[20:23], s40 offen offset:3072 nt lds\n"
  "v_mfma_f32_16x16x32_bf16 a[136:139], v[208:211], v[80:83], a[136:139]\n"
  "s_add_u32 s40, s40, 16384\n"
  "v_exp_f32 v25, v25\n"
  "v_mfma_f32_16x16x32_bf16 a[136:139], v[208:211], v[112:115], a[136:139]\n"
  "v_cvt_pk_bf16_f32 v24, v24, v25\n"
  "v_mfma_f32_16x16x32_bf16 a[12:15], a[176:179], v[88:91], a[12:15]\n"
  "v_exp_f32 v26, v26\n"
  "v_mfma_f32_16x16x32_bf16 a[12:15], a[208:211], v[120:123], a[12:15]\n"
  "v_mfma_f32_16x16x32_bf16 a[28:31], a[180:183], v[88:91], a[28:31]\n"
  "v_exp_f32 v27, v27\n"
  "v_mfma_f32_16x16x32_bf16 a[28:31], a[212:215], v[120:123], a[28:31]\n"
  "v_cvt_pk_bf16_f32 v25, v26, v27\n"
  "v_mfma_f32_16x16x32_bf16 a[44:47], a[184:187], v[88:91], a[44:47]\n"
  "v_exp_f32 v28, v28\n"
  "v_mfma_f32_16x16x32_bf16 a[44:47], a[216:219], v[120:123], a[44:47]\n"
  "v_mfma_f32_16x16x32_bf16 a[60:63], a[188:191], v[88:91], a[60:63]\n"
  "v_exp_f32 v29, v29\n"
  "v_mfma_f32_16x16x32_bf16 a[60:63], a[220:223], v[120:123], a[60:63]\n"
  "v_cvt_pk_bf16_f32 v26, v28, v29\n"
  "v_mfma_f32_16x16x32_bf16 a[76:79], a[192:195], v[88:91], a[76:79]\n"
  "v_exp_f32 v30, v30\n"
  "v_mfma_f32_16x16x32_bf16 a[76:79], a[224:227], v[120:123], a[76:79]\n"
  "v_mfma_f32_16x16x32_bf16 a[92:95], a[196:199], v[88:91], a[92:95]\n"
  "v_exp_f32 v31, v31\n"
  "v_mfma_f32_16x16x32_bf16 a[92:95], a[228:231], v[120:123], a[92:95]\n"
  "v_cvt_pk_bf16_f32 v27, v30, v31\n"
  "v_mfma_f32_16x16x32_bf16 a[108:111], a[200:203], v[88:91], a[108:111]\n"
  "v_exp_f32 v32, v32\n"
  "v_mfma_f32_16x16x32_bf16 a[108:111], a[232:235], v[120:123], a[108:111]\n"
  "v_mfma_f32_16x16x32_bf16 a[124:127], a[204:207], v[88:91], a[124:127]\n"
  "v_exp_f32 v33, v33\n"
  "v_mfma_f32_16x16x32_bf16 a[124:127], a[236:239], v[120:123], a[124:127]\n"
  "v_cvt_pk_bf16_f32 v32, v32, v33\n"
  "v_mfma_f32_16x16x32_bf16 a[140:143], v[208:211], v[88:91], a[140:143]\n"
  "v_exp_f32 v34, v34\n"
  "v_mfma_f32_16x16x32_bf16 a[140:143], v[208:211], v[120:123], a[140:143]\n"
  "s_waitcnt lgkmcnt(0)\n"
  "v_mfma_f32_16x16x32_bf16 v[64:67], a[144:147], v[128:131], v[192:195]\n"
  "v_exp_f32 v35, v35\n"
  "v_mfma_f32_16x16x32_bf16 v[64:67], a[148:151], v[132:135], v[64:67]\n"
  "ds_read_b64_tr_b16 a[176:177], v216 offset:0\n"
  "v_cvt_pk_bf16_f32 v33, v34, v35\n"
  "v_mfma_f32_16x16x32_bf16 v[64:67], a[152:155], v[136:139], v[64:67]\n"
  "v_exp_f32 v36, v36\n"
  "v_mfma_f32_16x16x32_bf16 v[64:67], a[156:159], v[140:143], v[64:67]\n"
  "ds_read_b64_tr_b16 a[178:179], v216 offset:4096\n"
  "ds_read_b64_tr_b16 a[208:209], v216 offset:8192\n"
  "v_mfma_f32_16x16x32_bf16 v[72:75], a[144:147], v[144:147], v[196:199]\n"
  "v_exp_f32 v37, v37\n"
  "v_mfma_f32_16x16x32_bf16 v[72:75], a[148:151], v[148:151], v[72:75]\n"
  "ds_read_b64_tr_b16 a[210:211], v216 offset:12288\n"
  "v_cvt_pk_bf16_f32 v34, v36, v37\n"
  "v_mfma_f32_16x16x32_bf16 v[72:75], a[152:155], v[152:155], v[72:75]\n"
  "v_exp_f32 v38, v38\n"
  "v_mfma_f32_16x16x32_bf16 v[72:75], a[156:159], v[156:159], v[72:75]\n"
  "ds_read_b64_tr_b16 a[180:181], v217 offset:0\n"
  "ds_read_b64_tr_b16 a[182:183], v217 offset:4096\n"
  "v_mfma_f32_16x16x32_bf16 v[80:83], a[144:147], v[160:163], v[200:203]\n"
  "v_exp_f32 v39, v39\n"
  "v_mfma_f32_16x16x32_bf16 v[80:83], a[148:151], v[164:167], v[80:83]\n"
  "ds_read_b64_tr_b16 a[212:213], v217 offset:8192\n"
  "v_cvt_pk_bf16_f32 v35, v38, v39\n"
  "v_mfma_f32_16x16x32_bf16 v[80:83], a[152:155], v[168:171], v[80:83]\n"
  "v_exp_f32 v40, v40\n"
  "v_mfma_f32_16x16x32_bf16 v[80:83], a[156:159], v[172:175], v[80:83]\n"
  "ds_read_b64_tr_b16 a[214:215], v217 offset:12288\n"
  "ds_read_b64_tr_b16 a[184:185], v218 offset:0\n"
  "v_mfma_f32_16x16x32_bf16 v[88:91], a[144:147], v[176:179], v[204:207]\n"
  "v_exp_f32 v41, v41\n"
  "v_mfma_f32_16x16x32_bf16 v[88:91], a[148:151], v[180:183], v[88:91]\n"
  "ds_read_b64_tr_b16 a[186:187], v218 offset:4096\n"
  "v_cvt_pk_bf16_f32 v40, v40, v41\n"
  "v_mfma_f32_16x16x32_bf16 v[88:91], a[152:155], v[184:187], v[88:91]\n"
  "v_exp_f32 v42, v42\n"
  "v_mfma_f32_16x16x32_bf16 v[88:91], a[156:159], v[188:191], v[88:91]\n"
  "ds_read_b64_tr_b16 a[216:217], v218 offset:8192\n"
  "ds_read_b64_tr_b16 a[218:219], v218 offset:12288\n"
  "v_mfma_f32_16x16x32_bf16 v[68:71], a[160:163], v[128:131], v[192:195]\n"
  "v_exp_f32 v43, v43\n"
  "v_mfma_f32_16x16x32_bf16 v[68:71], a[164:167], v[132:135], v[68:71]\n"
  "ds_read_b128 a[144:147], v224 offset:24576\n"
  "v_cvt_pk_bf16_f32 v41, v42, v43\n"
  "v_mfma_f32_16x16x32_bf16 v[68:71], a[168:171], v[136:139], v[68:71]\n"
  "v_exp_f32 v44, v44\n"
  "v_mfma_f32_16x16x32_bf16 v[68:71], a[172:175], v[140:143], v[68:71]\n"
  "ds_read_b128 a[148:151], v225 offset:24576\n"
  "ds_read_b128 a[152:155], v226 offset:24576\n"
  "v_mfma_f32_16x16x32_bf16 v[76:79], a[160:163], v[144:147], v[196:199]\n"
  "v_exp_f32 v45, v45\n"
  "v_mfma_f32_16x16x32_bf16 v[76:79], a[164:167], v[148:151], v[76:79]\n"
  "ds_read_b128 a[156:159], v227 offset:24576\n"
  "v_cvt_pk_bf16_f32 v42, v44, v45\n"
  "v_mfma_f32_16x16x32_bf16 v[76:79], a[168:171], v[152:155], v[76:79]\n"
  "v_exp_f32 v46, v46\n"
  "v_mfma_f32_16x16x32_bf16 v[76:79], a[172:175], v[156:159], v[76:79]\n"
  "ds_read_b64_tr_b16 a[188:189], v219 offset:0\n"
  "ds_read_b64_tr_b16 a[190:191], v219 offset:4096\n"
  "v_mfma_f32_16x16x32_bf16 v[84:87], a[160:163], v[160:163], v[200:203]\n"
  "v_exp_f32 v47, v47\n"
  "v_mfma_f32_16x16x32_bf16 v[84:87], a[164:167], v[164:167], v[84:87]\n"
  "ds_read_b64_tr_b16 a[220:221], v219 offset:8192\n"
  "v_cvt_pk_bf16_f32 v43, v46, v47\n"
  "v_mfma_f32_16x16x32_bf16 v[84:87], a[168:171], v[168:171], v[84:87]\n"
  "v_exp_f32 v48, v48\n"
  "v_mfma_f32_16x16x32_bf16 v[84:87], a[172:175], v[172:175], v[84:87]\n"
  "ds_read_b64_tr_b16 a[222:223], v219 offset:12288\n"
  "ds_read_b64_tr_b16 a[192:193], v220 offset:0\n"
  "v_mfma_f32_16x16x32_bf16 v[92:95], a[160:163], v[176:179], v[204:207]\n"
  "v_exp_f32 v49, v49\n"
  "v_mfma_f32_16x16x32_bf16 v[92:95], a[164:167], v[180:183], v[92:95]\n"
  "ds_read_b64_tr_b16 a[194:195], v220 offset:4096\n"
  "v_cvt_pk_bf16_f32 v48, v48, v49\n"
  "v_mfma_f32_16x16x32_bf16 v[92:95], a[168:171], v[184:187], v[92:95]\n"
  "v_exp_f32 v50, v50\n"
  "v_mfma_f32_16x16x32_bf16 v[92:95], a[172:175], v[188:191], v[92:95]\n"
  "s_waitcnt lgkmcnt(6)\n"
  "ds_read_b64_tr_b16 a[224:225], v220 offset:8192\n"
  "ds_read_b64_tr_b16 a[226:227], v220 offset:12288\n"
  "v_mfma_f32_16x16x32_bf16 v[96:99], a[144:147], v[128:131], v[192:195]\n"
  "v_exp_f32 v51, v51\n"
  "v_mfma_f32_16x16x32_bf16 v[96:99], a[148:151], v[132:135], v[96:99]\n"
  "ds_read_b128 a[160:163], v224 offset:28672\n"
  "v_cvt_pk_bf16_f32 v49, v50, v51\n"
  "v_mfma_f32_16x16x32_bf16 v[96:99], a[152:155], v[136:139], v[96:99]\n"
  "v_exp_f32 v52, v52\n"
  "v_mfma_f32_16x16x32_bf16 v[96:99], a[156:159], v[140:143], v[96:99]\n"
  "ds_read_b128 a[164:167], v225 offset:28672\n"
  "ds_read_b128 a[168:171], v226 offset:28672\n"
  "v_mfma_f32_16x16x32_bf16 v[104:107], a[144:147], v[144:147], v[196:199]\n"
  "v_exp_f32 v53, v53\n"
  "v_mfma_f32_16x16x32_bf16 v[104:107], a[148:151], v[148:151], v[104:107]\n"
  "ds_read_b128 a[172:175], v227 offset:28672\n"
  "v_cvt_pk_bf16_f32 v50, v52, v53\n"
  "v_mfma_f32_16x16x32_bf16 v[104:107], a[152:155], v[152:155], v[104:107]\n"
  "v_exp_f32 v54, v54\n"
  "v_mfma_f32_16x16x32_bf16 v[104:107], a[156:159], v[156:159], v[104:107]\n"
  "ds_read_b64_tr_b16 a[196:197], v221 offset:0\n"
  "ds_read_b64_tr_b16 a[198:199], v221 offset:4096\n"
  "v_mfma_f32_16x16x32_bf16 v[112:115], a[144:147], v[160:163], v[200:203]\n"
  "v_exp_f32 v55, v55\n"
  "v_mfma_f32_16x16x32_bf16 v[112:115], a[148:151], v[164:167], v[112:115]\n"
  "ds_read_b64_tr_b16 a[228:229], v221 offset:8192\n"
  "v_cvt_pk_bf16_f32 v51, v54, v55\n"
  "v_mfma_f32_16x16x32_bf16 v[112:115], a[152:155], v[168:171], v[112:115]\n"
  "v_exp_f32 v56, v56\n"
  "v_mfma_f32_16x16x32_bf16 v[112:115], a[156:159], v[172:175], v[112:115]\n"
  "ds_read_b64_tr_b16 a[230:231], v221 offset:12288\n"
  "ds_read_b64_tr_b16 a[200:201], v222 offset:0\n"
  "v_mfma_f32_16x16x32_bf16 v[120:123], a[144:147], v[176:179], v[204:207]\n"
  "v_exp_f32 v57, v57\n"
  "v_mfma_f32_16x16x32_bf16 v[120:123], a[148:151], v[180:183], v[120:123]\n"
  "ds_read_b64_tr_b16 a[202:203], v222 offset:4096\n"
  "v_cvt_pk_bf16_f32 v56, v56, v57\n"
  "v_mfma_f32_16x16x32_bf16 v[120:123], a[152:155], v[184:187], v[120:123]\n"
  "v_exp_f32 v58, v58\n"
  "v_mfma_f32_16x16x32_bf16 v[120:123], a[156:159], v[188:191], v[120:123]\n"
  "s_waitcnt lgkmcnt(6)\n"
  "ds_read_b64_tr_b16 a[232:233], v222 offset:8192\n"
  "ds_read_b64_tr_b16 a[234:235], v222 offset:12288\n"
  "v_mfma_f32_16x16x32_bf16 v[100:103], a[160:163], v[128:131], v[192:195]\n"
  "v_exp_f32 v59, v59\n"
  "v_mfma_f32_16x16x32_bf16 v[100:103], a[164:167], v[132:135], v[100:103]\n"
  "v_cvt_pk_bf16_f32 v57, v58, v59\n"
  "v_mfma_f32_16x16x32_bf16 v[100:103], a[168:171], v[136:139], v[100:103]\n"
  "v_exp_f32 v60, v60\n"
  "v_mfma_f32_16x16x32_bf16 v[100:103], a[172:175], v[140:143], v[100:103]\n"
  "ds_read_b64_tr_b16 a[204:205], v223 offset:0\n"
  "ds_read_b64_tr_b16 a[206:207], v223 offset:4096\n"
  "v_mfma_f32_16x16x32_bf16 v[108:111], a[160:163], v[144:147], v[196:199]\n"
  "v_exp_f32 v61, v61\n"
  "v_mfma_f32_16x16x32_bf16 v[108:111], a[164:167], v[148:151], v[108:111]\n"
  "v_cvt_pk_bf16_f32 v58, v60, v61\n"
  "v_mfma_f32_16x16x32_bf16 v[108:111], a[168:171], v[152:155], v[108:111]\n"
  "v_exp_f32 v62, v62\n"
  "v_mfma_f32_16x16x32_bf16 v[108:111], a[172:175], v[156:159], v[108:111]\n"
  "ds_read_b64_tr_b16 a[236:237], v223 offset:8192\n"
  "ds_read_b64_tr_b16 a[238:239], v223 offset:12288\n"
  "v_mfma_f32_16x16x32_bf16 v[116:119], a[160:163], v[160:163], v[200:203]\n"
  "v_exp_f32 v63, v63\n"
  "v_mfma_f32_16x16x32_bf16 v[116:119], a[164:167], v[164:167], v[116:119]\n"
  "v_cvt_pk_bf16_f32 v59, v62, v63\n"
  "v_mfma_f32_16x16x32_bf16 v[116:119], a[168:171], v[168:171], v[116:119]\n"
  "v_mfma_f32_16x16x32_bf16 v[116:119], a[172:175], v[172:175], v[116:119]\n"
  "v_mfma_f32_16x16x32_bf16 v[124:127], a[160:163], v[176:179], v[204:207]\n"
  "v_mfma_f32_16x16x32_bf16 v[124:127], a[164:167], v[180:183], v[124:127]\n"
  "v_mfma_f32_16x16x32_bf16 v[124:127], a[168:171], v[184:187], v[124:127]\n"
  "v_mfma_f32_16x16x32_bf16 v[124:127], a[172:175], v[188:191], v[124:127]\n"
  "s_waitcnt vmcnt(4) lgkmcnt(0)\n"
  "s_barrier\n"
  "v_mfma_f32_16x16x32_bf16 a[0:3], a[176:179], v[0:3], a[0:3]\n"
  "s_add_u32 m0, s42, 98304\n"
  "v_mfma_f32_16x16x32_bf16 a[0:3], a[208:211], v[32:35], a[0:3]\n"
  "buffer_load_dwordx4 v232, s[24:27], s41 offen offset:0 nt lds\n"
  "v_mfma_f32_16x16x32_bf16 a[16:19], a[180:183], v[0:3], a[16:19]\n"
  "v_exp_f32 v64, v64\n"
  "v_mfma_f32_16x16x32_bf16 a[16:19], a[212:215], v[32:35], a[16:19]\n"
  "buffer_load_dwordx4 v233, s[24:27], s41 offen offset:1024 nt lds\n"
  "v_mfma_f32_16x16x32_bf16 a[32:35], a[184:187], v[0:3], a[32:35]\n"
  "v_exp_f32 v65, v65\n"
  "v_mfma_f32_16x16x32_bf16 a[32:35], a[216:219], v[32:35], a[32:35]\n"
  "v_cvt_pk_bf16_f32 v64, v64, v65\n"
  "v_mfma_f32_16x16x32_bf16 a[48:51], a[188:191], v[0:3], a[48:51]\n"
  "v_exp_f32 v66, v66\n"
  "v_mfma_f32_16x16x32_bf16 a[48:51], a[220:223], v[32:35], a[48:51]\n"
  "ds_read_b128 a[144:147], v224 offset:32768\n"
  "v_mfma_f32_16x16x32_bf16 a[64:67], a[192:195], v[0:3], a[64:67]\n"
  "v_exp_f32 v67, v67\n"
  "v_mfma_f32_16x16x32_bf16 a[64:67], a[224:227], v[32:35], a[64:67]\n"
  "ds_read_b128 a[148:151], v225 offset:32768\n"
  "v_cvt_pk_bf16_f32 v65, v66, v67\n"
  "v_mfma_f32_16x16x32_bf16 a[80:83], a[196:199], v[0:3], a[80:83]\n"
  "v_exp_f32 v68, v68\n"
  "v_mfma_f32_16x16x32_bf16 a[80:83], a[228:231], v[32:35], a[80:83]\n"
  "ds_read_b128 a[152:155], v226 offset:32768\n"
  "v_mfma_f32_16x16x32_bf16 a[96:99], a[200:203], v[0:3], a[96:99]\n"
  "v_exp_f32 v69, v69\n"
  "v_mfma_f32_16x16x32_bf16 a[96:99], a[232:235], v[32:35], a[96:99]\n"
  "ds_read_b128 a[156:159], v227 offset:32768\n"
  "v_cvt_pk_bf16_f32 v66, v68, v69\n"
  "v_mfma_f32_16x16x32_bf16 a[112:115], a[204:207], v[0:3], a[112:115]\n"
  "v_exp_f32 v70, v70\n"
  "v_mfma_f32_16x16x32_bf16 a[112:115], a[236:239], v[32:35], a[112:115]\n"
  "ds_read_b128 a[160:163], v224 offset:36864\n"
  "v_mfma_f32_16x16x32_bf16 a[128:131], v[208:211], v[0:3], a[128:131]\n"
  "v_exp_f32 v71, v71\n"
  "v_mfma_f32_16x16x32_bf16 a[128:131], v[208:211], v[32:35], a[128:131]\n"
  "ds_read_b128 a[164:167], v225 offset:36864\n"
  "v_cvt_pk_bf16_f32 v67, v70, v71\n"
  "v_mfma_f32_16x16x32_bf16 a[4:7], a[176:179], v[8:11], a[4:7]\n"
  "v_exp_f32 v72, v72\n"
  "v_mfma_f32_16x16x32_bf16 a[4:7], a[208:211], v[40:43], a[4:7]\n"
  "ds_read_b128 a[168:171], v226 offset:36864\n"
  "v_mfma_f32_16x16x32_bf16 a[20:23], a[180:183], v[8:11], a[20:23]\n"
  "v_exp_f32 v73, v73\n"
  "v_mfma_f32_16x16x32_bf16 a[20:23], a[212:215], v[40:43], a[20:23]\n"
  "ds_read_b128 a[172:175], v227 offset:36864\n"
  "v_cvt_pk_bf16_f32 v72, v72, v73\n"
  "v_mfma_f32_16x16x32_bf16 a[36:39], a[184:187], v[8:11], a[36:39]\n"
  "v_exp_f32 v74, v74\n"
  "v_mfma_f32_16x16x32_bf16 a[36:39], a[216:219], v[40:43], a[36:39]\n"
  "buffer_load_dwordx4 v234, s[24:27], s41 offen offset:2048 nt lds\n"
  "v_mfma_f32_16x16x32_bf16 a[52:55], a[188:191], v[8:11], a[52:55]\n"
  "v_exp_f32 v75, v75\n"
  "v_mfma_f32_16x16x32_bf16 a[52:55], a[220:223], v[40:43], a[52:55]\n"
  "v_cvt_pk_bf16_f32 v73, v74, v75\n"
  "v_mfma_f32_16x16x32_bf16 a[68:71], a[192:195], v[8:11], a[68:71]\n"
  "v_exp_f32 v76, v76\n"
  "v_mfma_f32_16x16x32_bf16 a[68:71], a[224:227], v[40:43], a[68:71]\n"
  "buffer_load_dwordx4 v235, s[24:27], s41 offen offset:3072 nt lds\n"
  "v_mfma_f32_16x16x32_bf16 a[84:87], a[196:199], v[8:11], a[84:87]\n"
  "s_add_u32 s41, s41, 16384\n"
  "v_exp_f32 v77, v77\n"
  "v_mfma_f32_16x16x32_bf16 a[84:87], a[228:231], v[40:43], a[84:87]\n"
  "v_cvt_pk_bf16_f32 v74, v76, v77\n"
  "v_mfma_f32_16x16x32_bf16 a[100:103], a[200:203], v[8:11], a[100:103]\n"
  "v_exp_f32 v78, v78\n"
  "v_mfma_f32_16x16x32_bf16 a[100:103], a[232:235], v[40:43], a[100:103]\n"
  "v_mfma_f32_16x16x32_bf16 a[116:119], a[204:207], v[8:11], a[116:119]\n"
  "v_exp_f32 v79, v79\n"
  "v_mfma_f32_16x16x32_bf16 a[116:119], a[236:239], v[40:43], a[116:119]\n"
  "v_cvt_pk_bf16_f32 v75, v78, v79\n"
  "v_mfma_f32_16x16x32_bf16 a[132:135], v[208:211], v[8:11], a[132:135]\n"
  "v_exp_f32 v80, v80\n"
  "v_mfma_f32_16x16x32_bf16 a[132:135], v[208:211], v[40:43], a[132:135]\n"
  "v_mfma_f32_16x16x32_bf16 a[8:11], a[176:179], v[16:19], a[8:11]\n"
  "v_exp_f32 v81, v81\n"
  "v_mfma_f32_16x16x32_bf16 a[8:11], a[208:211], v[48:51], a[8:11]\n"
  "v_cvt_pk_bf16_f32 v80, v80, v81\n"
  "v_mfma_f32_16x16x32_bf16 a[24:27], a[180:183], v[16:19], a[24:27]\n"
  "s_add_u32 m0, s42, 0\n"
  "s_add_u32 s46, s52, 65536\n"
  "v_exp_f32 v82, v82\n"
  "v_mfma_f32_16x16x32_bf16 a[24:27], a[212:215], v[48:51], a[24:27]\n"
  "buffer_load_dwordx4 v244, s[76:79], s46 offen offset:0 lds\n"
  "v_mfma_f32_16x16x32_bf16 a[40:43], a[184:187], v[16:19], a[40:43]\n"
  "v_exp_f32 v83, v83\n"
  "v_mfma_f32_16x16x32_bf16 a[40:43], a[216:219], v[48:51], a[40:43]\n"
  "v_cvt_pk_bf16_f32 v81, v82, v83\n"
  "v_mfma_f32_16x16x32_bf16 a[56:59], a[188:191], v[16:19], a[56:59]\n"
  "v_exp_f32 v84, v84\n"
  "v_mfma_f32_16x16x32_bf16 a[56:59], a[220:223], v[48:51], a[56:59]\n"
  "buffer_load_dwordx4 v245, s[76:79], s46 offen offset:1024 lds\n"
  "v_mfma_f32_16x16x32_bf16 a[72:75], a[192:195], v[16:19], a[72:75]\n"
  "v_exp_f32 v85, v85\n"
  "v_mfma_f32_16x16x32_bf16 a[72:75], a[224:227], v[48:51], a[72:75]\n"
  "v_cvt_pk_bf16_f32 v82, v84, v85\n"
  "v_mfma_f32_16x16x32_bf16 a[88:91], a[196:199], v[16:19], a[88:91]\n"
  "v_exp_f32 v86, v86\n"
  "v_mfma_f32_16x16x32_bf16 a[88:91], a[228:231], v[48:51], a[88:91]\n"
  "buffer_load_dwordx4 v246, s[76:79], s46 offen offset:2048 lds\n"
  "v_mfma_f32_16x16x32_bf16 a[104:107], a[200:203], v[16:19], a[104:107]\n"
  "v_exp_f32 v87, v87\n"
  "v_mfma_f32_16x16x32_bf16 a[104:107], a[232:235], v[48:51], a[104:107]\n"
  "v_cvt_pk_bf16_f32 v83, v86, v87\n"
  "v_mfma_f32_16x16x32_bf16 a[120:123], a[204:207], v[16:19], a[120:123]\n"
  "v_exp_f32 v88, v88\n"
  "v_mfma_f32_16x16x32_bf16 a[120:123], a[236:239], v[48:51], a[120:123]\n"
  "buffer_load_dwordx4 v247, s[76:79], s46 offen offset:3072 lds\n"
  "v_mfma_f32_16x16x32_bf16 a[136:139], v[208:211], v[16:19], a[136:139]\n"
  "v_exp_f32 v89, v89\n"
  "v_mfma_f32_16x16x32_bf16 a[136:139], v[208:211], v[48:51], a[136:139]\n"
  "v_cvt_pk_bf16_f32 v88, v88, v89\n"
  "v_mfma_f32_16x16x32_bf16 a[12:15], a[176:179], v[24:27], a[12:15]\n"
  "v_exp_f32 v90, v90\n"
  "v_mfma_f32_16x16x32_bf16 a[12:15], a[208:211], v[56:59], a[12:15]\n"
  "v_mfma_f32_16x16x32_bf16 a[28:31], a[180:183], v[24:27], a[28:31]\n"
  "v_exp_f32 v91, v91\n"
  "v_mfma_f32_16x16x32_bf16 a[28:31], a[212:215], v[56:59], a[28:31]\n"
  "v_cvt_pk_bf16_f32 v89, v90, v91\n"
  "v_mfma_f32_16x16x32_bf16 a[44:47], a[184:187], v[24:27], a[44:47]\n"
  "v_exp_f32 v92, v92\n"
  "v_mfma_f32_16x16x32_bf16 a[44:47], a[216:219], v[56:59], a[44:47]\n"
  "v_mfma_f32_16x16x32_bf16 a[60:63], a[188:191], v[24:27], a[60:63]\n"
  "v_exp_f32 v93, v93\n"
  "v_mfma_f32_16x16x32_bf16 a[60:63], a[220:223], v[56:59], a[60:63]\n"
  "v_cvt_pk_bf16_f32 v90, v92, v93\n"
  "v_mfma_f32_16x16x32_bf16 a[76:79], a[192:195], v[24:27], a[76:79]\n"
  "v_exp_f32 v94, v94\n"
  "v_mfma_f32_16x16x32_bf16 a[76:79], a[224:227], v[56:59], a[76:79]\n"
  "v_mfma_f32_16x16x32_bf16 a[92:95], a[196:199], v[24:27], a[92:95]\n"
  "v_exp_f32 v95, v95\n"
  "v_mfma_f32_16x16x32_bf16 a[92:95], a[228:231], v[56:59], a[92:95]\n"
  "v_cvt_pk_bf16_f32 v91, v94, v95\n"
  "v_mfma_f32_16x16x32_bf16 a[108:111], a[200:203], v[24:27], a[108:111]\n"
  "v_exp_f32 v96, v96\n"
  "v_mfma_f32_16x16x32_bf16 a[108:111], a[232:235], v[56:59], a[108:111]\n"
  "v_mfma_f32_16x16x32_bf16 a[124:127], a[204:207], v[24:27], a[124:127]\n"
  "v_exp_f32 v97, v97\n"
  "v_mfma_f32_16x16x32_bf16 a[124:127], a[236:239], v[56:59], a[124:127]\n"
  "v_cvt_pk_bf16_f32 v96, v96, v97\n"
  "v_mfma_f32_16x16x32_bf16 a[140:143], v[208:211], v[24:27], a[140:143]\n"
  "v_exp_f32 v98, v98\n"
  "v_mfma_f32_16x16x32_bf16 a[140:143], v[208:211], v[56:59], a[140:143]\n"
  "s_waitcnt lgkmcnt(0)\n"
  "v_mfma_f32_16x16x32_bf16 v[0:3], a[144:147], v[128:131], v[192:195]\n"
  "v_exp_f32 v99, v99\n"
  "v_mfma_f32_16x16x32_bf16 v[0:3], a[148:151], v[132:135], v[0:3]\n"
  "ds_read_b64_tr_b16 a[176:177], v216 offset:16384\n"
  "v_cvt_pk_bf16_f32 v97, v98, v99\n"
  "v_mfma_f32_16x16x32_bf16 v[0:3], a[152:155], v[136:139], v[0:3]\n"
  "v_exp_f32 v100, v100\n"
  "v_mfma_f32_16x16x32_bf16 v[0:3], a[156:159], v[140:143], v[0:3]\n"
  "ds_read_b64_tr_b16 a[178:179], v216 offset:20480\n"
  "ds_read_b64_tr_b16 a[208:209], v216 offset:24576\n"
  "v_mfma_f32_16x16x32_bf16 v[8:11], a[144:147], v[144:147], v[196:199]\n"
  "v_exp_f32 v101, v101\n"
  "v_mfma_f32_16x16x32_bf16 v[8:11], a[148:151], v[148:151], v[8:11]\n"
  "ds_read_b64_tr_b16 a[210:211], v216 offset:28672\n"
  "v_cvt_pk_bf16_f32 v98, v100, v101\n"
  "v_mfma_f32_16x16x32_bf16 v[8:11], a[152:155], v[152:155], v[8:11]\n"
  "v_exp_f32 v102, v102\n"
  "v_mfma_f32_16x16x32_bf16 v[8:11], a[156:159], v[156:159], v[8:11]\n"
  "ds_read_b64_tr_b16 a[180:181], v217 offset:16384\n"
  "ds_read_b64_tr_b16 a[182:183], v217 offset:20480\n"
  "v_mfma_f32_16x16x32_bf16 v[16:19], a[144:147], v[160:163], v[200:203]\n"
  "v_exp_f32 v103, v103\n"
  "v_mfma_f32_16x16x32_bf16 v[16:19], a[148:151], v[164:167], v[16:19]\n"
  "ds_read_b64_tr_b16 a[212:213], v217 offset:24576\n"
  "v_cvt_pk_bf16_f32 v99, v102, v103\n"
  "v_mfma_f32_16x16x32_bf16 v[16:19], a[152:155], v[168:171], v[16:19]\n"
  "v_exp_f32 v104, v104\n"
  "v_mfma_f32_16x16x32_bf16 v[16:19], a[156:159], v[172:175], v[16:19]\n"
  "ds_read_b64_tr_b16 a[214:215], v217 offset:28672\n"
  "ds_read_b64_tr_b16 a[184:185], v218 offset:16384\n"
  "v_mfma_f32_16x16x32_bf16 v[24:27], a[144:147], v[176:179], v[204:207]\n"
  "v_exp_f32 v105, v105\n"
  "v_mfma_f32_16x16x32_bf16 v[24:27], a[148:151], v[180:183], v[24:27]\n"
  "ds_read_b64_tr_b16 a[186:187], v218 offset:20480\n"
  "v_cvt_pk_bf16_f32 v104, v104, v105\n"
  "v_mfma_f32_16x16x32_bf16 v[24:27], a[152:155], v[184:187], v[24:27]\n"
  "v_exp_f32 v106, v106\n"
  "v_mfma_f32_16x16x32_bf16 v[24:27], a[156:159], v[188:191], v[24:27]\n"
  "ds_read_b64_tr_b16 a[216:217], v218 offset:24576\n"
  "ds_read_b64_tr_b16 a[218:219], v218 offset:28672\n"
  "v_mfma_f32_16x16x32_bf16 v[4:7], a[160:163], v[128:131], v[192:195]\n"
  "v_exp_f32 v107, v107\n"
  "v_mfma_f32_16x16x32_bf16 v[4:7], a[164:167], v[132:135], v[4:7]\n"
  "ds_read_b128 a[144:147], v224 offset:40960\n"
  "v_cvt_pk_bf16_f32 v105, v106, v107\n"
  "v_mfma_f32_16x16x32_bf16 v[4:7], a[168:171], v[136:139], v[4:7]\n"
  "v_exp_f32 v108, v108\n"
  "v_mfma_f32_16x16x32_bf16 v[4:7], a[172:175], v[140:143], v[4:7]\n"
  "ds_read_b128 a[148:151], v225 offset:40960\n"
  "ds_read_b128 a[152:155], v226 offset:40960\n"
  "v_mfma_f32_16x16x32_bf16 v[12:15], a[160:163], v[144:147], v[196:199]\n"
  "v_exp_f32 v109, v109\n"
  "v_mfma_f32_16x16x32_bf16 v[12:15], a[164:167], v[148:151], v[12:15]\n"
  "ds_read_b128 a[156:159], v227 offset:40960\n"
  "v_cvt_pk_bf16_f32 v106, v108, v109\n"
  "v_mfma_f32_16x16x32_bf16 v[12:15], a[168:171], v[152:155], v[12:15]\n"
  "v_exp_f32 v110, v110\n"
  "v_mfma_f32_16x16x32_bf16 v[12:15], a[172:175], v[156:159], v[12:15]\n"
  "ds_read_b64_tr_b16 a[188:189], v219 offset:16384\n"
  "ds_read_b64_tr_b16 a[190:191], v219 offset:20480\n"
  "v_mfma_f32_16x16x32_bf16 v[20:23], a[160:163], v[160:163], v[200:203]\n"
  "v_exp_f32 v111, v111\n"
  "v_mfma_f32_16x16x32_bf16 v[20:23], a[164:167], v[164:167], v[20:23]\n"
  "ds_read_b64_tr_b16 a[220:221], v219 offset:24576\n"
  "v_cvt_pk_bf16_f32 v107, v110, v111\n"
  "v_mfma_f32_16x16x32_bf16 v[20:23], a[168:171], v[168:171], v[20:23]\n"
  "v_exp_f32 v112, v112\n"
  "v_mfma_f32_16x16x32_bf16 v[20:23], a[172:175], v[172:175], v[20:23]\n"
  "ds_read_b64_tr_b16 a[222:223], v219 offset:28672\n"
  "ds_read_b64_tr_b16 a[192:193], v220 offset:16384\n"
  "v_mfma_f32_16x16x32_bf16 v[28:31], a[160:163], v[176:179], v[204:207]\n"
  "v_exp_f32 v113, v113\n"
  "v_mfma_f32_16x16x32_bf16 v[28:31], a[164:167], v[180:183], v[28:31]\n"
  "ds_read_b64_tr_b16 a[194:195], v220 offset:20480\n"
  "v_cvt_pk_bf16_f32 v112, v112, v113\n"
  "v_mfma_f32_16x16x32_bf16 v[28:31], a[168:171], v[184:187], v[28:31]\n"
  "v_exp_f32 v114, v114\n"
  "v_mfma_f32_16x16x32_bf16 v[28:31], a[172:175], v[188:191], v[28:31]\n"
  "s_waitcnt lgkmcnt(6)\n"
  "ds_read_b64_tr_b16 a[224:225], v220 offset:24576\n"
  "ds_read_b64_tr_b16 a[226:227], v220 offset:28672\n"
  "v_mfma_f32_16x16x32_bf16 v[32:35], a[144:147], v[128:131], v[192:195]\n"
  "v_exp_f32 v115, v115\n"
  "v_mfma_f32_16x16x32_bf16 v[32:35], a[148:151], v[132:135], v[32:35]\n"
  "ds_read_b128 a[160:163], v224 offset:45056\n"
  "v_cvt_pk_bf16_f32 v113, v114, v115\n"
  "v_mfma_f32_16x16x32_bf16 v[32:35], a[152:155], v[136:139], v[32:35]\n"
  "v_exp_f32 v116, v116\n"
  "v_mfma_f32_16x16x32_bf16 v[32:35], a[156:159], v[140:143], v[32:35]\n"
  "ds_read_b128 a[164:167], v225 offset:45056\n"
  "ds_read_b128 a[168:171], v226 offset:45056\n"
  "v_mfma_f32_16x16x32_bf16 v[40:43], a[144:147], v[144:147], v[196:199]\n"
  "v_exp_f32 v117, v117\n"
  "v_mfma_f32_16x16x32_bf16 v[40:43], a[148:151], v[148:151], v[40:43]\n"
  "ds_read_b128 a[172:175], v227 offset:45056\n"
  "v_cvt_pk_bf16_f32 v114, v116, v117\n"
  "v_mfma_f32_16x16x32_bf16 v[40:43], a[152:155], v[152:155], v[40:43]\n"
  "v_exp_f32 v118, v118\n"
  "v_mfma_f32_16x16x32_bf16 v[40:43], a[156:159], v[156:159], v[40:43]\n"
  "ds_read_b64_tr_b16 a[196:197], v221 offset:16384\n"
  "ds_read_b64_tr_b16 a[198:199], v221 offset:20480\n"
  "v_mfma_f32_16x16x32_bf16 v[48:51], a[144:147], v[160:163], v[200:203]\n"
  "v_exp_f32 v119, v119\n"
  "v_mfma_f32_16x16x32_bf16 v[48:51], a[148:151], v[164:167], v[48:51]\n"
  "ds_read_b64_tr_b16 a[228:229], v221 offset:24576\n"
  "v_cvt_pk_bf16_f32 v115, v118, v119\n"
  "v_mfma_f32_16x16x32_bf16 v[48:51], a[152:155], v[168:171], v[48:51]\n"
  "v_exp_f32 v120, v120\n"
  "v_mfma_f32_16x16x32_bf16 v[48:51], a[156:159], v[172:175], v[48:51]\n"
  "ds_read_b64_tr_b16 a[230:231], v221 offset:28672\n"
  "ds_read_b64_tr_b16 a[200:201], v222 offset:16384\n"
  "v_mfma_f32_16x16x32_bf16 v[56:59], a[144:147], v[176:179], v[204:207]\n"
  "v_exp_f32 v121, v121\n"
  "v_mfma_f32_16x16x32_bf16 v[56:59], a[148:151], v[180:183], v[56:59]\n"
  "ds_read_b64_tr_b16 a[202:203], v222 offset:20480\n"
  "v_cvt_pk_bf16_f32 v120, v120, v121\n"
  "v_mfma_f32_16x16x32_bf16 v[56:59], a[152:155], v[184:187], v[56:59]\n"
  "v_exp_f32 v122, v122\n"
  "v_mfma_f32_16x16x32_bf16 v[56:59], a[156:159], v[188:191], v[56:59]\n"
  "s_waitcnt lgkmcnt(6)\n"
  "ds_read_b64_tr_b16 a[232:233], v222 offset:24576\n"
  "ds_read_b64_tr_b16 a[234:235], v222 offset:28672\n"
  "v_mfma_f32_16x16x32_bf16 v[36:39], a[160:163], v[128:131], v[192:195]\n"
  "v_exp_f32 v123, v123\n"
  "v_mfma_f32_16x16x32_bf16 v[36:39], a[164:167], v[132:135], v[36:39]\n"
  "v_cvt_pk_bf16_f32 v121, v122, v123\n"
  "v_mfma_f32_16x16x32_bf16 v[36:39], a[168:171], v[136:139], v[36:39]\n"
  "v_exp_f32 v124, v124\n"
  "v_mfma_f32_16x16x32_bf16 v[36:39], a[172:175], v[140:143], v[36:39]\n"
  "ds_read_b64_tr_b16 a[204:205], v223 offset:16384\n"
  "ds_read_b64_tr_b16 a[206:207], v223 offset:20480\n"
  "v_mfma_f32_16x16x32_bf16 v[44:47], a[160:163], v[144:147], v[196:199]\n"
  "v_exp_f32 v125, v125\n"
  "v_mfma_f32_16x16x32_bf16 v[44:47], a[164:167], v[148:151], v[44:47]\n"
  "v_cvt_pk_bf16_f32 v122, v124, v125\n"
  "v_mfma_f32_16x16x32_bf16 v[44:47], a[168:171], v[152:155], v[44:47]\n"
  "v_exp_f32 v126, v126\n"
  "v_mfma_f32_16x16x32_bf16 v[44:47], a[172:175], v[156:159], v[44:47]\n"
  "ds_read_b64_tr_b16 a[236:237], v223 offset:24576\n"
  "ds_read_b64_tr_b16 a[238:239], v223 offset:28672\n"
  "v_mfma_f32_16x16x32_bf16 v[52:55], a[160:163], v[160:163], v[200:203]\n"
  "v_exp_f32 v127, v127\n"
  "v_mfma_f32_16x16x32_bf16 v[52:55], a[164:167], v[164:167], v[52:55]\n"
  "v_cvt_pk_bf16_f32 v123, v126, v127\n"
  "v_mfma_f32_16x16x32_bf16 v[52:55], a[168:171], v[168:171], v[52:55]\n"
  "v_mfma_f32_16x16x32_bf16 v[52:55], a[172:175], v[172:175], v[52:55]\n"
  "v_mfma_f32_16x16x32_bf16 v[60:63], a[160:163], v[176:179], v[204:207]\n"
  "v_mfma_f32_16x16x32_bf16 v[60:63], a[164:167], v[180:183], v[60:63]\n"
  "v_mfma_f32_16x16x32_bf16 v[60:63], a[168:171], v[184:187], v[60:63]\n"
  "v_mfma_f32_16x16x32_bf16 v[60:63], a[172:175], v[188:191], v[60:63]\n"
  "s_waitcnt vmcnt(4) lgkmcnt(0)\n"
  "s_barrier\n"
  "v_mfma_f32_16x16x32_bf16 a[0:3], a[176:179], v[64:67], a[0:3]\n"
  "s_add_u32 m0, s42, 114688\n"
  "v_mfma_f32_16x16x32_bf16 a[0:3], a[208:211], v[96:99], a[0:3]\n"
  "buffer_load_dwordx4 v232, s[24:27], s41 offen offset:0 nt lds\n"
  "v_mfma_f32_16x16x32_bf16 a[16:19], a[180:183], v[64:67], a[16:19]\n"
  "v_exp_f32 v0, v0\n"
  "v_mfma_f32_16x16x32_bf16 a[16:19], a[212:215], v[96:99], a[16:19]\n"
  "buffer_load_dwordx4 v233, s[24:27], s41 offen offset:1024 nt lds\n"
  "v_mfma_f32_16x16x32_bf16 a[32:35], a[184:187], v[64:67], a[32:35]\n"
  "v_exp_f32 v1, v1\n"
  "v_mfma_f32_16x16x32_bf16 a[32:35], a[216:219], v[96:99], a[32:35]\n"
  "v_cvt_pk_bf16_f32 v0, v0, v1\n"
  "v_mfma_f32_16x16x32_bf16 a[48:51], a[188:191], v[64:67], a[48:51]\n"
  "v_exp_f32 v2, v2\n"
  "v_mfma_f32_16x16x32_bf16 a[48:51], a[220:223], v[96:99], a[48:51]\n"
  "ds_read_b128 a[144:147], v224 offset:49152\n"
  "v_mfma_f32_16x16x32_bf16 a[64:67], a[192:195], v[64:67], a[64:67]\n"
  "v_exp_f32 v3, v3\n"
  "v_mfma_f32_16x16x32_bf16 a[64:67], a[224:227], v[96:99], a[64:67]\n"
  "ds_read_b128 a[148:151], v225 offset:49152\n"
  "v_cvt_pk_bf16_f32 v1, v2, v3\n"
  "v_mfma_f32_16x16x32_bf16 a[80:83], a[196:199], v[64:67], a[80:83]\n"
  "v_exp_f32 v4, v4\n"
  "v_mfma_f32_16x16x32_bf16 a[80:83], a[228:231], v[96:99], a[80:83]\n"
  "ds_read_b128 a[152:155], v226 offset:49152\n"
  "v_mfma_f32_16x16x32_bf16 a[96:99], a[200:203], v[64:67], a[96:99]\n"
  "v_exp_f32 v5, v5\n"
  "v_mfma_f32_16x16x32_bf16 a[96:99], a[232:235], v[96:99], a[96:99]\n"
  "ds_read_b128 a[156:159], v227 offset:49152\n"
  "v_cvt_pk_bf16_f32 v2, v4, v5\n"
  "v_mfma_f32_16x16x32_bf16 a[112:115], a[204:207], v[64:67], a[112:115]\n"
  "v_exp_f32 v6, v6\n"
  "v_mfma_f32_16x16x32_bf16 a[112:115], a[236:239], v[96:99], a[112:115]\n"
  "ds_read_b128 a[160:163], v224 offset:53248\n"
  "v_mfma_f32_16x16x32_bf16 a[128:131], v[208:211], v[64:67], a[128:131]\n"
  "v_exp_f32 v7, v7\n"
  "v_mfma_f32_16x16x32_bf16 a[128:131], v[208:211], v[96:99], a[128:131]\n"
  "ds_read_b128 a[164:167], v225 offset:53248\n"
  "v_cvt_pk_bf16_f32 v3, v6, v7\n"
  "v_mfma_f32_16x16x32_bf16 a[4:7], a[176:179], v[72:75], a[4:7]\n"
  "v_exp_f32 v8, v8\n"
  "v_mfma_f32_16x16x32_bf16 a[4:7], a[208:211], v[104:107], a[4:7]\n"
  "ds_read_b128 a[168:171], v226 offset:53248\n"
  "v_mfma_f32_16x16x32_bf16 a[20:23], a[180:183], v[72:75], a[20:23]\n"
  "v_exp_f32 v9, v9\n"
  "v_mfma_f32_16x16x32_bf16 a[20:23], a[212:215], v[104:107], a[20:23]\n"
  "ds_read_b128 a[172:175], v227 offset:53248\n"
  "v_cvt_pk_bf16_f32 v8, v8, v9\n"
  "v_mfma_f32_16x16x32_bf16 a[36:39], a[184:187], v[72:75], a[36:39]\n"
  "v_exp_f32 v10, v10\n"
  "v_mfma_f32_16x16x32_bf16 a[36:39], a[216:219], v[104:107], a[36:39]\n"
  "buffer_load_dwordx4 v234, s[24:27], s41 offen offset:2048 nt lds\n"
  "v_mfma_f32_16x16x32_bf16 a[52:55], a[188:191], v[72:75], a[52:55]\n"
  "v_exp_f32 v11, v11\n"
  "v_mfma_f32_16x16x32_bf16 a[52:55], a[220:223], v[104:107], a[52:55]\n"
  "v_cvt_pk_bf16_f32 v9, v10, v11\n"
  "v_mfma_f32_16x16x32_bf16 a[68:71], a[192:195], v[72:75], a[68:71]\n"
  "v_exp_f32 v12, v12\n"
  "v_mfma_f32_16x16x32_bf16 a[68:71], a[224:227], v[104:107], a[68:71]\n"
  "buffer_load_dwordx4 v235, s[24:27], s41 offen offset:3072 nt lds\n"
  "v_mfma_f32_16x16x32_bf16 a[84:87], a[196:199], v[72:75], a[84:87]\n"
  "s_add_u32 s41, s41, 16384\n"
  "v_exp_f32 v13, v13\n"
  "v_mfma_f32_16x16x32_bf16 a[84:87], a[228:231], v[104:107], a[84:87]\n"
  "v_cvt_pk_bf16_f32 v10, v12, v13\n"
  "v_mfma_f32_16x16x32_bf16 a[100:103], a[200:203], v[72:75], a[100:103]\n"
  "v_exp_f32 v14, v14\n"
  "v_mfma_f32_16x16x32_bf16 a[100:103], a[232:235], v[104:107], a[100:103]\n"
  "v_mfma_f32_16x16x32_bf16 a[116:119], a[204:207], v[72:75], a[116:119]\n"
  "v_exp_f32 v15, v15\n"
  "v_mfma_f32_16x16x32_bf16 a[116:119], a[236:239], v[104:107], a[116:119]\n"
  "v_cvt_pk_bf16_f32 v11, v14, v15\n"
  "v_mfma_f32_16x16x32_bf16 a[132:135], v[208:211], v[72:75], a[132:135]\n"
  "v_exp_f32 v16, v16\n"
  "v_mfma_f32_16x16x32_bf16 a[132:135], v[208:211], v[104:107], a[132:135]\n"
  "v_mfma_f32_16x16x32_bf16 a[8:11], a[176:179], v[80:83], a[8:11]\n"
  "v_exp_f32 v17, v17\n"
  "v_mfma_f32_16x16x32_bf16 a[8:11], a[208:211], v[112:115], a[8:11]\n"
  "v_cvt_pk_bf16_f32 v16, v16, v17\n"
  "v_mfma_f32_16x16x32_bf16 a[24:27], a[180:183], v[80:83], a[24:27]\n"
  "s_add_u32 m0, s42, 16384\n"
  "s_add_u32 s46, s52, 98304\n"
  "v_exp_f32 v18, v18\n"
  "v_mfma_f32_16x16x32_bf16 a[24:27], a[212:215], v[112:115], a[24:27]\n"
  "buffer_load_dwordx4 v244, s[76:79], s46 offen offset:0 lds\n"
  "v_mfma_f32_16x16x32_bf16 a[40:43], a[184:187], v[80:83], a[40:43]\n"
  "v_exp_f32 v19, v19\n"
  "v_mfma_f32_16x16x32_bf16 a[40:43], a[216:219], v[112:115], a[40:43]\n"
  "v_cvt_pk_bf16_f32 v17, v18, v19\n"
  "v_mfma_f32_16x16x32_bf16 a[56:59], a[188:191], v[80:83], a[56:59]\n"
  "v_exp_f32 v20, v20\n"
  "v_mfma_f32_16x16x32_bf16 a[56:59], a[220:223], v[112:115], a[56:59]\n"
  "buffer_load_dwordx4 v245, s[76:79], s46 offen offset:1024 lds\n"
  "v_mfma_f32_16x16x32_bf16 a[72:75], a[192:195], v[80:83], a[72:75]\n"
  "v_exp_f32 v21, v21\n"
  "v_mfma_f32_16x16x32_bf16 a[72:75], a[224:227], v[112:115], a[72:75]\n"
  "v_cvt_pk_bf16_f32 v18, v20, v21\n"
  "v_mfma_f32_16x16x32_bf16 a[88:91], a[196:199], v[80:83], a[88:91]\n"
  "v_exp_f32 v22, v22\n"
  "v_mfma_f32_16x16x32_bf16 a[88:91], a[228:231], v[112:115], a[88:91]\n"
  "buffer_load_dwordx4 v246, s[76:79], s46 offen offset:2048 lds\n"
  "v_mfma_f32_16x16x32_bf16 a[104:107], a[200:203], v[80:83], a[104:107]\n"
  "v_exp_f32 v23, v23\n"
  "v_mfma_f32_16x16x32_bf16 a[104:107], a[232:235], v[112:115], a[104:107]\n"
  "v_cvt_pk_bf16_f32 v19, v22, v23\n"
  "v_mfma_f32_16x16x32_bf16 a[120:123], a[204:207], v[80:83], a[120:123]\n"
  "v_exp_f32 v24, v24\n"
  "v_mfma_f32_16x16x32_bf16 a[120:123], a[236:239], v[112:115], a[120:123]\n"
  "buffer_load_dwordx4 v247, s[76:79], s46 offen offset:3072 lds\n"
  "v_mfma_f32_16x16x32_bf16 a[136:139], v[208:211], v[80:83], a[136:139]\n"
  "v_exp_f32 v25, v25\n"
  "v_mfma_f32_16x16x32_bf16 a[136:139], v[208:211], v[112:115], a[136:139]\n"
  "v_cvt_pk_bf16_f32 v24, v24, v25\n"
  "v_mfma_f32_16x16x32_bf16 a[12:15], a[176:179], v[88:91], a[12:15]\n"
  "v_exp_f32 v26, v26\n"
  "v_mfma_f32_16x16x32_bf16 a[12:15], a[208:211], v[120:123], a[12:15]\n"
  "v_mfma_f32_16x16x32_bf16 a[28:31], a[180:183], v[88:91], a[28:31]\n"
  "v_exp_f32 v27, v27\n"
  "v_mfma_f32_16x16x32_bf16 a[28:31], a[212:215], v[120:123], a[28:31]\n"
  "v_cvt_pk_bf16_f32 v25, v26, v27\n"
  "v_mfma_f32_16x16x32_bf16 a[44:47], a[184:187], v[88:91], a[44:47]\n"
  "v_exp_f32 v28, v28\n"
  "v_mfma_f32_16x16x32_bf16 a[44:47], a[216:219], v[120:123], a[44:47]\n"
  "v_mfma_f32_16x16x32_bf16 a[60:63], a[188:191], v[88:91], a[60:63]\n"
  "v_exp_f32 v29, v29\n"
  "v_mfma_f32_16x16x32_bf16 a[60:63], a[220:223], v[120:123], a[60:63]\n"
  "v_cvt_pk_bf16_f32 v26, v28, v29\n"
  "v_mfma_f32_16x16x32_bf16 a[76:79], a[192:195], v[88:91], a[76:79]\n"
  "v_exp_f32 v30, v30\n"
  "v_mfma_f32_16x16x32_bf16 a[76:79], a[224:227], v[120:123], a[76:79]\n"
  "v_mfma_f32_16x16x32_bf16 a[92:95], a[196:199], v[88:91], a[92:95]\n"
  "v_exp_f32 v31, v31\n"
  "v_mfma_f32_16x16x32_bf16 a[92:95], a[228:231], v[120:123], a[92:95]\n"
  "v_cvt_pk_bf16_f32 v27, v30, v31\n"
  "v_mfma_f32_16x16x32_bf16 a[108:111], a[200:203], v[88:91], a[108:111]\n"
  "v_exp_f32 v32, v32\n"
  "v_mfma_f32_16x16x32_bf16 a[108:111], a[232:235], v[120:123], a[108:111]\n"
  "v_mfma_f32_16x16x32_bf16 a[124:127], a[204:207], v[88:91], a[124:127]\n"
  "v_exp_f32 v33, v33\n"
  "v_mfma_f32_16x16x32_bf16 a[124:127], a[236:239], v[120:123], a[124:127]\n"
  "v_cvt_pk_bf16_f32 v32, v32, v33\n"
  "v_mfma_f32_16x16x32_bf16 a[140:143], v[208:211], v[88:91], a[140:143]\n"
  "v_exp_f32 v34, v34\n"
  "v_mfma_f32_16x16x32_bf16 a[140:143], v[208:211], v[120:123], a[140:143]\n"
  "s_waitcnt lgkmcnt(0)\n"
  "v_mfma_f32_16x16x32_bf16 v[64:67], a[144:147], v[128:131], v[192:195]\n"
  "v_exp_f32 v35, v35\n"
  "v_mfma_f32_16x16x32_bf16 v[64:67], a[148:151], v[132:135], v[64:67]\n"
  "ds_read_b64_tr_b16 a[176:177], v216 offset:32768\n"
  "v_cvt_pk_bf16_f32 v33, v34, v35\n"
  "v_mfma_f32_16x16x32_bf16 v[64:67], a[152:155], v[136:139], v[64:67]\n"
  "v_exp_f32 v36, v36\n"
  "v_mfma_f32_16x16x32_bf16 v[64:67], a[156:159], v[140:143], v[64:67]\n"
  "ds_read_b64_tr_b16 a[178:179], v216 offset:36864\n"
  "ds_read_b64_tr_b16 a[208:209], v216 offset:40960\n"
  "v_mfma_f32_16x16x32_bf16 v[72:75], a[144:147], v[144:147], v[196:199]\n"
  "v_exp_f32 v37, v37\n"
  "v_mfma_f32_16x16x32_bf16 v[72:75], a[148:151], v[148:151], v[72:75]\n"
  "ds_read_b64_tr_b16 a[210:211], v216 offset:45056\n"
  "v_cvt_pk_bf16_f32 v34, v36, v37\n"
  "v_mfma_f32_16x16x32_bf16 v[72:75], a[152:155], v[152:155], v[72:75]\n"
  "v_exp_f32 v38, v38\n"
  "v_mfma_f32_16x16x32_bf16 v[72:75], a[156:159], v[156:159], v[72:75]\n"
  "ds_read_b64_tr_b16 a[180:181], v217 offset:32768\n"
  "ds_read_b64_tr_b16 a[182:183], v217 offset:36864\n"
  "v_mfma_f32_16x16x32_bf16 v[80:83], a[144:147], v[160:163], v[200:203]\n"
  "v_exp_f32 v39, v39\n"
  "v_mfma_f32_16x16x32_bf16 v[80:83], a[148:151], v[164:167], v[80:83]\n"
  "ds_read_b64_tr_b16 a[212:213], v217 offset:40960\n"
  "v_cvt_pk_bf16_f32 v35, v38, v39\n"
  "v_mfma_f32_16x16x32_bf16 v[80:83], a[152:155], v[168:171], v[80:83]\n"
  "v_exp_f32 v40, v40\n"
  "v_mfma_f32_16x16x32_bf16 v[80:83], a[156:159], v[172:175], v[80:83]\n"
  "ds_read_b64_tr_b16 a[214:215], v217 offset:45056\n"
  "ds_read_b64_tr_b16 a[184:185], v218 offset:32768\n"
  "v_mfma_f32_16x16x32_bf16 v[88:91], a[144:147], v[176:179], v[204:207]\n"
  "v_exp_f32 v41, v41\n"
  "v_mfma_f32_16x16x32_bf16 v[88:91], a[148:151], v[180:183], v[88:91]\n"
  "ds_read_b64_tr_b16 a[186:187], v218 offset:36864\n"
  "v_cvt_pk_bf16_f32 v40, v40, v41\n"
  "v_mfma_f32_16x16x32_bf16 v[88:91], a[152:155], v[184:187], v[88:91]\n"
  "v_exp_f32 v42, v42\n"
  "v_mfma_f32_16x16x32_bf16 v[88:91], a[156:159], v[188:191], v[88:91]\n"
  "ds_read_b64_tr_b16 a[216:217], v218 offset:40960\n"
  "ds_read_b64_tr_b16 a[218:219], v218 offset:45056\n"
  "v_mfma_f32_16x16x32_bf16 v[68:71], a[160:163], v[128:131], v[192:195]\n"
  "v_exp_f32 v43, v43\n"
  "v_mfma_f32_16x16x32_bf16 v[68:71], a[164:167], v[132:135], v[68:71]\n"
  "ds_read_b128 a[144:147], v224 offset:57344\n"
  "v_cvt_pk_bf16_f32 v41, v42, v43\n"
  "v_mfma_f32_16x16x32_bf16 v[68:71], a[168:171], v[136:139], v[68:71]\n"
  "v_exp_f32 v44, v44\n"
  "v_mfma_f32_16x16x32_bf16 v[68:71], a[172:175], v[140:143], v[68:71]\n"
  "ds_read_b128 a[148:151], v225 offset:57344\n"
  "ds_read_b128 a[152:155], v226 offset:57344\n"
  "v_mfma_f32_16x16x32_bf16 v[76:79], a[160:163], v[144:147], v[196:199]\n"
  "v_exp_f32 v45, v45\n"
  "v_mfma_f32_16x16x32_bf16 v[76:79], a[164:167], v[148:151], v[76:79]\n"
  "ds_read_b128 a[156:159], v227 offset:57344\n"
  "v_cvt_pk_bf16_f32 v42, v44, v45\n"
  "v_mfma_f32_16x16x32_bf16 v[76:79], a[168:171], v[152:155], v[76:79]\n"
  "v_exp_f32 v46, v46\n"
  "v_mfma_f32_16x16x32_bf16 v[76:79], a[172:175], v[156:159], v[76:79]\n"
  "ds_read_b64_tr_b16 a[188:189], v219 offset:32768\n"
  "ds_read_b64_tr_b16 a[190:191], v219 offset:36864\n"
  "v_mfma_f32_16x16x32_bf16 v[84:87], a[160:163], v[160:163], v[200:203]\n"
  "v_exp_f32 v47, v47\n"
  "v_mfma_f32_16x16x32_bf16 v[84:87], a[164:167], v[164:167], v[84:87]\n"
  "ds_read_b64_tr_b16 a[220:221], v219 offset:40960\n"
  "v_cvt_pk_bf16_f32 v43, v46, v47\n"
  "v_mfma_f32_16x16x32_bf16 v[84:87], a[168:171], v[168:171], v[84:87]\n"
  "v_exp_f32 v48, v48\n"
  "v_mfma_f32_16x16x32_bf16 v[84:87], a[172:175], v[172:175], v[84:87]\n"
  "ds_read_b64_tr_b16 a[222:223], v219 offset:45056\n"
  "ds_read_b64_tr_b16 a[192:193], v220 offset:32768\n"
  "v_mfma_f32_16x16x32_bf16 v[92:95], a[160:163], v[176:179], v[204:207]\n"
  "v_exp_f32 v49, v49\n"
  "v_mfma_f32_16x16x32_bf16 v[92:95], a[164:167], v[180:183], v[92:95]\n"
  "ds_read_b64_tr_b16 a[194:195], v220 offset:36864\n"
  "v_cvt_pk_bf16_f32 v48, v48, v49\n"
  "v_mfma_f32_16x16x32_bf16 v[92:95], a[168:171], v[184:187], v[92:95]\n"
  "v_exp_f32 v50, v50\n"
  "v_mfma_f32_16x16x32_bf16 v[92:95], a[172:175], v[188:191], v[92:95]\n"
  "s_waitcnt lgkmcnt(6)\n"
  "ds_read_b64_tr_b16 a[224:225], v220 offset:40960\n"
  "ds_read_b64_tr_b16 a[226:227], v220 offset:45056\n"
  "v_mfma_f32_16x16x32_bf16 v[96:99], a[144:147], v[128:131], v[192:195]\n"
  "v_exp_f32 v51, v51\n"
  "v_mfma_f32_16x16x32_bf16 v[96:99], a[148:151], v[132:135], v[96:99]\n"
  "ds_read_b128 a[160:163], v224 offset:61440\n"
  "v_cvt_pk_bf16_f32 v49, v50, v51\n"
  "v_mfma_f32_16x16x32_bf16 v[96:99], a[152:155], v[136:139], v[96:99]\n"
  "v_exp_f32 v52, v52\n"
  "v_mfma_f32_16x16x32_bf16 v[96:99], a[156:159], v[140:143], v[96:99]\n"
  "ds_read_b128 a[164:167], v225 offset:61440\n"
  "ds_read_b128 a[168:171], v226 offset:61440\n"
  "v_mfma_f32_16x16x32_bf16 v[104:107], a[144:147], v[144:147], v[196:199]\n"
  "v_exp_f32 v53, v53\n"
  "v_mfma_f32_16x16x32_bf16 v[104:107], a[148:151], v[148:151], v[104:107]\n"
  "ds_read_b128 a[172:175], v227 offset:61440\n"
  "v_cvt_pk_bf16_f32 v50, v52, v53\n"
  "v_mfma_f32_16x16x32_bf16 v[104:107], a[152:155], v[152:155], v[104:107]\n"
  "v_exp_f32 v54, v54\n"
  "v_mfma_f32_16x16x32_bf16 v[104:107], a[156:159], v[156:159], v[104:107]\n"
  "ds_read_b64_tr_b16 a[196:197], v221 offset:32768\n"
  "ds_read_b64_tr_b16 a[198:199], v221 offset:36864\n"
  "v_mfma_f32_16x16x32_bf16 v[112:115], a[144:147], v[160:163], v[200:203]\n"
  "v_exp_f32 v55, v55\n"
  "v_mfma_f32_16x16x32_bf16 v[112:115], a[148:151], v[164:167], v[112:115]\n"
  "ds_read_b64_tr_b16 a[228:229], v221 offset:40960\n"
  "v_cvt_pk_bf16_f32 v51, v54, v55\n"
  "v_mfma_f32_16x16x32_bf16 v[112:115], a[152:155], v[168:171], v[112:115]\n"
  "v_exp_f32 v56, v56\n"
  "v_mfma_f32_16x16x32_bf16 v[112:115], a[156:159], v[172:175], v[112:115]\n"
  "ds_read_b64_tr_b16 a[230:231], v221 offset:45056\n"
  "ds_read_b64_tr_b16 a[200:201], v222 offset:32768\n"
  "v_mfma_f32_16x16x32_bf16 v[120:123], a[144:147], v[176:179], v[204:207]\n"
  "v_exp_f32 v57, v57\n"
  "v_mfma_f32_16x16x32_bf16 v[120:123], a[148:151], v[180:183], v[120:123]\n"
  "ds_read_b64_tr_b16 a[202:203], v222 offset:36864\n"
  "v_cvt_pk_bf16_f32 v56, v56, v57\n"
  "v_mfma_f32_16x16x32_bf16 v[120:123], a[152:155], v[184:187], v[120:123]\n"
  "v_exp_f32 v58, v58\n"
  "v_mfma_f32_16x16x32_bf16 v[120:123], a[156:159], v[188:191], v[120:123]\n"
  "s_waitcnt lgkmcnt(6)\n"
  "ds_read_b64_tr_b16 a[232:233], v222 offset:40960\n"
  "ds_read_b64_tr_b16 a[234:235], v222 offset:45056\n"
  "v_mfma_f32_16x16x32_bf16 v[100:103], a[160:163], v[128:131], v[192:195]\n"
  "v_exp_f32 v59, v59\n"
  "v_mfma_f32_16x16x32_bf16 v[100:103], a[164:167], v[132:135], v[100:103]\n"
  "v_cvt_pk_bf16_f32 v57, v58, v59\n"
  "v_mfma_f32_16x16x32_bf16 v[100:103], a[168:171], v[136:139], v[100:103]\n"
  "v_exp_f32 v60, v60\n"
  "v_mfma_f32_16x16x32_bf16 v[100:103], a[172:175], v[140:143], v[100:103]\n"
  "ds_read_b64_tr_b16 a[204:205], v223 offset:32768\n"
  "ds_read_b64_tr_b16 a[206:207], v223 offset:36864\n"
  "v_mfma_f32_16x16x32_bf16 v[108:111], a[160:163], v[144:147], v[196:199]\n"
  "v_exp_f32 v61, v61\n"
  "v_mfma_f32_16x16x32_bf16 v[108:111], a[164:167], v[148:151], v[108:111]\n"
  "v_cvt_pk_bf16_f32 v58, v60, v61\n"
  "v_mfma_f32_16x16x32_bf16 v[108:111], a[168:171], v[152:155], v[108:111]\n"
  "v_exp_f32 v62, v62\n"
  "v_mfma_f32_16x16x32_bf16 v[108:111], a[172:175], v[156:159], v[108:111]\n"
  "ds_read_b64_tr_b16 a[236:237], v223 offset:40960\n"
  "ds_read_b64_tr_b16 a[238:239], v223 offset:45056\n"
  "v_mfma_f32_16x16x32_bf16 v[116:119], a[160:163], v[160:163], v[200:203]\n"
  "v_exp_f32 v63, v63\n"
  "v_mfma_f32_16x16x32_bf16 v[116:119], a[164:167], v[164:167], v[116:119]\n"
  "v_cvt_pk_bf16_f32 v59, v62, v63\n"
  "v_mfma_f32_16x16x32_bf16 v[116:119], a[168:171], v[168:171], v[116:119]\n"
  "v_mfma_f32_16x16x32_bf16 v[116:119], a[172:175], v[172:175], v[116:119]\n"
  "v_mfma_f32_16x16x32_bf16 v[124:127], a[160:163], v[176:179], v[204:207]\n"
  "v_mfma_f32_16x16x32_bf16 v[124:127], a[164:167], v[180:183], v[124:127]\n"
  "v_mfma_f32_16x16x32_bf16 v[124:127], a[168:171], v[184:187], v[124:127]\n"
  "v_mfma_f32_16x16x32_bf16 v[124:127], a[172:175], v[188:191], v[124:127]\n"
  "s_waitcnt vmcnt(4) lgkmcnt(0)\n"
  "s_barrier\n"
  "v_mfma_f32_16x16x32_bf16 a[0:3], a[176:179], v[0:3], a[0:3]\n"
  "s_add_u32 m0, s42, 65536\n"
  "s_add_u32 s46, s53, 0\n"
  "v_mfma_f32_16x16x32_bf16 a[0:3], a[208:211], v[32:35], a[0:3]\n"
  "buffer_load_dwordx4 v244, s[76:79], s46 offen offset:0 lds\n"
  "v_mfma_f32_16x16x32_bf16 a[16:19], a[180:183], v[0:3], a[16:19]\n"
  "v_exp_f32 v64, v64\n"
  "v_mfma_f32_16x16x32_bf16 a[16:19], a[212:215], v[32:35], a[16:19]\n"
  "buffer_load_dwordx4 v245, s[76:79], s46 offen offset:1024 lds\n"
  "v_mfma_f32_16x16x32_bf16 a[32:35], a[184:187], v[0:3], a[32:35]\n"
  "v_exp_f32 v65, v65\n"
  "v_mfma_f32_16x16x32_bf16 a[32:35], a[216:219], v[32:35], a[32:35]\n"
  "v_cvt_pk_bf16_f32 v64, v64, v65\n"
  "v_mfma_f32_16x16x32_bf16 a[48:51], a[188:191], v[0:3], a[48:51]\n"
  "v_exp_f32 v66, v66\n"
  "v_mfma_f32_16x16x32_bf16 a[48:51], a[220:223], v[32:35], a[48:51]\n"
  "v_mfma_f32_16x16x32_bf16 a[64:67], a[192:195], v[0:3], a[64:67]\n"
  "v_exp_f32 v67, v67\n"
  "v_mfma_f32_16x16x32_bf16 a[64:67], a[224:227], v[32:35], a[64:67]\n"
  "v_cvt_pk_bf16_f32 v65, v66, v67\n"
  "v_mfma_f32_16x16x32_bf16 a[80:83], a[196:199], v[0:3], a[80:83]\n"
  "v_exp_f32 v68, v68\n"
  "v_mfma_f32_16x16x32_bf16 a[80:83], a[228:231], v[32:35], a[80:83]\n"
  "v_mfma_f32_16x16x32_bf16 a[96:99], a[200:203], v[0:3], a[96:99]\n"
  "v_exp_f32 v69, v69\n"
  "v_mfma_f32_16x16x32_bf16 a[96:99], a[232:235], v[32:35], a[96:99]\n"
  "v_cvt_pk_bf16_f32 v66, v68, v69\n"
  "v_mfma_f32_16x16x32_bf16 a[112:115], a[204:207], v[0:3], a[112:115]\n"
  "v_exp_f32 v70, v70\n"
  "v_mfma_f32_16x16x32_bf16 a[112:115], a[236:239], v[32:35], a[112:115]\n"
  "v_mfma_f32_16x16x32_bf16 a[128:131], v[208:211], v[0:3], a[128:131]\n"
  "v_exp_f32 v71, v71\n"
  "v_mfma_f32_16x16x32_bf16 a[128:131], v[208:211], v[32:35], a[128:131]\n"
  "v_cvt_pk_bf16_f32 v67, v70, v71\n"
  "v_mfma_f32_16x16x32_bf16 a[4:7], a[176:179], v[8:11], a[4:7]\n"
  "v_exp_f32 v72, v72\n"
  "v_mfma_f32_16x16x32_bf16 a[4:7], a[208:211], v[40:43], a[4:7]\n"
  "v_mfma_f32_16x16x32_bf16 a[20:23], a[180:183], v[8:11], a[20:23]\n"
  "v_exp_f32 v73, v73\n"
  "v_mfma_f32_16x16x32_bf16 a[20:23], a[212:215], v[40:43], a[20:23]\n"
  "v_cvt_pk_bf16_f32 v72, v72, v73\n"
  "v_mfma_f32_16x16x32_bf16 a[36:39], a[184:187], v[8:11], a[36:39]\n"
  "v_exp_f32 v74, v74\n"
  "v_mfma_f32_16x16x32_bf16 a[36:39], a[216:219], v[40:43], a[36:39]\n"
  "buffer_load_dwordx4 v246, s[76:79], s46 offen offset:2048 lds\n"
  "v_mfma_f32_16x16x32_bf16 a[52:55], a[188:191], v[8:11], a[52:55]\n"
  "v_exp_f32 v75, v75\n"
  "v_mfma_f32_16x16x32_bf16 a[52:55], a[220:223], v[40:43], a[52:55]\n"
  "v_cvt_pk_bf16_f32 v73, v74, v75\n"
  "v_mfma_f32_16x16x32_bf16 a[68:71], a[192:195], v[8:11], a[68:71]\n"
  "v_exp_f32 v76, v76\n"
  "v_mfma_f32_16x16x32_bf16 a[68:71], a[224:227], v[40:43], a[68:71]\n"
  "buffer_load_dwordx4 v247, s[76:79], s46 offen offset:3072 lds\n"
  "v_mfma_f32_16x16x32_bf16 a[84:87], a[196:199], v[8:11], a[84:87]\n"
  "v_exp_f32 v77, v77\n"
  "v_mfma_f32_16x16x32_bf16 a[84:87], a[228:231], v[40:43], a[84:87]\n"
  "v_cvt_pk_bf16_f32 v74, v76, v77\n"
  "v_mfma_f32_16x16x32_bf16 a[100:103], a[200:203], v[8:11], a[100:103]\n"
  "v_exp_f32 v78, v78\n"
  "v_mfma_f32_16x16x32_bf16 a[100:103], a[232:235], v[40:43], a[100:103]\n"
  "v_mfma_f32_16x16x32_bf16 a[116:119], a[204:207], v[8:11], a[116:119]\n"
  "v_exp_f32 v79, v79\n"
  "v_mfma_f32_16x16x32_bf16 a[116:119], a[236:239], v[40:43], a[116:119]\n"
  "v_cvt_pk_bf16_f32 v75, v78, v79\n"
  "v_mfma_f32_16x16x32_bf16 a[132:135], v[208:211], v[8:11], a[132:135]\n"
  "v_exp_f32 v80, v80\n"
  "v_mfma_f32_16x16x32_bf16 a[132:135], v[208:211], v[40:43], a[132:135]\n"
  "v_mfma_f32_16x16x32_bf16 a[8:11], a[176:179], v[16:19], a[8:11]\n"
  "v_exp_f32 v81, v81\n"
  "v_mfma_f32_16x16x32_bf16 a[8:11], a[208:211], v[48:51], a[8:11]\n"
  "v_cvt_pk_bf16_f32 v80, v80, v81\n"
  "v_mfma_f32_16x16x32_bf16 a[24:27], a[180:183], v[16:19], a[24:27]\n"
  "s_add_u32 m0, s42, 32768\n"
  "s_add_u32 s46, s53, 32768\n"
  "v_exp_f32 v82, v82\n"
  "v_mfma_f32_16x16x32_bf16 a[24:27], a[212:215], v[48:51], a[24:27]\n"
  "buffer_load_dwordx4 v244, s[76:79], s46 offen offset:0 lds\n"
  "v_mfma_f32_16x16x32_bf16 a[40:43], a[184:187], v[16:19], a[40:43]\n"
  "v_exp_f32 v83, v83\n"
  "v_mfma_f32_16x16x32_bf16 a[40:43], a[216:219], v[48:51], a[40:43]\n"
  "v_cvt_pk_bf16_f32 v81, v82, v83\n"
  "v_mfma_f32_16x16x32_bf16 a[56:59], a[188:191], v[16:19], a[56:59]\n"
  "v_exp_f32 v84, v84\n"
  "v_mfma_f32_16x16x32_bf16 a[56:59], a[220:223], v[48:51], a[56:59]\n"
  "buffer_load_dwordx4 v245, s[76:79], s46 offen offset:1024 lds\n"
  "v_mfma_f32_16x16x32_bf16 a[72:75], a[192:195], v[16:19], a[72:75]\n"
  "v_exp_f32 v85, v85\n"
  "v_mfma_f32_16x16x32_bf16 a[72:75], a[224:227], v[48:51], a[72:75]\n"
  "v_cvt_pk_bf16_f32 v82, v84, v85\n"
  "v_mfma_f32_16x16x32_bf16 a[88:91], a[196:199], v[16:19], a[88:91]\n"
  "v_exp_f32 v86, v86\n"
  "v_mfma_f32_16x16x32_bf16 a[88:91], a[228:231], v[48:51], a[88:91]\n"
  "buffer_load_dwordx4 v246, s[76:79], s46 offen offset:2048 lds\n"
  "v_mfma_f32_16x16x32_bf16 a[104:107], a[200:203], v[16:19], a[104:107]\n"
  "v_exp_f32 v87, v87\n"
  "v_mfma_f32_16x16x32_bf16 a[104:107], a[232:235], v[48:51], a[104:107]\n"
  "v_cvt_pk_bf16_f32 v83, v86, v87\n"
  "v_mfma_f32_16x16x32_bf16 a[120:123], a[204:207], v[16:19], a[120:123]\n"
  "v_exp_f32 v88, v88\n"
  "v_mfma_f32_16x16x32_bf16 a[120:123], a[236:239], v[48:51], a[120:123]\n"
  "buffer_load_dwordx4 v247, s[76:79], s46 offen offset:3072 lds\n"
  "v_mfma_f32_16x16x32_bf16 a[136:139], v[208:211], v[16:19], a[136:139]\n"
  "v_exp_f32 v89, v89\n"
  "v_mfma_f32_16x16x32_bf16 a[136:139], v[208:211], v[48:51], a[136:139]\n"
  "v_cvt_pk_bf16_f32 v88, v88, v89\n"
  "v_mfma_f32_16x16x32_bf16 a[12:15], a[176:179], v[24:27], a[12:15]\n"
  "v_exp_f32 v90, v90\n"
  "v_mfma_f32_16x16x32_bf16 a[12:15], a[208:211], v[56:59], a[12:15]\n"
  "v_mfma_f32_16x16x32_bf16 a[28:31], a[180:183], v[24:27], a[28:31]\n"
  "v_exp_f32 v91, v91\n"
  "v_mfma_f32_16x16x32_bf16 a[28:31], a[212:215], v[56:59], a[28:31]\n"
  "v_cvt_pk_bf16_f32 v89, v90, v91\n"
  "v_mfma_f32_16x16x32_bf16 a[44:47], a[184:187], v[24:27], a[44:47]\n"
  "v_exp_f32 v92, v92\n"
  "v_mfma_f32_16x16x32_bf16 a[44:47], a[216:219], v[56:59], a[44:47]\n"
  "v_mfma_f32_16x16x32_bf16 a[60:63], a[188:191], v[24:27], a[60:63]\n"
  "v_exp_f32 v93, v93\n"
  "v_mfma_f32_16x16x32_bf16 a[60:63], a[220:223], v[56:59], a[60:63]\n"
  "v_cvt_pk_bf16_f32 v90, v92, v93\n"
  "v_mfma_f32_16x16x32_bf16 a[76:79], a[192:195], v[24:27], a[76:79]\n"
  "v_exp_f32 v94, v94\n"
  "v_mfma_f32_16x16x32_bf16 a[76:79], a[224:227], v[56:59], a[76:79]\n"
  "v_mfma_f32_16x16x32_bf16 a[92:95], a[196:199], v[24:27], a[92:95]\n"
  "v_exp_f32 v95, v95\n"
  "v_mfma_f32_16x16x32_bf16 a[92:95], a[228:231], v[56:59], a[92:95]\n"
  "v_cvt_pk_bf16_f32 v91, v94, v95\n"
  "v_mfma_f32_16x16x32_bf16 a[108:111], a[200:203], v[24:27], a[108:111]\n"
  "v_exp_f32 v96, v96\n"
  "v_mfma_f32_16x16x32_bf16 a[108:111], a[232:235], v[56:59], a[108:111]\n"
  "v_mfma_f32_16x16x32_bf16 a[124:127], a[204:207], v[24:27], a[124:127]\n"
  "v_exp_f32 v97, v97\n"
  "v_mfma_f32_16x16x32_bf16 a[124:127], a[236:239], v[56:59], a[124:127]\n"
  "v_cvt_pk_bf16_f32 v96, v96, v97\n"
  "v_mfma_f32_16x16x32_bf16 a[140:143], v[208:211], v[24:27], a[140:143]\n"
  "v_exp_f32 v98, v98\n"
  "v_mfma_f32_16x16x32_bf16 a[140:143], v[208:211], v[56:59], a[140:143]\n"
  "v_exp_f32 v99, v99\n"
  "s_nop 0\n"
  "ds_read_b64_tr_b16 a[176:177], v216 offset:49152\n"
  "v_cvt_pk_bf16_f32 v97, v98, v99\n"
  "v_exp_f32 v100, v100\n"
  "s_nop 0\n"
  "ds_read_b64_tr_b16 a[178:179], v216 offset:53248\n"
  "ds_read_b64_tr_b16 a[208:209], v216 offset:57344\n"
  "v_exp_f32 v101, v101\n"
  "s_nop 0\n"
  "ds_read_b64_tr_b16 a[210:211], v216 offset:61440\n"
  "v_cvt_pk_bf16_f32 v98, v100, v101\n"
  "v_exp_f32 v102, v102\n"
  "s_nop 0\n"
  "ds_read_b64_tr_b16 a[180:181], v217 offset:49152\n"
  "ds_read_b64_tr_b16 a[182:183], v217 offset:53248\n"
  "v_exp_f32 v103, v103\n"
  "s_nop 0\n"
  "ds_read_b64_tr_b16 a[212:213], v217 offset:57344\n"
  "v_cvt_pk_bf16_f32 v99, v102, v103\n"
  "v_exp_f32 v104, v104\n"
  "s_nop 0\n"
  "ds_read_b64_tr_b16 a[214:215], v217 offset:61440\n"
  "ds_read_b64_tr_b16 a[184:185], v218 offset:49152\n"
  "v_exp_f32 v105, v105\n"
  "s_nop 0\n"
  "ds_read_b64_tr_b16 a[186:187], v218 offset:53248\n"
  "v_cvt_pk_bf16_f32 v104, v104, v105\n"
  "v_exp_f32 v106, v106\n"
  "s_nop 0\n"
  "ds_read_b64_tr_b16 a[216:217], v218 offset:57344\n"
  "ds_read_b64_tr_b16 a[218:219], v218 offset:61440\n"
  "v_exp_f32 v107, v107\n"
  "s_nop 0\n"
  "v_cvt_pk_bf16_f32 v105, v106, v107\n"
  "v_exp_f32 v108, v108\n"
  "v_exp_f32 v109, v109\n"
  "s_nop 0\n"
  "v_cvt_pk_bf16_f32 v106, v108, v109\n"
  "v_exp_f32 v110, v110\n"
  "s_nop 0\n"
  "ds_read_b64_tr_b16 a[188:189], v219 offset:49152\n"
  "ds_read_b64_tr_b16 a[190:191], v219 offset:53248\n"
  "v_exp_f32 v111, v111\n"
  "s_nop 0\n"
  "ds_read_b64_tr_b16 a[220:221], v219 offset:57344\n"
  "v_cvt_pk_bf16_f32 v107, v110, v111\n"
  "v_exp_f32 v112, v112\n"
  "s_nop 0\n"
  "ds_read_b64_tr_b16 a[222:223], v219 offset:61440\n"
  "ds_read_b64_tr_b16 a[192:193], v220 offset:49152\n"
  "v_exp_f32 v113, v113\n"
  "s_nop 0\n"
  "ds_read_b64_tr_b16 a[194:195], v220 offset:53248\n"
  "v_cvt_pk_bf16_f32 v112, v112, v113\n"
  "v_exp_f32 v114, v114\n"
  "s_nop 0\n"
  "ds_read_b64_tr_b16 a[224:225], v220 offset:57344\n"
  "ds_read_b64_tr_b16 a[226:227], v220 offset:61440\n"
  "v_exp_f32 v115, v115\n"
  "s_nop 0\n"
  "v_cvt_pk_bf16_f32 v113, v114, v115\n"
  "v_exp_f32 v116, v116\n"
  "v_exp_f32 v117, v117\n"
  "s_nop 0\n"
  "v_cvt_pk_bf16_f32 v114, v116, v117\n"
  "v_exp_f32 v118, v118\n"
  "s_nop 0\n"
  "ds_read_b64_tr_b16 a[196:197], v221 offset:49152\n"
  "ds_read_b64_tr_b16 a[198:199], v221 offset:53248\n"
  "v_exp_f32 v119, v119\n"
  "s_nop 0\n"
  "ds_read_b64_tr_b16 a[228:229], v221 offset:57344\n"
  "v_cvt_pk_bf16_f32 v115, v118, v119\n"
  "v_exp_f32 v120, v120\n"
  "s_nop 0\n"
  "ds_read_b64_tr_b16 a[230:231], v221 offset:61440\n"
  "ds_read_b64_tr_b16 a[200:201], v222 offset:49152\n"
  "v_exp_f32 v121, v121\n"
  "s_nop 0\n"
  "ds_read_b64_tr_b16 a[202:203], v222 offset:53248\n"
  "v_cvt_pk_bf16_f32 v120, v120, v121\n"
  "v_exp_f32 v122, v122\n"
  "s_nop 0\n"
  "ds_read_b64_tr_b16 a[232:233], v222 offset:57344\n"
  "ds_read_b64_tr_b16 a[234:235], v222 offset:61440\n"
  "v_exp_f32 v123, v123\n"
  "s_nop 0\n"
  "v_cvt_pk_bf16_f32 v121, v122, v123\n"
  "v_exp_f32 v124, v124\n"
  "s_nop 0\n"
  "ds_read_b64_tr_b16 a[204:205], v223 offset:49152\n"
  "ds_read_b64_tr_b16 a[206:207], v223 offset:53248\n"
  "v_exp_f32 v125, v125\n"
  "s_nop 0\n"
  "v_cvt_pk_bf16_f32 v122, v124, v125\n"
  "v_exp_f32 v126, v126\n"
  "s_nop 0\n"
  "ds_read_b64_tr_b16 a[236:237], v223 offset:57344\n"
  "ds_read_b64_tr_b16 a[238:239], v223 offset:61440\n"
  "v_exp_f32 v127, v127\n"
  "s_nop 0\n"
  "v_cvt_pk_bf16_f32 v123, v126, v127\n"
  "s_waitcnt lgkmcnt(0)\n"
  "v_mfma_f32_16x16x32_bf16 a[0:3], a[176:179], v[64:67], a[0:3]\n"
  "s_add_u32 m0, s42, 81920\n"
  "s_add_u32 s46, s53, 65536\n"
  "v_mfma_f32_16x16x32_bf16 a[0:3], a[208:211], v[96:99], a[0:3]\n"
  "buffer_load_dwordx4 v244, s[76:79], s46 offen offset:0 lds\n"
  "v_mfma_f32_16x16x32_bf16 a[16:19], a[180:183], v[64:67], a[16:19]\n"
  "v_mfma_f32_16x16x32_bf16 a[16:19], a[212:215], v[96:99], a[16:19]\n"
  "buffer_load_dwordx4 v245, s[76:79], s46 offen offset:1024 lds\n"
  "v_mfma_f32_16x16x32_bf16 a[32:35], a[184:187], v[64:67], a[32:35]\n"
  "v_mfma_f32_16x16x32_bf16 a[32:35], a[216:219], v[96:99], a[32:35]\n"
  "v_mfma_f32_16x16x32_bf16 a[48:51], a[188:191], v[64:67], a[48:51]\n"
  "v_mfma_f32_16x16x32_bf16 a[48:51], a[220:223], v[96:99], a[48:51]\n"
  "v_mfma_f32_16x16x32_bf16 a[64:67], a[192:195], v[64:67], a[64:67]\n"
  "v_mfma_f32_16x16x32_bf16 a[64:67], a[224:227], v[96:99], a[64:67]\n"
  "v_mfma_f32_16x16x32_bf16 a[80:83], a[196:199], v[64:67], a[80:83]\n"
  "v_mfma_f32_16x16x32_bf16 a[80:83], a[228:231], v[96:99], a[80:83]\n"
  "v_mfma_f32_16x16x32_bf16 a[96:99], a[200:203], v[64:67], a[96:99]\n"
  "v_mfma_f32_16x16x32_bf16 a[96:99], a[232:235], v[96:99], a[96:99]\n"
  "v_mfma_f32_16x16x32_bf16 a[112:115], a[204:207], v[64:67], a[112:115]\n"
  "v_mfma_f32_16x16x32_bf16 a[112:115], a[236:239], v[96:99], a[112:115]\n"
  "v_mfma_f32_16x16x32_bf16 a[128:131], v[208:211], v[64:67], a[128:131]\n"
  "v_mfma_f32_16x16x32_bf16 a[128:131], v[208:211], v[96:99], a[128:131]\n"
  "v_mfma_f32_16x16x32_bf16 a[4:7], a[176:179], v[72:75], a[4:7]\n"
  "v_mfma_f32_16x16x32_bf16 a[4:7], a[208:211], v[104:107], a[4:7]\n"
  "v_mfma_f32_16x16x32_bf16 a[20:23], a[180:183], v[72:75], a[20:23]\n"
  "v_mfma_f32_16x16x32_bf16 a[20:23], a[212:215], v[104:107], a[20:23]\n"
  "v_mfma_f32_16x16x32_bf16 a[36:39], a[184:187], v[72:75], a[36:39]\n"
  "v_mfma_f32_16x16x32_bf16 a[36:39], a[216:219], v[104:107], a[36:39]\n"
  "buffer_load_dwordx4 v246, s[76:79], s46 offen offset:2048 lds\n"
  "v_mfma_f32_16x16x32_bf16 a[52:55], a[188:191], v[72:75], a[52:55]\n"
  "v_mfma_f32_16x16x32_bf16 a[52:55], a[220:223], v[104:107], a[52:55]\n"
  "v_mfma_f32_16x16x32_bf16 a[68:71], a[192:195], v[72:75], a[68:71]\n"
  "v_mfma_f32_16x16x32_bf16 a[68:71], a[224:227], v[104:107], a[68:71]\n"
  "buffer_load_dwordx4 v247, s[76:79], s46 offen offset:3072 lds\n"
  "v_mfma_f32_16x16x32_bf16 a[84:87], a[196:199], v[72:75], a[84:87]\n"
  "v_mfma_f32_16x16x32_bf16 a[84:87], a[228:231], v[104:107], a[84:87]\n"
  "v_mfma_f32_16x16x32_bf16 a[100:103], a[200:203], v[72:75], a[100:103]\n"
  "v_mfma_f32_16x16x32_bf16 a[100:103], a[232:235], v[104:107], a[100:103]\n"
  "v_mfma_f32_16x16x32_bf16 a[116:119], a[204:207], v[72:75], a[116:119]\n"
  "v_mfma_f32_16x16x32_bf16 a[116:119], a[236:239], v[104:107], a[116:119]\n"
  "v_mfma_f32_16x16x32_bf16 a[132:135], v[208:211], v[72:75], a[132:135]\n"
  "v_mfma_f32_16x16x32_bf16 a[132:135], v[208:211], v[104:107], a[132:135]\n"
  "v_mfma_f32_16x16x32_bf16 a[8:11], a[176:179], v[80:83], a[8:11]\n"
  "v_mfma_f32_16x16x32_bf16 a[8:11], a[208:211], v[112:115], a[8:11]\n"
  "v_mfma_f32_16x16x32_bf16 a[24:27], a[180:183], v[80:83], a[24:27]\n"
  "s_add_u32 m0, s42, 49152\n"
  "s_add_u32 s46, s53, 98304\n"
  "v_mfma_f32_16x16x32_bf16 a[24:27], a[212:215], v[112:115], a[24:27]\n"
  "buffer_load_dwordx4 v244, s[76:79], s46 offen offset:0 lds\n"
  "v_mfma_f32_16x16x32_bf16 a[40:43], a[184:187], v[80:83], a[40:43]\n"
  "v_mfma_f32_16x16x32_bf16 a[40:43], a[216:219], v[112:115], a[40:43]\n"
  "v_mfma_f32_16x16x32_bf16 a[56:59], a[188:191], v[80:83], a[56:59]\n"
  "v_mfma_f32_16x16x32_bf16 a[56:59], a[220:223], v[112:115], a[56:59]\n"
  "buffer_load_dwordx4 v245, s[76:79], s46 offen offset:1024 lds\n"
  "v_mfma_f32_16x16x32_bf16 a[72:75], a[192:195], v[80:83], a[72:75]\n"
  "v_mfma_f32_16x16x32_bf16 a[72:75], a[224:227], v[112:115], a[72:75]\n"
  "v_mfma_f32_16x16x32_bf16 a[88:91], a[196:199], v[80:83], a[88:91]\n"
  "v_mfma_f32_16x16x32_bf16 a[88:91], a[228:231], v[112:115], a[88:91]\n"
  "buffer_load_dwordx4 v246, s[76:79], s46 offen offset:2048 lds\n"
  "v_mfma_f32_16x16x32_bf16 a[104:107], a[200:203], v[80:83], a[104:107]\n"
  "v_mfma_f32_16x16x32_bf16 a[104:107], a[232:235], v[112:115], a[104:107]\n"
  "v_mfma_f32_16x16x32_bf16 a[120:123], a[204:207], v[80:83], a[120:123]\n"
  "v_mfma_f32_16x16x32_bf16 a[120:123], a[236:239], v[112:115], a[120:123]\n"
  "buffer_load_dwordx4 v247, s[76:79], s46 offen offset:3072 lds\n"
  "v_mfma_f32_16x16x32_bf16 a[136:139], v[208:211], v[80:83], a[136:139]\n"
  "v_mfma_f32_16x16x32_bf16 a[136:139], v[208:211], v[112:115], a[136:139]\n"
  "v_mfma_f32_16x16x32_bf16 a[12:15], a[176:179], v[88:91], a[12:15]\n"
  "v_mfma_f32_16x16x32_bf16 a[12:15], a[208:211], v[120:123], a[12:15]\n"
  "v_mfma_f32_16x16x32_bf16 a[28:31], a[180:183], v[88:91], a[28:31]\n"
  "v_mfma_f32_16x16x32_bf16 a[28:31], a[212:215], v[120:123], a[28:31]\n"
  "v_mfma_f32_16x16x32_bf16 a[44:47], a[184:187], v[88:91], a[44:47]\n"
  "v_mfma_f32_16x16x32_bf16 a[44:47], a[216:219], v[120:123], a[44:47]\n"
  "v_mfma_f32_16x16x32_bf16 a[60:63], a[188:191], v[88:91], a[60:63]\n"
  "v_mfma_f32_16x16x32_bf16 a[60:63], a[220:223], v[120:123], a[60:63]\n"
  "v_mfma_f32_16x16x32_bf16 a[76:79], a[192:195], v[88:91], a[76:79]\n"
  "v_mfma_f32_16x16x32_bf16 a[76:79], a[224:227], v[120:123], a[76:79]\n"
  "v_mfma_f32_16x16x32_bf16 a[92:95], a[196:199], v[88:91], a[92:95]\n"
  "v_mfma_f32_16x16x32_bf16 a[92:95], a[228:231], v[120:123], a[92:95]\n"
  "v_mfma_f32_16x16x32_bf16 a[108:111], a[200:203], v[88:91], a[108:111]\n"
  "v_mfma_f32_16x16x32_bf16 a[108:111], a[232:235], v[120:123], a[108:111]\n"
  "v_mfma_f32_16x16x32_bf16 a[124:127], a[204:207], v[88:91], a[124:127]\n"
  "v_mfma_f32_16x16x32_bf16 a[124:127], a[236:239], v[120:123], a[124:127]\n"
  "v_mfma_f32_16x16x32_bf16 a[140:143], v[208:211], v[88:91], a[140:143]\n"
  "v_mfma_f32_16x16x32_bf16 a[140:143], v[208:211], v[120:123], a[140:143]\n"
  "s_add_u32 m0, s42, 98304\n"
  "s_add_u32 s46, s54, 0\n"
  "s_nop 0\n"
  "buffer_load_dwordx4 v244, s[76:79], s46 offen offset:0 lds\n"
  "buffer_load_dwordx4 v245, s[76:79], s46 offen offset:1024 lds\n"
  "buffer_load_dwordx4 v246, s[76:79], s46 offen offset:2048 lds\n"
  "buffer_load_dwordx4 v247, s[76:79], s46 offen offset:3072 lds\n"
  "s_nop 7\n"
  "s_mov_b32 s68, 0x4e800000\n"
  "v_accvgpr_read_b32 v64, a128\n"
  "v_accvgpr_read_b32 v65, a132\n"
  "v_accvgpr_read_b32 v66, a136\n"
  "v_accvgpr_read_b32 v67, a140\n"
  "v_mov_b32 v68, v64\n"
  "v_mov_b32 v69, v65\n"
  "v_mov_b32 v70, v66\n"
  "v_mov_b32 v71, v67\n"
  "s_nop 0\n"
  "v_permlane32_swap_b32 v64, v68\n"
  "v_permlane32_swap_b32 v65, v69\n"
  "v_permlane32_swap_b32 v66, v70\n"
  "v_permlane32_swap_b32 v67, v71\n"
  "s_nop 0\n"
  "v_add_f32 v64, v64, v68\n"
  "v_add_f32 v65, v65, v69\n"
  "v_add_f32 v66, v66, v70\n"
  "v_add_f32 v67, v67, v71\n"
  "v_mov_b32 v68, v64\n"
  "v_mov_b32 v69, v65\n"
  "v_mov_b32 v70, v66\n"
  "v_mov_b32 v71, v67\n"
  "s_nop 0\n"
  "v_permlane16_swap_b32 v64, v68\n"
  "v_permlane16_swap_b32 v65, v69\n"
  "v_permlane16_swap_b32 v66, v70\n"
  "v_permlane16_swap_b32 v67, v71\n"
  "s_nop 0\n"
  "v_add_f32 v64, v64, v68\n"
  "v_add_f32 v65, v65, v69\n"
  "v_add_f32 v66, v66, v70\n"
  "v_add_f32 v67, v67, v71\n"
  "v_rcp_f32 v212, v64\n"
  "v_rcp_f32 v213, v65\n"
  "v_rcp_f32 v214, v66\n"
  "v_rcp_f32 v215, v67\n"
  "v_add_f32 v69, v64, v65\n"
  "v_add_f32 v70, v66, v67\n"
  "v_add_f32 v69, v69, v70\n"
  "v_cmp_nge_f32 vcc, s68, v69\n"
  "s_lshl_b32 s45, s74, 5\n"
  "s_add_u32 s45, s45, s71\n"
  "s_lshl_b32 s45, s45, 16\n"
  "s_mov_b64 s[36:37], %[ws]\n"
  "s_add_u32 s36, s36, s45\n"
  "s_addc_u32 s37, s37, 0\n"
  "s_and_b32 s37, s37, 0xffff\n"
  "s_mov_b32 s38, 0x10000\n"
  "s_mov_b32 s39, 0x20000\n"
  "s_or_b32 s70, vcc_lo, vcc_hi\n"
  "v_lshlrev_b32 v239, 4, v236\n"
  "s_lshl_b32 s45, %[wid], 14\n"
  "v_mov_b32 v240, v212\n"
  "v_mov_b32 v241, v212\n"
  "v_accvgpr_read_b32 v0, a0\n"
  "v_accvgpr_read_b32 v1, a1\n"
  "v_accvgpr_read_b32 v2, a2\n"
  "v_accvgpr_read_b32 v3, a3\n"
  "v_accvgpr_read_b32 v4, a16\n"
  "v_accvgpr_read_b32 v5, a17\n"
  "v_accvgpr_read_b32 v6, a18\n"
  "v_accvgpr_read_b32 v7, a19\n"
  "v_pk_mul_f32 v[0:1], v[0:1], v[240:241]\n"
  "v_pk_mul_f32 v[2:3], v[2:3], v[240:241]\n"
  "v_pk_mul_f32 v[4:5], v[4:5], v[240:241]\n"
  "v_pk_mul_f32 v[6:7], v[6:7], v[240:241]\n"
  "v_cvt_pk_bf16_f32 v8, v0, v1\n"
  "v_cvt_pk_bf16_f32 v9, v2, v3\n"
  "v_cvt_pk_bf16_f32 v10, v4, v5\n"
  "v_cvt_pk_bf16_f32 v11, v6, v7\n"
  "s_add_u32 s46, s45, 0\n"
  "buffer_store_dwordx4 v[8:11], v239, s[36:39], s46 offen sc1\n"
  "v_accvgpr_read_b32 v16, a32\n"
  "v_accvgpr_read_b32 v17, a33\n"
  "v_accvgpr_read_b32 v18, a34\n"
  "v_accvgpr_read_b32 v19, a35\n"
  "v_accvgpr_read_b32 v20, a48\n"
  "v_accvgpr_read_b32 v21, a49\n"
  "v_accvgpr_read_b32 v22, a50\n"
  "v_accvgpr_read_b32 v23, a51\n"
  "v_pk_mul_f32 v[16:17], v[16:17], v[240:241]\n"
  "v_pk_mul_f32 v[18:19], v[18:19], v[240:241]\n"
  "v_pk_mul_f32 v[20:21], v[20:21], v[240:241]\n"
  "v_pk_mul_f32 v[22:23], v[22:23], v[240:241]\n"
  "v_cvt_pk_bf16_f32 v24, v16, v17\n"
  "v_cvt_pk_bf16_f32 v25, v18, v19\n"
  "v_cvt_pk_bf16_f32 v26, v20, v21\n"
  "v_cvt_pk_bf16_f32 v27, v22, v23\n"
  "s_add_u32 s46, s45, 1024\n"
  "buffer_store_dwordx4 v[24:27], v239, s[36:39], s46 offen sc1\n"
  "v_accvgpr_read_b32 v32, a64\n"
  "v_accvgpr_read_b32 v33, a65\n"
  "v_accvgpr_read_b32 v34, a66\n"
  "v_accvgpr_read_b32 v35, a67\n"
  "v_accvgpr_read_b32 v36, a80\n"
  "v_accvgpr_read_b32 v37, a81\n"
  "v_accvgpr_read_b32 v38, a82\n"
  "v_accvgpr_read_b32 v39, a83\n"
  "v_pk_mul_f32 v[32:33], v[32:33], v[240:241]\n"
  "v_pk_mul_f32 v[34:35], v[34:35], v[240:241]\n"
  "v_pk_mul_f32 v[36:37], v[36:37], v[240:241]\n"
  "v_pk_mul_f32 v[38:39], v[38:39], v[240:241]\n"
  "v_cvt_pk_bf16_f32 v40, v32, v33\n"
  "v_cvt_pk_bf16_f32 v41, v34, v35\n"
  "v_cvt_pk_bf16_f32 v42, v36, v37\n"
  "v_cvt_pk_bf16_f32 v43, v38, v39\n"
  "s_add_u32 s46, s45, 2048\n"
  "buffer_store_dwordx4 v[40:43], v239, s[36:39], s46 offen sc1\n"
  "v_accvgpr_read_b32 v48, a96\n"
  "v_accvgpr_read_b32 v49, a97\n"
  "v_accvgpr_read_b32 v50, a98\n"
  "v_accvgpr_read_b32 v51, a99\n"
  "v_accvgpr_read_b32 v52, a112\n"
  "v_accvgpr_read_b32 v53, a113\n"
  "v_accvgpr_read_b32 v54, a114\n"
  "v_accvgpr_read_b32 v55, a115\n"
  "v_pk_mul_f32 v[48:49], v[48:49], v[240:241]\n"
  "v_pk_mul_f32 v[50:51], v[50:51], v[240:241]\n"
  "v_pk_mul_f32 v[52:53], v[52:53], v[240:241]\n"
  "v_pk_mul_f32 v[54:55], v[54:55], v[240:241]\n"
  "v_cvt_pk_bf16_f32 v56, v48, v49\n"
  "v_cvt_pk_bf16_f32 v57, v50, v51\n"
  "v_cvt_pk_bf16_f32 v58, v52, v53\n"
  "v_cvt_pk_bf16_f32 v59, v54, v55\n"
  "s_add_u32 s46, s45, 3072\n"
  "buffer_store_dwordx4 v[56:59], v239, s[36:39], s46 offen sc1\n"
  "v_mov_b32 v240, v213\n"
  "v_mov_b32 v241, v213\n"
  "v_accvgpr_read_b32 v0, a4\n"
  "v_accvgpr_read_b32 v1, a5\n"
  "v_accvgpr_read_b32 v2, a6\n"
  "v_accvgpr_read_b32 v3, a7\n"
  "v_accvgpr_read_b32 v4, a20\n"
  "v_accvgpr_read_b32 v5, a21\n"
  "v_accvgpr_read_b32 v6, a22\n"
  "v_accvgpr_read_b32 v7, a23\n"
  "v_pk_mul_f32 v[0:1], v[0:1], v[240:241]\n"
  "v_pk_mul_f32 v[2:3], v[2:3], v[240:241]\n"
  "v_pk_mul_f32 v[4:5], v[4:5], v[240:241]\n"
  "v_pk_mul_f32 v[6:7], v[6:7], v[240:241]\n"
  "v_cvt_pk_bf16_f32 v8, v0, v1\n"
  "v_cvt_pk_bf16_f32 v9, v2, v3\n"
  "v_cvt_pk_bf16_f32 v10, v4, v5\n"
  "v_cvt_pk_bf16_f32 v11, v6, v7\n"
  "s_add_u32 s46, s45, 4096\n"
  "buffer_store_dwordx4 v[8:11], v239, s[36:39], s46 offen sc1\n"
  "v_accvgpr_read_b32 v16, a36\n"
  "v_accvgpr_read_b32 v17, a37\n"
  "v_accvgpr_read_b32 v18, a38\n"
  "v_accvgpr_read_b32 v19, a39\n"
  "v_accvgpr_read_b32 v20, a52\n"
  "v_accvgpr_read_b32 v21, a53\n"
  "v_accvgpr_read_b32 v22, a54\n"
  "v_accvgpr_read_b32 v23, a55\n"
  "v_pk_mul_f32 v[16:17], v[16:17], v[240:241]\n"
  "v_pk_mul_f32 v[18:19], v[18:19], v[240:241]\n"
  "v_pk_mul_f32 v[20:21], v[20:21], v[240:241]\n"
  "v_pk_mul_f32 v[22:23], v[22:23], v[240:241]\n"
  "v_cvt_pk_bf16_f32 v24, v16, v17\n"
  "v_cvt_pk_bf16_f32 v25, v18, v19\n"
  "v_cvt_pk_bf16_f32 v26, v20, v21\n"
  "v_cvt_pk_bf16_f32 v27, v22, v23\n"
  "s_add_u32 s46, s45, 5120\n"
  "buffer_store_dwordx4 v[24:27], v239, s[36:39], s46 offen sc1\n"
  "v_accvgpr_read_b32 v32, a68\n"
  "v_accvgpr_read_b32 v33, a69\n"
  "v_accvgpr_read_b32 v34, a70\n"
  "v_accvgpr_read_b32 v35, a71\n"
  "v_accvgpr_read_b32 v36, a84\n"
  "v_accvgpr_read_b32 v37, a85\n"
  "v_accvgpr_read_b32 v38, a86\n"
  "v_accvgpr_read_b32 v39, a87\n"
  "v_pk_mul_f32 v[32:33], v[32:33], v[240:241]\n"
  "v_pk_mul_f32 v[34:35], v[34:35], v[240:241]\n"
  "v_pk_mul_f32 v[36:37], v[36:37], v[240:241]\n"
  "v_pk_mul_f32 v[38:39], v[38:39], v[240:241]\n"
  "v_cvt_pk_bf16_f32 v40, v32, v33\n"
  "v_cvt_pk_bf16_f32 v41, v34, v35\n"
  "v_cvt_pk_bf16_f32 v42, v36, v37\n"
  "v_cvt_pk_bf16_f32 v43, v38, v39\n"
  "s_add_u32 s46, s45, 6144\n"
  "buffer_store_dwordx4 v[40:43], v239, s[36:39], s46 offen sc1\n"
  "v_accvgpr_read_b32 v48, a100\n"
  "v_accvgpr_read_b32 v49, a101\n"
  "v_accvgpr_read_b32 v50, a102\n"
  "v_accvgpr_read_b32 v51, a103\n"
  "v_accvgpr_read_b32 v52, a116\n"
  "v_accvgpr_read_b32 v53, a117\n"
  "v_accvgpr_read_b32 v54, a118\n"
  "v_accvgpr_read_b32 v55, a119\n"
  "v_pk_mul_f32 v[48:49], v[48:49], v[240:241]\n"
  "v_pk_mul_f32 v[50:51], v[50:51], v[240:241]\n"
  "v_pk_mul_f32 v[52:53], v[52:53], v[240:241]\n"
  "v_pk_mul_f32 v[54:55], v[54:55], v[240:241]\n"
  "v_cvt_pk_bf16_f32 v56, v48, v49\n"
  "v_cvt_pk_bf16_f32 v57, v50, v51\n"
  "v_cvt_pk_bf16_f32 v58, v52, v53\n"
  "v_cvt_pk_bf16_f32 v59, v54, v55\n"
  "s_add_u32 s46, s45, 7168\n"
  "buffer_store_dwordx4 v[56:59], v239, s[36:39], s46 offen sc1\n"
  "v_mov_b32 v240, v214\n"
  "v_mov_b32 v241, v214\n"
  "v_accvgpr_read_b32 v0, a8\n"
  "v_accvgpr_read_b32 v1, a9\n"
  "v_accvgpr_read_b32 v2, a10\n"
  "v_accvgpr_read_b32 v3, a11\n"
  "v_accvgpr_read_b32 v4, a24\n"
  "v_accvgpr_read_b32 v5, a25\n"
  "v_accvgpr_read_b32 v6, a26\n"
  "v_accvgpr_read_b32 v7, a27\n"
  "v_pk_mul_f32 v[0:1], v[0:1], v[240:241]\n"
  "v_pk_mul_f32 v[2:3], v[2:3], v[240:241]\n"
  "v_pk_mul_f32 v[4:5], v[4:5], v[240:241]\n"
  "v_pk_mul_f32 v[6:7], v[6:7], v[240:241]\n"
  "v_cvt_pk_bf16_f32 v8, v0, v1\n"
  "v_cvt_pk_bf16_f32 v9, v2, v3\n"
  "v_cvt_pk_bf16_f32 v10, v4, v5\n"
  "v_cvt_pk_bf16_f32 v11, v6, v7\n"
  "s_add_u32 s46, s45, 8192\n"
  "buffer_store_dwordx4 v[8:11], v239, s[36:39], s46 offen sc1\n"
  "v_accvgpr_read_b32 v16, a40\n"
  "v_accvgpr_read_b32 v17, a41\n"
  "v_accvgpr_read_b32 v18, a42\n"
  "v_accvgpr_read_b32 v19, a43\n"
  "v_accvgpr_read_b32 v20, a56\n"
  "v_accvgpr_read_b32 v21, a57\n"
  "v_accvgpr_read_b32 v22, a58\n"
  "v_accvgpr_read_b32 v23, a59\n"
  "v_pk_mul_f32 v[16:17], v[16:17], v[240:241]\n"
  "v_pk_mul_f32 v[18:19], v[18:19], v[240:241]\n"
  "v_pk_mul_f32 v[20:21], v[20:21], v[240:241]\n"
  "v_pk_mul_f32 v[22:23], v[22:23], v[240:241]\n"
  "v_cvt_pk_bf16_f32 v24, v16, v17\n"
  "v_cvt_pk_bf16_f32 v25, v18, v19\n"
  "v_cvt_pk_bf16_f32 v26, v20, v21\n"
  "v_cvt_pk_bf16_f32 v27, v22, v23\n"
  "s_add_u32 s46, s45, 9216\n"
  "buffer_store_dwordx4 v[24:27], v239, s[36:39], s46 offen sc1\n"
  "v_accvgpr_read_b32 v32, a72\n"
  "v_accvgpr_read_b32 v33, a73\n"
  "v_accvgpr_read_b32 v34, a74\n"
  "v_accvgpr_read_b32 v35, a75\n"
  "v_accvgpr_read_b32 v36, a88\n"
  "v_accvgpr_read_b32 v37, a89\n"
  "v_accvgpr_read_b32 v38, a90\n"
  "v_accvgpr_read_b32 v39, a91\n"
  "v_pk_mul_f32 v[32:33], v[32:33], v[240:241]\n"
  "v_pk_mul_f32 v[34:35], v[34:35], v[240:241]\n"
  "v_pk_mul_f32 v[36:37], v[36:37], v[240:241]\n"
  "v_pk_mul_f32 v[38:39], v[38:39], v[240:241]\n"
  "v_cvt_pk_bf16_f32 v40, v32, v33\n"
  "v_cvt_pk_bf16_f32 v41, v34, v35\n"
  "v_cvt_pk_bf16_f32 v42, v36, v37\n"
  "v_cvt_pk_bf16_f32 v43, v38, v39\n"
  "s_add_u32 s46, s45, 10240\n"
  "buffer_store_dwordx4 v[40:43], v239, s[36:39], s46 offen sc1\n"
  "v_accvgpr_read_b32 v48, a104\n"
  "v_accvgpr_read_b32 v49, a105\n"
  "v_accvgpr_read_b32 v50, a106\n"
  "v_accvgpr_read_b32 v51, a107\n"
  "v_accvgpr_read_b32 v52, a120\n"
  "v_accvgpr_read_b32 v53, a121\n"
  "v_accvgpr_read_b32 v54, a122\n"
  "v_accvgpr_read_b32 v55, a123\n"
  "v_pk_mul_f32 v[48:49], v[48:49], v[240:241]\n"
  "v_pk_mul_f32 v[50:51], v[50:51], v[240:241]\n"
  "v_pk_mul_f32 v[52:53], v[52:53], v[240:241]\n"
  "v_pk_mul_f32 v[54:55], v[54:55], v[240:241]\n"
  "v_cvt_pk_bf16_f32 v56, v48, v49\n"
  "v_cvt_pk_bf16_f32 v57, v50, v51\n"
  "v_cvt_pk_bf16_f32 v58, v52, v53\n"
  "v_cvt_pk_bf16_f32 v59, v54, v55\n"
  "s_add_u32 s46, s45, 11264\n"
  "buffer_store_dwordx4 v[56:59], v239, s[36:39], s46 offen sc1\n"
  "v_mov_b32 v240, v215\n"
  "v_mov_b32 v241, v215\n"
  "v_accvgpr_read_b32 v0, a12\n"
  "v_accvgpr_read_b32 v1, a13\n"
  "v_accvgpr_read_b32 v2, a14\n"
  "v_accvgpr_read_b32 v3, a15\n"
  "v_accvgpr_read_b32 v4, a28\n"
  "v_accvgpr_read_b32 v5, a29\n"
  "v_accvgpr_read_b32 v6, a30\n"
  "v_accvgpr_read_b32 v7, a31\n"
  "v_pk_mul_f32 v[0:1], v[0:1], v[240:241]\n"
  "v_pk_mul_f32 v[2:3], v[2:3], v[240:241]\n"
  "v_pk_mul_f32 v[4:5], v[4:5], v[240:241]\n"
  "v_pk_mul_f32 v[6:7], v[6:7], v[240:241]\n"
  "v_cvt_pk_bf16_f32 v8, v0, v1\n"
  "v_cvt_pk_bf16_f32 v9, v2, v3\n"
  "v_cvt_pk_bf16_f32 v10, v4, v5\n"
  "v_cvt_pk_bf16_f32 v11, v6, v7\n"
  "s_add_u32 s46, s45, 12288\n"
  "buffer_store_dwordx4 v[8:11], v239, s[36:39], s46 offen sc1\n"
  "v_accvgpr_read_b32 v16, a44\n"
  "v_accvgpr_read_b32 v17, a45\n"
  "v_accvgpr_read_b32 v18, a46\n"
  "v_accvgpr_read_b32 v19, a47\n"
  "v_accvgpr_read_b32 v20, a60\n"
  "v_accvgpr_read_b32 v21, a61\n"
  "v_accvgpr_read_b32 v22, a62\n"
  "v_accvgpr_read_b32 v23, a63\n"
  "v_pk_mul_f32 v[16:17], v[16:17], v[240:241]\n"
  "v_pk_mul_f32 v[18:19], v[18:19], v[240:241]\n"
  "v_pk_mul_f32 v[20:21], v[20:21], v[240:241]\n"
  "v_pk_mul_f32 v[22:23], v[22:23], v[240:241]\n"
  "v_cvt_pk_bf16_f32 v24, v16, v17\n"
  "v_cvt_pk_bf16_f32 v25, v18, v19\n"
  "v_cvt_pk_bf16_f32 v26, v20, v21\n"
  "v_cvt_pk_bf16_f32 v27, v22, v23\n"
  "s_add_u32 s46, s45, 13312\n"
  "buffer_store_dwordx4 v[24:27], v239, s[36:39], s46 offen sc1\n"
  "v_accvgpr_read_b32 v32, a76\n"
  "v_accvgpr_read_b32 v33, a77\n"
  "v_accvgpr_read_b32 v34, a78\n"
  "v_accvgpr_read_b32 v35, a79\n"
  "v_accvgpr_read_b32 v36, a92\n"
  "v_accvgpr_read_b32 v37, a93\n"
  "v_accvgpr_read_b32 v38, a94\n"
  "v_accvgpr_read_b32 v39, a95\n"
  "v_pk_mul_f32 v[32:33], v[32:33], v[240:241]\n"
  "v_pk_mul_f32 v[34:35], v[34:35], v[240:241]\n"
  "v_pk_mul_f32 v[36:37], v[36:37], v[240:241]\n"
  "v_pk_mul_f32 v[38:39], v[38:39], v[240:241]\n"
  "v_cvt_pk_bf16_f32 v40, v32, v33\n"
  "v_cvt_pk_bf16_f32 v41, v34, v35\n"
  "v_cvt_pk_bf16_f32 v42, v36, v37\n"
  "v_cvt_pk_bf16_f32 v43, v38, v39\n"
  "s_add_u32 s46, s45, 14336\n"
  "buffer_store_dwordx4 v[40:43], v239, s[36:39], s46 offen sc1\n"
  "v_accvgpr_read_b32 v48, a108\n"
  "v_accvgpr_read_b32 v49, a109\n"
  "v_accvgpr_read_b32 v50, a110\n"
  "v_accvgpr_read_b32 v51, a111\n"
  "v_accvgpr_read_b32 v52, a124\n"
  "v_accvgpr_read_b32 v53, a125\n"
  "v_accvgpr_read_b32 v54, a126\n"
  "v_accvgpr_read_b32 v55, a127\n"
  "v_pk_mul_f32 v[48:49], v[48:49], v[240:241]\n"
  "v_pk_mul_f32 v[50:51], v[50:51], v[240:241]\n"
  "v_pk_mul_f32 v[52:53], v[52:53], v[240:241]\n"
  "v_pk_mul_f32 v[54:55], v[54:55], v[240:241]\n"
  "v_cvt_pk_bf16_f32 v56, v48, v49\n"
  "v_cvt_pk_bf16_f32 v57, v50, v51\n"
  "v_cvt_pk_bf16_f32 v58, v52, v53\n"
  "v_cvt_pk_bf16_f32 v59, v54, v55\n"
  "s_add_u32 s46, s45, 15360\n"
  "buffer_store_dwordx4 v[56:59], v239, s[36:39], s46 offen sc1\n"
  "s_mov_b32 %[flag], s70\n"
  "s_mov_b32 %[tag_lo], s72\n"
  "s_mov_b32 %[tag_hi], s73\n"
  : [flag] "=&s"(flag), [tag_lo] "=&s"(tag_lo), [tag_hi] "=&s"(tag_hi)
  : [x] "s"(x), [kc] "s"(Kc), [vc] "s"(Vc), [wq] "s"(wq), [wo] "s"(wo), [ws] "s"(ws), [ws_lo] "s"((unsigned)(unsigned long)ws), [ws_hi] "s"((unsigned)((unsigned long)ws >> 32)),
    [bid] "s"(bid), [wid] "s"(wid)
  : "v0", "v1", "v2", "v3", "v4", "v5", "v6", "v7", "v8", "v9", "v10", "v11", "v12", "v13", "v14", "v15", "v16", "v17", "v18", "v19", "v20", "v21", "v22", "v23", "v24", "v25", "v26", "v27", "v28", "v29", "v30", "v31", "v32", "v33", "v34", "v35", "v36", "v37", "v38", "v39", "v40", "v41", "v42", "v43", "v44", "v45", "v46", "v47", "v48", "v49", "v50", "v51", "v52", "v53", "v54", "v55", "v56", "v57", "v58", "v59", "v60", "v61", "v62", "v63", "v64", "v65", "v66", "v67", "v68", "v69", "v70", "v71", "v72", "v73", "v74", "v75", "v76", "v77", "v78", "v79", "v80", "v81", "v82", "v83", "v84", "v85", "v86", "v87", "v88", "v89", "v90", "v91", "v92", "v93", "v94", "v95", "v96", "v97", "v98", "v99", "v100", "v101", "v102", "v103", "v104", "v105", "v106", "v107", "v108", "v109", "v110", "v111", "v112", "v113", "v114", "v115", "v116", "v117", "v118", "v119", "v120", "v121", "v122", "v123", "v124", "v125", "v126", "v127", "v128", "v129", "v130", "v131", "v132", "v133", "v134", "v135", "v136", "v137", "v138", "v139", "v140", "v141", "v142", "v143", "v144", "v145", "v146", "v147", "v148", "v149", "v150", "v151", "v152", "v153", "v154", "v155", "v156", "v157", "v158", "v159", "v160", "v161", "v162", "v163", "v164", "v165", "v166", "v167", "v168", "v169", "v170", "v171", "v172", "v173", "v174", "v175", "v176", "v177", "v178", "v179", "v180", "v181", "v182", "v183", "v184", "v185", "v186", "v187", "v188", "v189", "v190", "v191", "v192", "v193", "v194", "v195", "v196", "v197", "v198", "v199", "v200", "v201", "v202", "v203", "v204", "v205", "v206", "v207", "v208", "v209", "v210", "v211", "v212", "v213", "v214", "v215", "v216", "v217", "v218", "v219", "v220", "v221", "v222", "v223", "v224", "v225", "v226", "v227", "v228", "v229", "v230", "v231", "v232", "v233", "v234", "v235", "v236", "v237", "v238", "v239", "v240", "v241", "v242", "v243", "v244", "v245", "v246", "v247", "v248", "v249", "v250", "v251", "v252", "v253", "v254", "v255", "a0", "a1", "a2", "a3", "a4", "a5", "a6", "a7", "a8", "a9", "a10", "a11", "a12", "a13", "a14", "a15", "a16", "a17", "a18", "a19", "a20", "a21", "a22", "a23", "a24", "a25", "a26", "a27", "a28", "a29", "a30", "a31", "a32", "a33", "a34", "a35", "a36", "a37", "a38", "a39", "a40", "a41", "a42", "a43", "a44", "a45", "a46", "a47", "a48", "a49", "a50", "a51", "a52", "a53", "a54", "a55", "a56", "a57", "a58", "a59", "a60", "a61", "a62", "a63", "a64", "a65", "a66", "a67", "a68", "a69", "a70", "a71", "a72", "a73", "a74", "a75", "a76", "a77", "a78", "a79", "a80", "a81", "a82", "a83", "a84", "a85", "a86", "a87", "a88", "a89", "a90", "a91", "a92", "a93", "a94", "a95", "a96", "a97", "a98", "a99", "a100", "a101", "a102", "a103", "a104", "a105", "a106", "a107", "a108", "a109", "a110", "a111", "a112", "a113", "a114", "a115", "a116", "a117", "a118", "a119", "a120", "a121", "a122", "a123", "a124", "a125", "a126", "a127", "a128", "a129", "a130", "a131", "a132", "a133", "a134", "a135", "a136", "a137", "a138", "a139", "a140", "a141", "a142", "a143", "a144", "a145", "a146", "a147", "a148", "a149", "a150", "a151", "a152", "a153", "a154", "a155", "a156", "a157", "a158", "a159", "a160", "a161", "a162", "a163", "a164", "a165", "a166", "a167", "a168", "a169", "a170", "a171", "a172", "a173", "a174", "a175", "a176", "a177", "a178", "a179", "a180", "a181", "a182", "a183", "a184", "a185", "a186", "a187", "a188", "a189", "a190", "a191", "a192", "a193", "a194", "a195", "a196", "a197", "a198", "a199", "a200", "a201", "a202", "a203", "a204", "a205", "a206", "a207", "a208", "a209", "a210", "a211", "a212", "a213", "a214", "a215", "a216", "a217", "a218", "a219", "a220", "a221", "a222", "a223", "a224", "a225", "a226", "a227", "a228", "a229", "a230", "a231", "a232", "a233", "a234", "a235", "a236", "a237", "a238", "a239", "a240", "a241", "a242", "a243", "a244", "a245", "a246", "a247", "a248", "a249", "a250", "a251", "a252", "a253", "a254", "a255", "s20", "s21", "s22", "s23", "s24", "s25", "s26", "s27", "s28", "s29", "s30", "s31", "s36", "s37", "s38", "s39", "s40", "s41", "s42", "s43", "s44", "s45", "s46", "s47", "s48", "s49", "s50", "s51", "s52", "s53", "s54", "s55", "s56", "s57", "s58", "s59", "s60", "s61", "s62", "s63", "s64", "s65", "s66", "s67", "s68", "s69", "s70", "s71", "s72", "s73", "s74", "s75", "s76", "s77", "s78", "s79", "s80", "s81", "s82", "s83", "s84", "s85", "s86", "s87", "vcc", "scc", "memory");
  if (__builtin_expect(flag != 0, 0)) {
    const int lane = __builtin_amdgcn_mbcnt_hi(~0u, __builtin_amdgcn_mbcnt_lo(~0u, 0u));
    asm volatile("s_waitcnt vmcnt(0)" ::: "memory");
    for (int qb = 0; qb < 2; ++qb) safe_unit(x, Kc, Vc, wq, (unsigned short*)ws, bid & 7, bid >> 3, 64 * wid + 32 * qb, lane);
  }
  asm volatile(
  "v_mbcnt_lo_u32_b32 v249, -1, 0\n"
  "v_mbcnt_hi_u32_b32 v249, -1, v249\n"
  "s_lshr_b32 s75, %[bid], 3\n"
  "s_mov_b32 s71, s75\n"
  "s_and_b32 s74, %[bid], 7\n"
  "s_and_b32 s45, %[bid], 7\n"
  "s_lshl_b32 s45, s45, 21\n"
  "s_mov_b64 s[20:21], %[ws]\n"
  "s_add_u32 s20, s20, s45\n"
  "s_addc_u32 s21, s21, 0\n"
  "s_and_b32 s21, s21, 0xffff\n"
  "s_mov_b32 s22, 0x200000\n"
  "s_mov_b32 s23, 0x20000\n"
  "s_lshr_b32 s68, s75, 2\n"
  "s_lshl_b32 s68, s68, 7\n"
  "s_mov_b64 s[76:77], %[wo]\n"
  "s_add_u32 s76, s76, s68\n"
  "s_addc_u32 s77, s77, 0\n"
  "s_and_b32 s77, s77, 0xffff\n"
  "s_mov_b32 s78, 0\n"
  "s_mov_b32 s79, 0x20000\n"
  "s_sub_u32 s78, 0x400000, s68\n"
  "s_and_b32 s45, %[bid], 7\n"
  "s_lshl_b32 s45, s45, 18\n"
  "s_and_b32 s46, s75, 3\n"
  "s_lshl_b32 s46, s46, 16\n"
  "s_add_u32 s45, s45, s46\n"
  "s_lshr_b32 s46, s75, 2\n"
  "s_lshl_b32 s46, s46, 7\n"
  "s_add_u32 s45, s45, s46\n"
  "s_mov_b64 s[24:25], %[out]\n"
  "s_add_u32 s24, s24, s45\n"
  "s_addc_u32 s25, s25, 0\n"
  "s_and_b32 s25, s25, 0xffff\n"
  "s_mov_b32 s26, 0x10000\n"
  "s_mov_b32 s27, 0x20000\n"
  "s_lshl_b32 s42, %[wid], 12\n"
  "s_lshl_b32 s47, %[wid], 13\n"
  "s_add_u32 s47, s47, 131072\n"
  "s_lshl_b32 s40, %[wid], 19\n"
  "s_and_b32 s46, s75, 3\n"
  "s_lshl_b32 s46, s46, 14\n"
  "s_add_u32 s40, s40, s46\n"
  "s_lshl_b32 s52, %[wid], 20\n"
  "s_add_u32 s52, s52, 0\n"
  "s_add_u32 s60, s40, 0\n"
  "s_lshl_b32 s53, %[wid], 20\n"
  "s_add_u32 s53, s53, 131072\n"
  "s_add_u32 s61, s40, 65536\n"
  "s_lshl_b32 s54, %[wid], 20\n"
  "s_add_u32 s54, s54, 262144\n"
  "s_add_u32 s62, s40, 131072\n"
  "s_lshl_b32 s55, %[wid], 20\n"
  "s_add_u32 s55, s55, 393216\n"
  "s_add_u32 s63, s40, 196608\n"
  "s_lshl_b32 s56, %[wid], 20\n"
  "s_add_u32 s56, s56, 524288\n"
  "s_add_u32 s64, s40, 262144\n"
  "s_lshl_b32 s57, %[wid], 20\n"
  "s_add_u32 s57, s57, 655360\n"
  "s_add_u32 s65, s40, 327680\n"
  "s_lshl_b32 s58, %[wid], 20\n"
  "s_add_u32 s58, s58, 786432\n"
  "s_add_u32 s66, s40, 393216\n"
  "s_lshl_b32 s59, %[wid], 20\n"
  "s_add_u32 s59, s59, 917504\n"
  "s_add_u32 s67, s40, 458752\n"
  "v_lshrrev_b32 v239, 4, v249\n"
  "v_and_b32 v240, 7, v249\n"
  "v_lshrrev_b32 v241, 3, v249\n"
  "v_lshlrev_b32 v242, 1, v239\n"
  "v_xor_b32 v242, v242, v240\n"
  "v_lshlrev_b32 v242, 4, v242\n"
  "v_lshl_add_u32 v242, v241, 10, v242\n"
  "v_add_u32 v244, 0, v242\n"
  "v_add_u32 v245, 7168, v242\n"
  "v_add_u32 v246, 14336, v242\n"
  "v_add_u32 v247, 21504, v242\n"
  "v_lshlrev_b32 v224, 4, v249\n"
  "v_and_b32 v238, 15, v249\n"
  "v_lshrrev_b32 v240, 2, v238\n"
  "v_and_b32 v241, 3, v249\n"
  "v_lshl_add_u32 v242, v239, 2, v240\n"
  "v_lshrrev_b32 v243, 1, v242\n"
  "v_and_b32 v243, 3, v243\n"
  "v_lshlrev_b32 v242, 7, v242\n"
  "v_lshl_add_u32 v242, v241, 3, v242\n"
  "v_xor_b32 v240, 0, v243\n"
  "v_lshl_add_u32 v240, v240, 5, v242\n"
  "v_add_u32 v226, s42, v240\n"
  "v_add_u32 v230, 0x10000, v226\n"
  "v_add_u32 v234, s47, v240\n"
  "v_xor_b32 v240, 1, v243\n"
  "v_lshl_add_u32 v240, v240, 5, v242\n"
  "v_add_u32 v227, s42, v240\n"
  "v_add_u32 v231, 0x10000, v227\n"
  "v_add_u32 v235, s47, v240\n"
  "v_xor_b32 v240, 2, v243\n"
  "v_lshl_add_u32 v240, v240, 5, v242\n"
  "v_add_u32 v228, s42, v240\n"
  "v_add_u32 v232, 0x10000, v228\n"
  "v_add_u32 v236, s47, v240\n"
  "v_xor_b32 v240, 3, v243\n"
  "v_lshl_add_u32 v240, v240, 5, v242\n"
  "v_add_u32 v229, s42, v240\n"
  "v_add_u32 v233, 0x10000, v229\n"
  "v_add_u32 v237, s47, v240\n"
  "v_and_b32 v248, 7, v249\n"
  "v_lshlrev_b32 v248, 6, v248\n"
  "s_lshl_b32 s45, %[wid], 9\n"
  "v_add_u32 v248, s45, v248\n"
  "s_and_b32 s45, %[bid], 7\n"
  "s_lshl_b32 s45, s45, 11\n"
  "s_add_u32 s38, s45, 0x1000000\n"
  "s_mov_b32 s39, 0\n"
  "s_add_u32 s38, s38, %[ws_lo]\n"
  "s_addc_u32 s39, s39, %[ws_hi]\n"
  "s_lshl_b32 s45, s74, 5\n"
  "s_add_u32 s45, s45, s71\n"
  "s_lshl_b32 s45, s45, 6\n"
  "s_add_u32 s68, s45, 0x1000000\n"
  "s_mov_b32 s69, 0\n"
  "s_add_u32 s68, s68, %[ws_lo]\n"
  "s_addc_u32 s69, s69, %[ws_hi]\n"
  "v_mov_b32 v238, 0\n"
  "v_mov_b32 v240, %[tag_lo]\n"
  "v_mov_b32 v241, %[tag_hi]\n"
  "s_waitcnt vmcnt(16)\n"
  "ds_read_b64_tr_b16 a[64:65], v234 offset:0\n"
  "ds_read_b64_tr_b16 a[66:67], v234 offset:2048\n"
  "ds_read_b64_tr_b16 a[68:69], v235 offset:0\n"
  "ds_read_b64_tr_b16 a[70:71], v235 offset:2048\n"
  "ds_read_b64_tr_b16 a[72:73], v236 offset:0\n"
  "ds_read_b64_tr_b16 a[74:75], v236 offset:2048\n"
  "ds_read_b64_tr_b16 a[76:77], v237 offset:0\n"
  "ds_read_b64_tr_b16 a[78:79], v237 offset:2048\n"
  "s_waitcnt lgkmcnt(7)\n"
  "ds_read_b64_tr_b16 a[80:81], v234 offset:4096\n"
  "ds_read_b64_tr_b16 a[82:83], v234 offset:6144\n"
  "ds_read_b64_tr_b16 a[84:85], v235 offset:4096\n"
  "ds_read_b64_tr_b16 a[86:87], v235 offset:6144\n"
  "ds_read_b64_tr_b16 a[88:89], v236 offset:4096\n"
  "ds_read_b64_tr_b16 a[90:91], v236 offset:6144\n"
  "ds_read_b64_tr_b16 a[92:93], v237 offset:4096\n"
  "ds_read_b64_tr_b16 a[94:95], v237 offset:6144\n"
  "s_waitcnt lgkmcnt(7)\n"
  "ds_read_b64_tr_b16 a[96:97], v226 offset:0\n"
  "ds_read_b64_tr_b16 a[98:99], v226 offset:2048\n"
  "ds_read_b64_tr_b16 a[100:101], v227 offset:0\n"
  "ds_read_b64_tr_b16 a[102:103], v227 offset:2048\n"
  "ds_read_b64_tr_b16 a[104:105], v228 offset:0\n"
  "ds_read_b64_tr_b16 a[106:107], v228 offset:2048\n"
  "ds_read_b64_tr_b16 a[108:109], v229 offset:0\n"
  "ds_read_b64_tr_b16 a[110:111], v229 offset:2048\n"
  "s_waitcnt lgkmcnt(7)\n"
  "ds_read_b64_tr_b16 a[112:113], v226 offset:16384\n"
  "ds_read_b64_tr_b16 a[114:115], v226 offset:18432\n"
  "ds_read_b64_tr_b16 a[116:117], v227 offset:16384\n"
  "ds_read_b64_tr_b16 a[118:119], v227 offset:18432\n"
  "ds_read_b64_tr_b16 a[120:121], v228 offset:16384\n"
  "ds_read_b64_tr_b16 a[122:123], v228 offset:18432\n"
  "ds_read_b64_tr_b16 a[124:125], v229 offset:16384\n"
  "ds_read_b64_tr_b16 a[126:127], v229 offset:18432\n"
  "s_waitcnt lgkmcnt(7)\n"
  "ds_read_b64_tr_b16 a[128:129], v230 offset:0\n"
  "ds_read_b64_tr_b16 a[130:131], v230 offset:2048\n"
  "ds_read_b64_tr_b16 a[132:133], v231 offset:0\n"
  "ds_read_b64_tr_b16 a[134:135], v231 offset:2048\n"
  "ds_read_b64_tr_b16 a[136:137], v232 offset:0\n"
  "ds_read_b64_tr_b16 a[138:139], v232 offset:2048\n"
  "ds_read_b64_tr_b16 a[140:141], v233 offset:0\n"
  "ds_read_b64_tr_b16 a[142:143], v233 offset:2048\n"
  "s_waitcnt lgkmcnt(7)\n"
  "ds_read_b64_tr_b16 a[144:145], v226 offset:32768\n"
  "ds_read_b64_tr_b16 a[146:147], v226 offset:34816\n"
  "ds_read_b64_tr_b16 a[148:149], v227 offset:32768\n"
  "ds_read_b64_tr_b16 a[150:151], v227 offset:34816\n"
  "ds_read_b64_tr_b16 a[152:153], v228 offset:32768\n"
  "ds_read_b64_tr_b16 a[154:155], v228 offset:34816\n"
  "ds_read_b64_tr_b16 a[156:157], v229 offset:32768\n"
  "ds_read_b64_tr_b16 a[158:159], v229 offset:34816\n"
  "s_waitcnt lgkmcnt(7)\n"
  "ds_read_b64_tr_b16 a[160:161], v230 offset:16384\n"
  "ds_read_b64_tr_b16 a[162:163], v230 offset:18432\n"
  "ds_read_b64_tr_b16 a[164:165], v231 offset:16384\n"
  "ds_read_b64_tr_b16 a[166:167], v231 offset:18432\n"
  "ds_read_b64_tr_b16 a[168:169], v232 offset:16384\n"
  "ds_read_b64_tr_b16 a[170:171], v232 offset:18432\n"
  "ds_read_b64_tr_b16 a[172:173], v233 offset:16384\n"
  "ds_read_b64_tr_b16 a[174:175], v233 offset:18432\n"
  "s_waitcnt lgkmcnt(7)\n"
  "ds_read_b64_tr_b16 a[176:177], v226 offset:49152\n"
  "ds_read_b64_tr_b16 a[178:179], v226 offset:51200\n"
  "ds_read_b64_tr_b16 a[180:181], v227 offset:49152\n"
  "ds_read_b64_tr_b16 a[182:183], v227 offset:51200\n"
  "ds_read_b64_tr_b16 a[184:185], v228 offset:49152\n"
  "ds_read_b64_tr_b16 a[186:187], v228 offset:51200\n"
  "ds_read_b64_tr_b16 a[188:189], v229 offset:49152\n"
  "ds_read_b64_tr_b16 a[190:191], v229 offset:51200\n"
  "s_waitcnt lgkmcnt(7)\n"
  "ds_read_b64_tr_b16 a[192:193], v230 offset:32768\n"
  "ds_read_b64_tr_b16 a[194:195], v230 offset:34816\n"
  "ds_read_b64_tr_b16 a[196:197], v231 offset:32768\n"
  "ds_read_b64_tr_b16 a[198:199], v231 offset:34816\n"
  "ds_read_b64_tr_b16 a[200:201], v232 offset:32768\n"
  "ds_read_b64_tr_b16 a[202:203], v232 offset:34816\n"
  "ds_read_b64_tr_b16 a[204:205], v233 offset:32768\n"
  "ds_read_b64_tr_b16 a[206:207], v233 offset:34816\n"
  "s_waitcnt lgkmcnt(7)\n"
  "s_waitcnt vmcnt(0)\n"
  "s_barrier\n"
  "s_cmp_lg_u32 %[wid], 0\n"
  "s_cbranch_scc1 L_nopub%=\n"
  "s_mov_b64 exec, 1\n"
  "global_store_dwordx2 v238, v[240:241], s[68:69] sc1\n"
  "s_mov_b64 exec, -1\n"
  "L_nopub%=:\n"
  "s_add_u32 m0, s42, 114688\n"
  "s_add_u32 s46, s54, 32768\n"
  "s_nop 0\n"
  "buffer_load_dwordx4 v244, s[76:79], s46 offen offset:0 lds\n"
  "buffer_load_dwordx4 v245, s[76:79], s46 offen offset:1024 lds\n"
  "buffer_load_dwordx4 v246, s[76:79], s46 offen offset:2048 lds\n"
  "buffer_load_dwordx4 v247, s[76:79], s46 offen offset:3072 lds\n"
  "s_waitcnt lgkmcnt(0)\n"
  "s_add_u32 m0, s47, 0\n"
  "s_add_u32 s46, s54, 65536\n"
  "s_nop 0\n"
  "buffer_load_dwordx4 v244, s[76:79], s46 offen offset:0 lds\n"
  "buffer_load_dwordx4 v245, s[76:79], s46 offen offset:1024 lds\n"
  "buffer_load_dwordx4 v246, s[76:79], s46 offen offset:2048 lds\n"
  "buffer_load_dwordx4 v247, s[76:79], s46 offen offset:3072 lds\n"
  "s_add_u32 m0, s47, 4096\n"
  "s_add_u32 s46, s54, 98304\n"
  "s_nop 0\n"
  "buffer_load_dwordx4 v244, s[76:79], s46 offen offset:0 lds\n"
  "buffer_load_dwordx4 v245, s[76:79], s46 offen offset:1024 lds\n"
  "buffer_load_dwordx4 v246, s[76:79], s46 offen offset:2048 lds\n"
  "buffer_load_dwordx4 v247, s[76:79], s46 offen offset:3072 lds\n"
  "s_add_u32 m0, s42, 0\n"
  "s_add_u32 s46, s55, 0\n"
  "s_nop 0\n"
  "buffer_load_dwordx4 v244, s[76:79], s46 offen offset:0 lds\n"
  "buffer_load_dwordx4 v245, s[76:79], s46 offen offset:1024 lds\n"
  "buffer_load_dwordx4 v246, s[76:79], s46 offen offset:2048 lds\n"
  "buffer_load_dwordx4 v247, s[76:79], s46 offen offset:3072 lds\n"
  "s_add_u32 m0, s42, 16384\n"
  "s_add_u32 s46, s55, 32768\n"
  "s_nop 0\n"
  "buffer_load_dwordx4 v244, s[76:79], s46 offen offset:0 lds\n"
  "buffer_load_dwordx4 v245, s[76:79], s46 offen offset:1024 lds\n"
  "buffer_load_dwordx4 v246, s[76:79], s46 offen offset:2048 lds\n"
  "buffer_load_dwordx4 v247, s[76:79], s46 offen offset:3072 lds\n"
  "s_add_u32 m0, s42, 65536\n"
  "s_add_u32 s46, s55, 65536\n"
  "s_nop 0\n"
  "buffer_load_dwordx4 v244, s[76:79], s46 offen offset:0 lds\n"
  "buffer_load_dwordx4 v245, s[76:79], s46 offen offset:1024 lds\n"
  "buffer_load_dwordx4 v246, s[76:79], s46 offen offset:2048 lds\n"
  "buffer_load_dwordx4 v247, s[76:79], s46 offen offset:3072 lds\n"
  "s_add_u32 m0, s42, 32768\n"
  "s_add_u32 s46, s55, 98304\n"
  "s_nop 0\n"
  "buffer_load_dwordx4 v244, s[76:79], s46 offen offset:0 lds\n"
  "buffer_load_dwordx4 v245, s[76:79], s46 offen offset:1024 lds\n"
  "buffer_load_dwordx4 v246, s[76:79], s46 offen offset:2048 lds\n"
  "buffer_load_dwordx4 v247, s[76:79], s46 offen offset:3072 lds\n"
  "s_add_u32 m0, s42, 81920\n"
  "s_add_u32 s46, s56, 0\n"
  "s_nop 0\n"
  "buffer_load_dwordx4 v244, s[76:79], s46 offen offset:0 lds\n"
  "buffer_load_dwordx4 v245, s[76:79], s46 offen offset:1024 lds\n"
  "buffer_load_dwordx4 v246, s[76:79], s46 offen offset:2048 lds\n"
  "buffer_load_dwordx4 v247, s[76:79], s46 offen offset:3072 lds\n"
  "s_add_u32 m0, s42, 49152\n"
  "s_add_u32 s46, s56, 32768\n"
  "s_nop 0\n"
  "buffer_load_dwordx4 v244, s[76:79], s46 offen offset:0 lds\n"
  "buffer_load_dwordx4 v245, s[76:79], s46 offen offset:1024 lds\n"
  "buffer_load_dwordx4 v246, s[76:79], s46 offen offset:2048 lds\n"
  "buffer_load_dwordx4 v247, s[76:79], s46 offen offset:3072 lds\n"
  "s_add_u32 m0, s42, 98304\n"
  "s_add_u32 s46, s56, 65536\n"
  "s_nop 0\n"
  "buffer_load_dwordx4 v244, s[76:79], s46 offen offset:0 lds\n"
  "buffer_load_dwordx4 v245, s[76:79], s46 offen offset:1024 lds\n"
  "buffer_load_dwordx4 v246, s[76:79], s46 offen offset:2048 lds\n"
  "buffer_load_dwordx4 v247, s[76:79], s46 offen offset:3072 lds\n"
  "s_waitcnt vmcnt(36)\n"
  "ds_read_b64_tr_b16 a[208:209], v230 offset:49152\n"
  "ds_read_b64_tr_b16 a[210:211], v230 offset:51200\n"
  "ds_read_b64_tr_b16 a[212:213], v231 offset:49152\n"
  "ds_read_b64_tr_b16 a[214:215], v231 offset:51200\n"
  "ds_read_b64_tr_b16 a[216:217], v232 offset:49152\n"
  "ds_read_b64_tr_b16 a[218:219], v232 offset:51200\n"
  "ds_read_b64_tr_b16 a[220:221], v233 offset:49152\n"
  "ds_read_b64_tr_b16 a[222:223], v233 offset:51200\n"
  "s_waitcnt lgkmcnt(0)\n"
  "s_add_u32 m0, s42, 114688\n"
  "s_add_u32 s46, s56, 98304\n"
  "s_nop 0\n"
  "buffer_load_dwordx4 v244, s[76:79], s46 offen offset:0 lds\n"
  "buffer_load_dwordx4 v245, s[76:79], s46 offen offset:1024 lds\n"
  "buffer_load_dwordx4 v246, s[76:79], s46 offen offset:2048 lds\n"
  "buffer_load_dwordx4 v247, s[76:79], s46 offen offset:3072 lds\n"
  "s_waitcnt vmcnt(36)\n"
  "ds_read_b64_tr_b16 a[224:225], v234 offset:0\n"
  "ds_read_b64_tr_b16 a[226:227], v234 offset:2048\n"
  "ds_read_b64_tr_b16 a[228:229], v235 offset:0\n"
  "ds_read_b64_tr_b16 a[230:231], v235 offset:2048\n"
  "ds_read_b64_tr_b16 a[232:233], v236 offset:0\n"
  "ds_read_b64_tr_b16 a[234:235], v236 offset:2048\n"
  "ds_read_b64_tr_b16 a[236:237], v237 offset:0\n"
  "ds_read_b64_tr_b16 a[238:239], v237 offset:2048\n"
  "s_waitcnt lgkmcnt(0)\n"
  "s_add_u32 m0, s47, 0\n"
  "s_add_u32 s46, s57, 0\n"
  "s_nop 0\n"
  "buffer_load_dwordx4 v244, s[76:79], s46 offen offset:0 lds\n"
  "buffer_load_dwordx4 v245, s[76:79], s46 offen offset:1024 lds\n"
  "buffer_load_dwordx4 v246, s[76:79], s46 offen offset:2048 lds\n"
  "buffer_load_dwordx4 v247, s[76:79], s46 offen offset:3072 lds\n"
  "s_waitcnt vmcnt(36)\n"
  "ds_read_b64_tr_b16 a[240:241], v234 offset:4096\n"
  "ds_read_b64_tr_b16 a[242:243], v234 offset:6144\n"
  "ds_read_b64_tr_b16 a[244:245], v235 offset:4096\n"
  "ds_read_b64_tr_b16 a[246:247], v235 offset:6144\n"
  "ds_read_b64_tr_b16 a[248:249], v236 offset:4096\n"
  "ds_read_b64_tr_b16 a[250:251], v236 offset:6144\n"
  "ds_read_b64_tr_b16 a[252:253], v237 offset:4096\n"
  "ds_read_b64_tr_b16 a[254:255], v237 offset:6144\n"
  "s_waitcnt lgkmcnt(0)\n"
  "s_add_u32 m0, s47, 4096\n"
  "s_add_u32 s46, s57, 32768\n"
  "s_nop 0\n"
  "buffer_load_dwordx4 v244, s[76:79], s46 offen offset:0 lds\n"
  "buffer_load_dwordx4 v245, s[76:79], s46 offen offset:1024 lds\n"
  "buffer_load_dwordx4 v246, s[76:79], s46 offen offset:2048 lds\n"
  "buffer_load_dwordx4 v247, s[76:79], s46 offen offset:3072 lds\n"
  "s_mov_b32 s44, 524288\n"
  "L_poll%=:\n"
  "global_load_dwordx2 v[252:253], v248, s[38:39] sc1\n"
  "s_waitcnt vmcnt(0)\n"
  "v_cmp_ne_u32 vcc, %[tag_lo], v252\n"
  "v_cmp_ne_u32 s[68:69], %[tag_hi], v253\n"
  "s_nop 3\n"
  "s_or_b64 vcc, vcc, s[68:69]\n"
  "s_cmp_eq_u64 vcc, 0\n"
  "s_cbranch_scc1 L_ready%=\n"
  "s_sleep 2\n"
  "s_sub_u32 s44, s44, 1\n"
  "s_cmp_lg_u32 s44, 0\n"
  "s_cbranch_scc1 L_poll%=\n"
  "L_ready%=:\n"
  "s_add_u32 s41, s60, 0\n"
  "buffer_load_dwordx4 v[0:3], v224, s[20:23], s41 offen sc1\n"
  "s_add_u32 s41, s60, 4096\n"
  "buffer_load_dwordx4 v[4:7], v224, s[20:23], s41 offen sc1\n"
  "s_add_u32 s41, s60, 8192\n"
  "buffer_load_dwordx4 v[8:11], v224, s[20:23], s41 offen sc1\n"
  "s_add_u32 s41, s60, 12288\n"
  "buffer_load_dwordx4 v[12:15], v224, s[20:23], s41 offen sc1\n"
  "s_add_u32 s41, s60, 1024\n"
  "buffer_load_dwordx4 v[16:19], v224, s[20:23], s41 offen sc1\n"
  "s_add_u32 s41, s60, 5120\n"
  "buffer_load_dwordx4 v[20:23], v224, s[20:23], s41 offen sc1\n"
  "s_add_u32 s41, s60, 9216\n"
  "buffer_load_dwordx4 v[24:27], v224, s[20:23], s41 offen sc1\n"
  "s_add_u32 s41, s60, 13312\n"
  "buffer_load_dwordx4 v[28:31], v224, s[20:23], s41 offen sc1\n"
  "s_add_u32 s41, s60, 2048\n"
  "buffer_load_dwordx4 v[32:35], v224, s[20:23], s41 offen sc1\n"
  "s_add_u32 s41, s60, 6144\n"
  "buffer_load_dwordx4 v[36:39], v224, s[20:23], s41 offen sc1\n"
  "s_add_u32 s41, s60, 10240\n"
  "buffer_load_dwordx4 v[40:43], v224, s[20:23], s41 offen sc1\n"
  "s_add_u32 s41, s60, 14336\n"
  "buffer_load_dwordx4 v[44:47], v224, s[20:23], s41 offen sc1\n"
  "s_add_u32 s41, s60, 3072\n"
  "buffer_load_dwordx4 v[48:51], v224, s[20:23], s41 offen sc1\n"
  "s_add_u32 s41, s60, 7168\n"
  "buffer_load_dwordx4 v[52:55], v224, s[20:23], s41 offen sc1\n"
  "s_add_u32 s41, s60, 11264\n"
  "buffer_load_dwordx4 v[56:59], v224, s[20:23], s41 offen sc1\n"
  "s_add_u32 s41, s60, 15360\n"
  "buffer_load_dwordx4 v[60:63], v224, s[20:23], s41 offen sc1\n"
  "s_add_u32 s41, s61, 0\n"
  "buffer_load_dwordx4 v[64:67], v224, s[20:23], s41 offen sc1\n"
  "s_add_u32 s41, s61, 4096\n"
  "buffer_load_dwordx4 v[68:71], v224, s[20:23], s41 offen sc1\n"
  "s_add_u32 s41, s61, 8192\n"
  "buffer_load_dwordx4 v[72:75], v224, s[20:23], s41 offen sc1\n"
  "s_add_u32 s41, s61, 12288\n"
  "buffer_load_dwordx4 v[76:79], v224, s[20:23], s41 offen sc1\n"
  "s_add_u32 s41, s61, 1024\n"
  "buffer_load_dwordx4 v[80:83], v224, s[20:23], s41 offen sc1\n"
  "s_add_u32 s41, s61, 5120\n"
  "buffer_load_dwordx4 v[84:87], v224, s[20:23], s41 offen sc1\n"
  "s_add_u32 s41, s61, 9216\n"
  "buffer_load_dwordx4 v[88:91], v224, s[20:23], s41 offen sc1\n"
  "s_add_u32 s41, s61, 13312\n"
  "buffer_load_dwordx4 v[92:95], v224, s[20:23], s41 offen sc1\n"
  "s_add_u32 s41, s61, 2048\n"
  "buffer_load_dwordx4 v[96:99], v224, s[20:23], s41 offen sc1\n"
  "s_add_u32 s41, s61, 6144\n"
  "buffer_load_dwordx4 v[100:103], v224, s[20:23], s41 offen sc1\n"
  "s_add_u32 s41, s61, 10240\n"
  "buffer_load_dwordx4 v[104:107], v224, s[20:23], s41 offen sc1\n"
  "s_add_u32 s41, s61, 14336\n"
  "buffer_load_dwordx4 v[108:111], v224, s[20:23], s41 offen sc1\n"
  "s_add_u32 s41, s61, 3072\n"
  "buffer_load_dwordx4 v[112:115], v224, s[20:23], s41 offen sc1\n"
  "s_add_u32 s41, s61, 7168\n"
  "buffer_load_dwordx4 v[116:119], v224, s[20:23], s41 offen sc1\n"
  "s_add_u32 s41, s61, 11264\n"
  "buffer_load_dwordx4 v[120:123], v224, s[20:23], s41 offen sc1\n"
  "s_add_u32 s41, s61, 15360\n"
  "buffer_load_dwordx4 v[124:127], v224, s[20:23], s41 offen sc1\n"
  "s_add_u32 s41, s62, 0\n"
  "buffer_load_dwordx4 v[128:131], v224, s[20:23], s41 offen sc1\n"
  "s_add_u32 s41, s62, 4096\n"
  "buffer_load_dwordx4 v[132:135], v224, s[20:23], s41 offen sc1\n"
  "s_add_u32 s41, s62, 8192\n"
  "buffer_load_dwordx4 v[136:139], v224, s[20:23], s41 offen sc1\n"
  "s_add_u32 s41, s62, 12288\n"
  "buffer_load_dwordx4 v[140:143], v224, s[20:23], s41 offen sc1\n"
  "s_add_u32 s41, s62, 1024\n"
  "buffer_load_dwordx4 v[144:147], v224, s[20:23], s41 offen sc1\n"
  "s_add_u32 s41, s62, 5120\n"
  "buffer_load_dwordx4 v[148:151], v224, s[20:23], s41 offen sc1\n"
  "s_add_u32 s41, s62, 9216\n"
  "buffer_load_dwordx4 v[152:155], v224, s[20:23], s41 offen sc1\n"
  "s_add_u32 s41, s62, 13312\n"
  "buffer_load_dwordx4 v[156:159], v224, s[20:23], s41 offen sc1\n"
  "s_add_u32 s41, s62, 2048\n"
  "buffer_load_dwordx4 v[160:163], v224, s[20:23], s41 offen sc1\n"
  "s_add_u32 s41, s62, 6144\n"
  "buffer_load_dwordx4 v[164:167], v224, s[20:23], s41 offen sc1\n"
  "s_add_u32 s41, s62, 10240\n"
  "buffer_load_dwordx4 v[168:171], v224, s[20:23], s41 offen sc1\n"
  "s_add_u32 s41, s62, 14336\n"
  "buffer_load_dwordx4 v[172:175], v224, s[20:23], s41 offen sc1\n"
  "s_add_u32 s41, s62, 3072\n"
  "buffer_load_dwordx4 v[176:179], v224, s[20:23], s41 offen sc1\n"
  "s_add_u32 s41, s62, 7168\n"
  "buffer_load_dwordx4 v[180:183], v224, s[20:23], s41 offen sc1\n"
  "s_add_u32 s41, s62, 11264\n"
  "buffer_load_dwordx4 v[184:187], v224, s[20:23], s41 offen sc1\n"
  "s_add_u32 s41, s62, 15360\n"
  "buffer_load_dwordx4 v[188:191], v224, s[20:23], s41 offen sc1\n"
  "s_waitcnt vmcnt(40)\n"
  "v_mfma_f32_16x16x32_bf16 a[0:3], a[64:67], v[0:3], 0\n"
  "v_mfma_f32_16x16x32_bf16 a[0:3], a[80:83], v[16:19], a[0:3]\n"
  "s_add_u32 s41, s63, 0\n"
  "v_mfma_f32_16x16x32_bf16 a[4:7], a[64:67], v[4:7], 0\n"
  "buffer_load_dwordx4 v[192:195], v224, s[20:23], s41 offen sc1\n"
  "v_mfma_f32_16x16x32_bf16 a[4:7], a[80:83], v[20:23], a[4:7]\n"
  "s_add_u32 s41, s63, 4096\n"
  "v_mfma_f32_16x16x32_bf16 a[8:11], a[64:67], v[8:11], 0\n"
  "buffer_load_dwordx4 v[196:199], v224, s[20:23], s41 offen sc1\n"
  "v_mfma_f32_16x16x32_bf16 a[8:11], a[80:83], v[24:27], a[8:11]\n"
  "s_add_u32 s41, s63, 8192\n"
  "v_mfma_f32_16x16x32_bf16 a[12:15], a[64:67], v[12:15], 0\n"
  "buffer_load_dwordx4 v[200:203], v224, s[20:23], s41 offen sc1\n"
  "v_mfma_f32_16x16x32_bf16 a[12:15], a[80:83], v[28:31], a[12:15]\n"
  "s_add_u32 s41, s63, 12288\n"
  "v_mfma_f32_16x16x32_bf16 a[16:19], a[68:71], v[0:3], 0\n"
  "buffer_load_dwordx4 v[204:207], v224, s[20:23], s41 offen sc1\n"
  "v_mfma_f32_16x16x32_bf16 a[16:19], a[84:87], v[16:19], a[16:19]\n"
  "s_add_u32 s41, s63, 1024\n"
  "v_mfma_f32_16x16x32_bf16 a[20:23], a[68:71], v[4:7], 0\n"
  "buffer_load_dwordx4 v[208:211], v224, s[20:23], s41 offen sc1\n"
  "v_mfma_f32_16x16x32_bf16 a[20:23], a[84:87], v[20:23], a[20:23]\n"
  "s_add_u32 s41, s63, 5120\n"
  "v_mfma_f32_16x16x32_bf16 a[24:27], a[68:71], v[8:11], 0\n"
  "buffer_load_dwordx4 v[212:215], v224, s[20:23], s41 offen sc1\n"
  "v_mfma_f32_16x16x32_bf16 a[24:27], a[84:87], v[24:27], a[24:27]\n"
  "s_add_u32 s41, s63, 9216\n"
  "v_mfma_f32_16x16x32_bf16 a[28:31], a[68:71], v[12:15], 0\n"
  "buffer_load_dwordx4 v[216:219], v224, s[20:23], s41 offen sc1\n"
  "v_mfma_f32_16x16x32_bf16 a[28:31], a[84:87], v[28:31], a[28:31]\n"
  "s_add_u32 s41, s63, 13312\n"
  "v_mfma_f32_16x16x32_bf16 a[32:35], a[72:75], v[0:3], 0\n"
  "buffer_load_dwordx4 v[220:223], v224, s[20:23], s41 offen sc1\n"
  "v_mfma_f32_16x16x32_bf16 a[32:35], a[88:91], v[16:19], a[32:35]\n"
  "v_mfma_f32_16x16x32_bf16 a[36:39], a[72:75], v[4:7], 0\n"
  "v_mfma_f32_16x16x32_bf16 a[36:39], a[88:91], v[20:23], a[36:39]\n"
  "v_mfma_f32_16x16x32_bf16 a[40:43], a[72:75], v[8:11], 0\n"
  "v_mfma_f32_16x16x32_bf16 a[40:43], a[88:91], v[24:27], a[40:43]\n"
  "v_mfma_f32_16x16x32_bf16 a[44:47], a[72:75], v[12:15], 0\n"
  "v_mfma_f32_16x16x32_bf16 a[44:47], a[88:91], v[28:31], a[44:47]\n"
  "v_mfma_f32_16x16x32_bf16 a[48:51], a[76:79], v[0:3], 0\n"
  "v_mfma_f32_16x16x32_bf16 a[48:51], a[92:95], v[16:19], a[48:51]\n"
  "v_mfma_f32_16x16x32_bf16 a[52:55], a[76:79], v[4:7], 0\n"
  "v_mfma_f32_16x16x32_bf16 a[52:55], a[92:95], v[20:23], a[52:55]\n"
  "v_mfma_f32_16x16x32_bf16 a[56:59], a[76:79], v[8:11], 0\n"
  "v_mfma_f32_16x16x32_bf16 a[56:59], a[92:95], v[24:27], a[56:59]\n"
  "v_mfma_f32_16x16x32_bf16 a[60:63], a[76:79], v[12:15], 0\n"
  "s_waitcnt vmcnt(40)\n"
  "v_mfma_f32_16x16x32_bf16 a[60:63], a[92:95], v[28:31], a[60:63]\n"
  "v_mfma_f32_16x16x32_bf16 a[0:3], a[96:99], v[32:35], a[0:3]\n"
  "v_mfma_f32_16x16x32_bf16 a[0:3], a[112:115], v[48:51], a[0:3]\n"
  "s_add_u32 s41, s63, 2048\n"
  "v_mfma_f32_16x16x32_bf16 a[4:7], a[96:99], v[36:39], a[4:7]\n"
  "buffer_load_dwordx4 v[0:3], v224, s[20:23], s41 offen sc1\n"
  "v_mfma_f32_16x16x32_bf16 a[4:7], a[112:115], v[52:55], a[4:7]\n"
  "s_add_u32 s41, s63, 6144\n"
  "v_mfma_f32_16x16x32_bf16 a[8:11], a[96:99], v[40:43], a[8:11]\n"
  "buffer_load_dwordx4 v[4:7], v224, s[20:23], s41 offen sc1\n"
  "v_mfma_f32_16x16x32_bf16 a[8:11], a[112:115], v[56:59], a[8:11]\n"
  "s_add_u32 s41, s63, 10240\n"
  "v_mfma_f32_16x16x32_bf16 a[12:15], a[96:99], v[44:47], a[12:15]\n"
  "buffer_load_dwordx4 v[8:11], v224, s[20:23], s41 offen sc1\n"
  "v_mfma_f32_16x16x32_bf16 a[12:15], a[112:115], v[60:63], a[12:15]\n"
  "s_add_u32 s41, s63, 14336\n"
  "v_mfma_f32_16x16x32_bf16 a[16:19], a[100:103], v[32:35], a[16:19]\n"
  "buffer_load_dwordx4 v[12:15], v224, s[20:23], s41 offen sc1\n"
  "v_mfma_f32_16x16x32_bf16 a[16:19], a[116:119], v[48:51], a[16:19]\n"
  "s_add_u32 s41, s63, 3072\n"
  "v_mfma_f32_16x16x32_bf16 a[20:23], a[100:103], v[36:39], a[20:23]\n"
  "buffer_load_dwordx4 v[16:19], v224, s[20:23], s41 offen sc1\n"
  "v_mfma_f32_16x16x32_bf16 a[20:23], a[116:119], v[52:55], a[20:23]\n"
  "s_add_u32 s41, s63, 7168\n"
  "v_mfma_f32_16x16x32_bf16 a[24:27], a[100:103], v[40:43], a[24:27]\n"
  "buffer_load_dwordx4 v[20:23], v224, s[20:23], s41 offen sc1\n"
  "v_mfma_f32_16x16x32_bf16 a[24:27], a[116:119], v[56:59], a[24:27]\n"
  "s_add_u32 s41, s63, 11264\n"
  "v_mfma_f32_16x16x32_bf16 a[28:31], a[100:103], v[44:47], a[28:31]\n"
  "buffer_load_dwordx4 v[24:27], v224, s[20:23], s41 offen sc1\n"
  "v_mfma_f32_16x16x32_bf16 a[28:31], a[116:119], v[60:63], a[28:31]\n"
  "s_add_u32 s41, s63, 15360\n"
  "v_mfma_f32_16x16x32_bf16 a[32:35], a[104:107], v[32:35], a[32:35]\n"
  "buffer_load_dwordx4 v[28:31], v224, s[20:23], s41 offen sc1\n"
  "v_mfma_f32_16x16x32_bf16 a[32:35], a[120:123], v[48:51], a[32:35]\n"
  "v_mfma_f32_16x16x32_bf16 a[36:39], a[104:107], v[36:39], a[36:39]\n"
  "v_mfma_f32_16x16x32_bf16 a[36:39], a[120:123], v[52:55], a[36:39]\n"
  "v_mfma_f32_16x16x32_bf16 a[40:43], a[104:107], v[40:43], a[40:43]\n"
  "v_mfma_f32_16x16x32_bf16 a[40:43], a[120:123], v[56:59], a[40:43]\n"
  "v_mfma_f32_16x16x32_bf16 a[44:47], a[104:107], v[44:47], a[44:47]\n"
  "v_mfma_f32_16x16x32_bf16 a[44:47], a[120:123], v[60:63], a[44:47]\n"
  "v_mfma_f32_16x16x32_bf16 a[48:51], a[108:111], v[32:35], a[48:51]\n"
  "v_mfma_f32_16x16x32_bf16 a[48:51], a[124:127], v[48:51], a[48:51]\n"
  "v_mfma_f32_16x16x32_bf16 a[52:55], a[108:111], v[36:39], a[52:55]\n"
  "v_mfma_f32_16x16x32_bf16 a[52:55], a[124:127], v[52:55], a[52:55]\n"
  "v_mfma_f32_16x16x32_bf16 a[56:59], a[108:111], v[40:43], a[56:59]\n"
  "v_mfma_f32_16x16x32_bf16 a[56:59], a[124:127], v[56:59], a[56:59]\n"
  "v_mfma_f32_16x16x32_bf16 a[60:63], a[108:111], v[44:47], a[60:63]\n"
  "s_waitcnt vmcnt(40)\n"
  "v_mfma_f32_16x16x32_bf16 a[60:63], a[124:127], v[60:63], a[60:63]\n"
  "v_mfma_f32_16x16x32_bf16 a[0:3], a[128:131], v[64:67], a[0:3]\n"
  "v_mfma_f32_16x16x32_bf16 a[0:3], a[144:147], v[80:83], a[0:3]\n"
  "s_add_u32 s41, s64, 0\n"
  "v_mfma_f32_16x16x32_bf16 a[4:7], a[128:131], v[68:71], a[4:7]\n"
  "buffer_load_dwordx4 v[32:35], v224, s[20:23], s41 offen sc1\n"
  "v_mfma_f32_16x16x32_bf16 a[4:7], a[144:147], v[84:87], a[4:7]\n"
  "s_add_u32 s41, s64, 4096\n"
  "v_mfma_f32_16x16x32_bf16 a[8:11], a[128:131], v[72:75], a[8:11]\n"
  "buffer_load_dwordx4 v[36:39], v224, s[20:23], s41 offen sc1\n"
  "v_mfma_f32_16x16x32_bf16 a[8:11], a[144:147], v[88:91], a[8:11]\n"
  "s_add_u32 s41, s64, 8192\n"
  "v_mfma_f32_16x16x32_bf16 a[12:15], a[128:131], v[76:79], a[12:15]\n"
  "buffer_load_dwordx4 v[40:43], v224, s[20:23], s41 offen sc1\n"
  "v_mfma_f32_16x16x32_bf16 a[12:15], a[144:147], v[92:95], a[12:15]\n"
  "s_add_u32 s41, s64, 12288\n"
  "v_mfma_f32_16x16x32_bf16 a[16:19], a[132:135], v[64:67], a[16:19]\n"
  "buffer_load_dwordx4 v[44:47], v224, s[20:23], s41 offen sc1\n"
  "v_mfma_f32_16x16x32_bf16 a[16:19], a[148:151], v[80:83], a[16:19]\n"
  "s_add_u32 s41, s64, 1024\n"
  "v_mfma_f32_16x16x32_bf16 a[20:23], a[132:135], v[68:71], a[20:23]\n"
  "buffer_load_dwordx4 v[48:51], v224, s[20:23], s41 offen sc1\n"
  "v_mfma_f32_16x16x32_bf16 a[20:23], a[148:151], v[84:87], a[20:23]\n"
  "s_add_u32 s41, s64, 5120\n"
  "v_mfma_f32_16x16x32_bf16 a[24:27], a[132:135], v[72:75], a[24:27]\n"
  "buffer_load_dwordx4 v[52:55], v224, s[20:23], s41 offen sc1\n"
  "v_mfma_f32_16x16x32_bf16 a[24:27], a[148:151], v[88:91], a[24:27]\n"
  "s_add_u32 s41, s64, 9216\n"
  "v_mfma_f32_16x16x32_bf16 a[28:31], a[132:135], v[76:79], a[28:31]\n"
  "buffer_load_dwordx4 v[56:59], v224, s[20:23], s41 offen sc1\n"
  "v_mfma_f32_16x16x32_bf16 a[28:31], a[148:151], v[92:95], a[28:31]\n"
  "s_add_u32 s41, s64, 13312\n"
  "v_mfma_f32_16x16x32_bf16 a[32:35], a[136:139], v[64:67], a[32:35]\n"
  "buffer_load_dwordx4 v[60:63], v224, s[20:23], s41 offen sc1\n"
  "v_mfma_f32_16x16x32_bf16 a[32:35], a[152:155], v[80:83], a[32:35]\n"
  "v_mfma_f32_16x16x32_bf16 a[36:39], a[136:139], v[68:71], a[36:39]\n"
  "v_mfma_f32_16x16x32_bf16 a[36:39], a[152:155], v[84:87], a[36:39]\n"
  "v_mfma_f32_16x16x32_bf16 a[40:43], a[136:139], v[72:75], a[40:43]\n"
  "v_mfma_f32_16x16x32_bf16 a[40:43], a[152:155], v[88:91], a[40:43]\n"
  "v_mfma_f32_16x16x32_bf16 a[44:47], a[136:139], v[76:79], a[44:47]\n"
  "v_mfma_f32_16x16x32_bf16 a[44:47], a[152:155], v[92:95], a[44:47]\n"
  "v_mfma_f32_16x16x32_bf16 a[48:51], a[140:143], v[64:67], a[48:51]\n"
  "v_mfma_f32_16x16x32_bf16 a[48:51], a[156:159], v[80:83], a[48:51]\n"
  "v_mfma_f32_16x16x32_bf16 a[52:55], a[140:143], v[68:71], a[52:55]\n"
  "v_mfma_f32_16x16x32_bf16 a[52:55], a[156:159], v[84:87], a[52:55]\n"
  "v_mfma_f32_16x16x32_bf16 a[56:59], a[140:143], v[72:75], a[56:59]\n"
  "v_mfma_f32_16x16x32_bf16 a[56:59], a[156:159], v[88:91], a[56:59]\n"
  "v_mfma_f32_16x16x32_bf16 a[60:63], a[140:143], v[76:79], a[60:63]\n"
  "s_waitcnt vmcnt(40)\n"
  "v_mfma_f32_16x16x32_bf16 a[60:63], a[156:159], v[92:95], a[60:63]\n"
  "v_mfma_f32_16x16x32_bf16 a[0:3], a[160:163], v[96:99], a[0:3]\n"
  "v_mfma_f32_16x16x32_bf16 a[0:3], a[176:179], v[112:115], a[0:3]\n"
  "s_add_u32 s41, s64, 2048\n"
  "v_mfma_f32_16x16x32_bf16 a[4:7], a[160:163], v[100:103], a[4:7]\n"
  "buffer_load_dwordx4 v[64:67], v224, s[20:23], s41 offen sc1\n"
  "v_mfma_f32_16x16x32_bf16 a[4:7], a[176:179], v[116:119], a[4:7]\n"
  "s_add_u32 s41, s64, 6144\n"
  "v_mfma_f32_16x16x32_bf16 a[8:11], a[160:163], v[104:107], a[8:11]\n"
  "buffer_load_dwordx4 v[68:71], v224, s[20:23], s41 offen sc1\n"
  "v_mfma_f32_16x16x32_bf16 a[8:11], a[176:179], v[120:123], a[8:11]\n"
  "s_add_u32 s41, s64, 10240\n"
  "v_mfma_f32_16x16x32_bf16 a[12:15], a[160:163], v[108:111], a[12:15]\n"
  "buffer_load_dwordx4 v[72:75], v224, s[20:23], s41 offen sc1\n"
  "v_mfma_f32_16x16x32_bf16 a[12:15], a[176:179], v[124:127], a[12:15]\n"
  "s_add_u32 s41, s64, 14336\n"
  "v_mfma_f32_16x16x32_bf16 a[16:19], a[164:167], v[96:99], a[16:19]\n"
  "buffer_load_dwordx4 v[76:79], v224, s[20:23], s41 offen sc1\n"
  "v_mfma_f32_16x16x32_bf16 a[16:19], a[180:183], v[112:115], a[16:19]\n"
  "s_add_u32 s41, s64, 3072\n"
  "v_mfma_f32_16x16x32_bf16 a[20:23], a[164:167], v[100:103], a[20:23]\n"
  "buffer_load_dwordx4 v[80:83], v224, s[20:23], s41 offen sc1\n"
  "v_mfma_f32_16x16x32_bf16 a[20:23], a[180:183], v[116:119], a[20:23]\n"
  "s_add_u32 s41, s64, 7168\n"
  "v_mfma_f32_16x16x32_bf16 a[24:27], a[164:167], v[104:107], a[24:27]\n"
  "buffer_load_dwordx4 v[84:87], v224, s[20:23], s41 offen sc1\n"
  "v_mfma_f32_16x16x32_bf16 a[24:27], a[180:183], v[120:123], a[24:27]\n"
  "s_add_u32 s41, s64, 11264\n"
  "v_mfma_f32_16x16x32_bf16 a[28:31], a[164:167], v[108:111], a[28:31]\n"
  "buffer_load_dwordx4 v[88:91], v224, s[20:23], s41 offen sc1\n"
  "v_mfma_f32_16x16x32_bf16 a[28:31], a[180:183], v[124:127], a[28:31]\n"
  "s_add_u32 s41, s64, 15360\n"
  "v_mfma_f32_16x16x32_bf16 a[32:35], a[168:171], v[96:99], a[32:35]\n"
  "buffer_load_dwordx4 v[92:95], v224, s[20:23], s41 offen sc1\n"
  "v_mfma_f32_16x16x32_bf16 a[32:35], a[184:187], v[112:115], a[32:35]\n"
  "v_mfma_f32_16x16x32_bf16 a[36:39], a[168:171], v[100:103], a[36:39]\n"
  "v_mfma_f32_16x16x32_bf16 a[36:39], a[184:187], v[116:119], a[36:39]\n"
  "v_mfma_f32_16x16x32_bf16 a[40:43], a[168:171], v[104:107], a[40:43]\n"
  "v_mfma_f32_16x16x32_bf16 a[40:43], a[184:187], v[120:123], a[40:43]\n"
  "v_mfma_f32_16x16x32_bf16 a[44:47], a[168:171], v[108:111], a[44:47]\n"
  "v_mfma_f32_16x16x32_bf16 a[44:47], a[184:187], v[124:127], a[44:47]\n"
  "v_mfma_f32_16x16x32_bf16 a[48:51], a[172:175], v[96:99], a[48:51]\n"
  "v_mfma_f32_16x16x32_bf16 a[48:51], a[188:191], v[112:115], a[48:51]\n"
  "v_mfma_f32_16x16x32_bf16 a[52:55], a[172:175], v[100:103], a[52:55]\n"
  "v_mfma_f32_16x16x32_bf16 a[52:55], a[188:191], v[116:119], a[52:55]\n"
  "v_mfma_f32_16x16x32_bf16 a[56:59], a[172:175], v[104:107], a[56:59]\n"
  "v_mfma_f32_16x16x32_bf16 a[56:59], a[188:191], v[120:123], a[56:59]\n"
  "v_mfma_f32_16x16x32_bf16 a[60:63], a[172:175], v[108:111], a[60:63]\n"
  "s_waitcnt vmcnt(40)\n"
  "v_mfma_f32_16x16x32_bf16 a[60:63], a[188:191], v[124:127], a[60:63]\n"
  "v_mfma_f32_16x16x32_bf16 a[0:3], a[192:195], v[128:131], a[0:3]\n"
  "v_mfma_f32_16x16x32_bf16 a[0:3], a[208:211], v[144:147], a[0:3]\n"
  "s_add_u32 s41, s65, 0\n"
  "v_mfma_f32_16x16x32_bf16 a[4:7], a[192:195], v[132:135], a[4:7]\n"
  "buffer_load_dwordx4 v[96:99], v224, s[20:23], s41 offen sc1\n"
  "v_mfma_f32_16x16x32_bf16 a[4:7], a[208:211], v[148:151], a[4:7]\n"
  "s_add_u32 s41, s65, 4096\n"
  "v_mfma_f32_16x16x32_bf16 a[8:11], a[192:195], v[136:139], a[8:11]\n"
  "buffer_load_dwordx4 v[100:103], v224, s[20:23], s41 offen sc1\n"
  "v_mfma_f32_16x16x32_bf16 a[8:11], a[208:211], v[152:155], a[8:11]\n"
  "s_add_u32 s41, s65, 8192\n"
  "v_mfma_f32_16x16x32_bf16 a[12:15], a[192:195], v[140:143], a[12:15]\n"
  "buffer_load_dwordx4 v[104:107], v224, s[20:23], s41 offen sc1\n"
  "v_mfma_f32_16x16x32_bf16 a[12:15], a[208:211], v[156:159], a[12:15]\n"
  "s_add_u32 s41, s65, 12288\n"
  "v_mfma_f32_16x16x32_bf16 a[16:19], a[196:199], v[128:131], a[16:19]\n"
  "buffer_load_dwordx4 v[108:111], v224, s[20:23], s41 offen sc1\n"
  "v_mfma_f32_16x16x32_bf16 a[16:19], a[212:215], v[144:147], a[16:19]\n"
  "s_add_u32 s41, s65, 1024\n"
  "v_mfma_f32_16x16x32_bf16 a[20:23], a[196:199], v[132:135], a[20:23]\n"
  "buffer_load_dwordx4 v[112:115], v224, s[20:23], s41 offen sc1\n"
  "v_mfma_f32_16x16x32_bf16 a[20:23], a[212:215], v[148:151], a[20:23]\n"
  "s_add_u32 s41, s65, 5120\n"
  "v_mfma_f32_16x16x32_bf16 a[24:27], a[196:199], v[136:139], a[24:27]\n"
  "buffer_load_dwordx4 v[116:119], v224, s[20:23], s41 offen sc1\n"
  "v_mfma_f32_16x16x32_bf16 a[24:27], a[212:215], v[152:155], a[24:27]\n"
  "s_add_u32 s41, s65, 9216\n"
  "v_mfma_f32_16x16x32_bf16 a[28:31], a[196:199], v[140:143], a[28:31]\n"
  "buffer_load_dwordx4 v[120:123], v224, s[20:23], s41 offen sc1\n"
  "v_mfma_f32_16x16x32_bf16 a[28:31], a[212:215], v[156:159], a[28:31]\n"
  "s_add_u32 s41, s65, 13312\n"
  "v_mfma_f32_16x16x32_bf16 a[32:35], a[200:203], v[128:131], a[32:35]\n"
  "buffer_load_dwordx4 v[124:127], v224, s[20:23], s41 offen sc1\n"
  "v_mfma_f32_16x16x32_bf16 a[32:35], a[216:219], v[144:147], a[32:35]\n"
  "v_mfma_f32_16x16x32_bf16 a[36:39], a[200:203], v[132:135], a[36:39]\n"
  "v_mfma_f32_16x16x32_bf16 a[36:39], a[216:219], v[148:151], a[36:39]\n"
  "v_mfma_f32_16x16x32_bf16 a[40:43], a[200:203], v[136:139], a[40:43]\n"
  "v_mfma_f32_16x16x32_bf16 a[40:43], a[216:219], v[152:155], a[40:43]\n"
  "v_mfma_f32_16x16x32_bf16 a[44:47], a[200:203], v[140:143], a[44:47]\n"
  "v_mfma_f32_16x16x32_bf16 a[44:47], a[216:219], v[156:159], a[44:47]\n"
  "v_mfma_f32_16x16x32_bf16 a[48:51], a[204:207], v[128:131], a[48:51]\n"
  "v_mfma_f32_16x16x32_bf16 a[48:51], a[220:223], v[144:147], a[48:51]\n"
  "v_mfma_f32_16x16x32_bf16 a[52:55], a[204:207], v[132:135], a[52:55]\n"
  "v_mfma_f32_16x16x32_bf16 a[52:55], a[220:223], v[148:151], a[52:55]\n"
  "v_mfma_f32_16x16x32_bf16 a[56:59], a[204:207], v[136:139], a[56:59]\n"
  "v_mfma_f32_16x16x32_bf16 a[56:59], a[220:223], v[152:155], a[56:59]\n"
  "v_mfma_f32_16x16x32_bf16 a[60:63], a[204:207], v[140:143], a[60:63]\n"
  "s_waitcnt vmcnt(40)\n"
  "v_mfma_f32_16x16x32_bf16 a[60:63], a[220:223], v[156:159], a[60:63]\n"
  "v_mfma_f32_16x16x32_bf16 a[0:3], a[224:227], v[160:163], a[0:3]\n"
  "s_waitcnt vmcnt(63)\n"
  "v_mfma_f32_16x16x32_bf16 a[0:3], a[240:243], v[176:179], a[0:3]\n"
  "s_add_u32 s41, s65, 2048\n"
  "ds_read_b64_tr_b16 a[64:65], v226 offset:0\n"
  "ds_read_b64_tr_b16 a[66:67], v226 offset:2048\n"
  "v_mfma_f32_16x16x32_bf16 a[4:7], a[224:227], v[164:167], a[4:7]\n"
  "buffer_load_dwordx4 v[128:131], v224, s[20:23], s41 offen sc1\n"
  "v_mfma_f32_16x16x32_bf16 a[4:7], a[240:243], v[180:183], a[4:7]\n"
  "s_add_u32 s41, s65, 6144\n"
  "ds_read_b64_tr_b16 a[68:69], v227 offset:0\n"
  "ds_read_b64_tr_b16 a[70:71], v227 offset:2048\n"
  "v_mfma_f32_16x16x32_bf16 a[8:11], a[224:227], v[168:171], a[8:11]\n"
  "buffer_load_dwordx4 v[132:135], v224, s[20:23], s41 offen sc1\n"
  "v_mfma_f32_16x16x32_bf16 a[8:11], a[240:243], v[184:187], a[8:11]\n"
  "s_add_u32 s41, s65, 10240\n"
  "ds_read_b64_tr_b16 a[72:73], v228 offset:0\n"
  "ds_read_b64_tr_b16 a[74:75], v228 offset:2048\n"
  "v_mfma_f32_16x16x32_bf16 a[12:15], a[224:227], v[172:175], a[12:15]\n"
  "buffer_load_dwordx4 v[136:139], v224, s[20:23], s41 offen sc1\n"
  "v_mfma_f32_16x16x32_bf16 a[12:15], a[240:243], v[188:191], a[12:15]\n"
  "s_add_u32 s41, s65, 14336\n"
  "ds_read_b64_tr_b16 a[76:77], v229 offset:0\n"
  "ds_read_b64_tr_b16 a[78:79], v229 offset:2048\n"
  "v_mfma_f32_16x16x32_bf16 a[16:19], a[228:231], v[160:163], a[16:19]\n"
  "buffer_load_dwordx4 v[140:143], v224, s[20:23], s41 offen sc1\n"
  "v_mfma_f32_16x16x32_bf16 a[16:19], a[244:247], v[176:179], a[16:19]\n"
  "s_add_u32 s41, s65, 3072\n"
  "ds_read_b64_tr_b16 a[80:81], v226 offset:16384\n"
  "ds_read_b64_tr_b16 a[82:83], v226 offset:18432\n"
  "v_mfma_f32_16x16x32_bf16 a[20:23], a[228:231], v[164:167], a[20:23]\n"
  "buffer_load_dwordx4 v[144:147], v224, s[20:23], s41 offen sc1\n"
  "v_mfma_f32_16x16x32_bf16 a[20:23], a[244:247], v[180:183], a[20:23]\n"
  "s_add_u32 s41, s65, 7168\n"
  "ds_read_b64_tr_b16 a[84:85], v227 offset:16384\n"
  "ds_read_b64_tr_b16 a[86:87], v227 offset:18432\n"
  "v_mfma_f32_16x16x32_bf16 a[24:27], a[228:231], v[168:171], a[24:27]\n"
  "buffer_load_dwordx4 v[148:151], v224, s[20:23], s41 offen sc1\n"
  "v_mfma_f32_16x16x32_bf16 a[24:27], a[244:247], v[184:187], a[24:27]\n"
  "s_add_u32 s41, s65, 11264\n"
  "ds_read_b64_tr_b16 a[88:89], v228 offset:16384\n"
  "ds_read_b64_tr_b16 a[90:91], v228 offset:18432\n"
  "v_mfma_f32_16x16x32_bf16 a[28:31], a[228:231], v[172:175], a[28:31]\n"
  "buffer_load_dwordx4 v[152:155], v224, s[20:23], s41 offen sc1\n"
  "v_mfma_f32_16x16x32_bf16 a[28:31], a[244:247], v[188:191], a[28:31]\n"
  "s_add_u32 s41, s65, 15360\n"
  "ds_read_b64_tr_b16 a[92:93], v229 offset:16384\n"
  "ds_read_b64_tr_b16 a[94:95], v229 offset:18432\n"
  "v_mfma_f32_16x16x32_bf16 a[32:35], a[232:235], v[160:163], a[32:35]\n"
  "buffer_load_dwordx4 v[156:159], v224, s[20:23], s41 offen sc1\n"
  "v_mfma_f32_16x16x32_bf16 a[32:35], a[248:251], v[176:179], a[32:35]\n"
  "v_mfma_f32_16x16x32_bf16 a[36:39], a[232:235], v[164:167], a[36:39]\n"
  "v_mfma_f32_16x16x32_bf16 a[36:39], a[248:251], v[180:183], a[36:39]\n"
  "v_mfma_f32_16x16x32_bf16 a[40:43], a[232:235], v[168:171], a[40:43]\n"
  "v_mfma_f32_16x16x32_bf16 a[40:43], a[248:251], v[184:187], a[40:43]\n"
  "v_mfma_f32_16x16x32_bf16 a[44:47], a[232:235], v[172:175], a[44:47]\n"
  "v_mfma_f32_16x16x32_bf16 a[44:47], a[248:251], v[188:191], a[44:47]\n"
  "v_mfma_f32_16x16x32_bf16 a[48:51], a[236:239], v[160:163], a[48:51]\n"
  "v_mfma_f32_16x16x32_bf16 a[48:51], a[252:255], v[176:179], a[48:51]\n"
  "v_mfma_f32_16x16x32_bf16 a[52:55], a[236:239], v[164:167], a[52:55]\n"
  "v_mfma_f32_16x16x32_bf16 a[52:55], a[252:255], v[180:183], a[52:55]\n"
  "v_mfma_f32_16x16x32_bf16 a[56:59], a[236:239], v[168:171], a[56:59]\n"
  "v_mfma_f32_16x16x32_bf16 a[56:59], a[252:255], v[184:187], a[56:59]\n"
  "v_mfma_f32_16x16x32_bf16 a[60:63], a[236:239], v[172:175], a[60:63]\n"
  "s_waitcnt vmcnt(40)\n"
  "v_mfma_f32_16x16x32_bf16 a[60:63], a[252:255], v[188:191], a[60:63]\n"
  "s_waitcnt lgkmcnt(0)\n"
  "v_mfma_f32_16x16x32_bf16 a[0:3], a[64:67], v[192:195], a[0:3]\n"
  "s_waitcnt vmcnt(63)\n"
  "v_mfma_f32_16x16x32_bf16 a[0:3], a[80:83], v[208:211], a[0:3]\n"
  "s_add_u32 s41, s66, 0\n"
  "ds_read_b64_tr_b16 a[96:97], v230 offset:0\n"
  "ds_read_b64_tr_b16 a[98:99], v230 offset:2048\n"
  "v_mfma_f32_16x16x32_bf16 a[4:7], a[64:67], v[196:199], a[4:7]\n"
  "buffer_load_dwordx4 v[160:163], v224, s[20:23], s41 offen sc1\n"
  "v_mfma_f32_16x16x32_bf16 a[4:7], a[80:83], v[212:215], a[4:7]\n"
  "s_add_u32 s41, s66, 4096\n"
  "ds_read_b64_tr_b16 a[100:101], v231 offset:0\n"
  "ds_read_b64_tr_b16 a[102:103], v231 offset:2048\n"
  "v_mfma_f32_16x16x32_bf16 a[8:11], a[64:67], v[200:203], a[8:11]\n"
  "buffer_load_dwordx4 v[164:167], v224, s[20:23], s41 offen sc1\n"
  "v_mfma_f32_16x16x32_bf16 a[8:11], a[80:83], v[216:219], a[8:11]\n"
  "s_add_u32 s41, s66, 8192\n"
  "ds_read_b64_tr_b16 a[104:105], v232 offset:0\n"
  "ds_read_b64_tr_b16 a[106:107], v232 offset:2048\n"
  "v_mfma_f32_16x16x32_bf16 a[12:15], a[64:67], v[204:207], a[12:15]\n"
  "buffer_load_dwordx4 v[168:171], v224, s[20:23], s41 offen sc1\n"
  "v_mfma_f32_16x16x32_bf16 a[12:15], a[80:83], v[220:223], a[12:15]\n"
  "s_add_u32 s41, s66, 12288\n"
  "ds_read_b64_tr_b16 a[108:109], v233 offset:0\n"
  "ds_read_b64_tr_b16 a[110:111], v233 offset:2048\n"
  "v_mfma_f32_16x16x32_bf16 a[16:19], a[68:71], v[192:195], a[16:19]\n"
  "buffer_load_dwordx4 v[172:175], v224, s[20:23], s41 offen sc1\n"
  "v_mfma_f32_16x16x32_bf16 a[16:19], a[84:87], v[208:211], a[16:19]\n"
  "s_add_u32 s41, s66, 1024\n"
  "ds_read_b64_tr_b16 a[112:113], v226 offset:32768\n"
  "ds_read_b64_tr_b16 a[114:115], v226 offset:34816\n"
  "v_mfma_f32_16x16x32_bf16 a[20:23], a[68:71], v[196:199], a[20:23]\n"
  "buffer_load_dwordx4 v[176:179], v224, s[20:23], s41 offen sc1\n"
  "v_mfma_f32_16x16x32_bf16 a[20:23], a[84:87], v[212:215], a[20:23]\n"
  "s_add_u32 s41, s66, 5120\n"
  "ds_read_b64_tr_b16 a[116:117], v227 offset:32768\n"
  "ds_read_b64_tr_b16 a[118:119], v227 offset:34816\n"
  "v_mfma_f32_16x16x32_bf16 a[24:27], a[68:71], v[200:203], a[24:27]\n"
  "buffer_load_dwordx4 v[180:183], v224, s[20:23], s41 offen sc1\n"
  "v_mfma_f32_16x16x32_bf16 a[24:27], a[84:87], v[216:219], a[24:27]\n"
  "s_add_u32 s41, s66, 9216\n"
  "ds_read_b64_tr_b16 a[120:121], v228 offset:32768\n"
  "ds_read_b64_tr_b16 a[122:123], v228 offset:34816\n"
  "v_mfma_f32_16x16x32_bf16 a[28:31], a[68:71], v[204:207], a[28:31]\n"
  "buffer_load_dwordx4 v[184:187], v224, s[20:23], s41 offen sc1\n"
  "v_mfma_f32_16x16x32_bf16 a[28:31], a[84:87], v[220:223], a[28:31]\n"
  "s_add_u32 s41, s66, 13312\n"
  "ds_read_b64_tr_b16 a[124:125], v229 offset:32768\n"
  "ds_read_b64_tr_b16 a[126:127], v229 offset:34816\n"
  "v_mfma_f32_16x16x32_bf16 a[32:35], a[72:75], v[192:195], a[32:35]\n"
  "buffer_load_dwordx4 v[188:191], v224, s[20:23], s41 offen sc1\n"
  "v_mfma_f32_16x16x32_bf16 a[32:35], a[88:91], v[208:211], a[32:35]\n"
  "s_add_u32 m0, s42, 0\n"
  "s_add_u32 s46, s57, 65536\n"
  "v_mfma_f32_16x16x32_bf16 a[36:39], a[72:75], v[196:199], a[36:39]\n"
  "buffer_load_dwordx4 v244, s[76:79], s46 offen offset:0 lds\n"
  "v_mfma_f32_16x16x32_bf16 a[36:39], a[88:91], v[212:215], a[36:39]\n"
  "buffer_load_dwordx4 v245, s[76:79], s46 offen offset:1024 lds\n"
  "v_mfma_f32_16x16x32_bf16 a[40:43], a[72:75], v[200:203], a[40:43]\n"
  "buffer_load_dwordx4 v246, s[76:79], s46 offen offset:2048 lds\n"
  "v_mfma_f32_16x16x32_bf16 a[40:43], a[88:91], v[216:219], a[40:43]\n"
  "buffer_load_dwordx4 v247, s[76:79], s46 offen offset:3072 lds\n"
  "v_mfma_f32_16x16x32_bf16 a[44:47], a[72:75], v[204:207], a[44:47]\n"
  "s_add_u32 m0, s42, 16384\n"
  "s_add_u32 s46, s57, 98304\n"
  "v_mfma_f32_16x16x32_bf16 a[44:47], a[88:91], v[220:223], a[44:47]\n"
  "buffer_load_dwordx4 v244, s[76:79], s46 offen offset:0 lds\n"
  "v_mfma_f32_16x16x32_bf16 a[48:51], a[76:79], v[192:195], a[48:51]\n"
  "buffer_load_dwordx4 v245, s[76:79], s46 offen offset:1024 lds\n"
  "v_mfma_f32_16x16x32_bf16 a[48:51], a[92:95], v[208:211], a[48:51]\n"
  "buffer_load_dwordx4 v246, s[76:79], s46 offen offset:2048 lds\n"
  "v_mfma_f32_16x16x32_bf16 a[52:55], a[76:79], v[196:199], a[52:55]\n"
  "buffer_load_dwordx4 v247, s[76:79], s46 offen offset:3072 lds\n"
  "v_mfma_f32_16x16x32_bf16 a[52:55], a[92:95], v[212:215], a[52:55]\n"
  "v_mfma_f32_16x16x32_bf16 a[56:59], a[76:79], v[200:203], a[56:59]\n"
  "v_mfma_f32_16x16x32_bf16 a[56:59], a[92:95], v[216:219], a[56:59]\n"
  "v_mfma_f32_16x16x32_bf16 a[60:63], a[76:79], v[204:207], a[60:63]\n"
  "s_waitcnt vmcnt(48)\n"
  "v_mfma_f32_16x16x32_bf16 a[60:63], a[92:95], v[220:223], a[60:63]\n"
  "s_waitcnt lgkmcnt(0)\n"
  "v_mfma_f32_16x16x32_bf16 a[0:3], a[96:99], v[0:3], a[0:3]\n"
  "s_waitcnt vmcnt(63)\n"
  "v_mfma_f32_16x16x32_bf16 a[0:3], a[112:115], v[16:19], a[0:3]\n"
  "s_add_u32 s41, s66, 2048\n"
  "ds_read_b64_tr_b16 a[128:129], v230 offset:16384\n"
  "ds_read_b64_tr_b16 a[130:131], v230 offset:18432\n"
  "v_mfma_f32_16x16x32_bf16 a[4:7], a[96:99], v[4:7], a[4:7]\n"
  "buffer_load_dwordx4 v[192:195], v224, s[20:23], s41 offen sc1\n"
  "v_mfma_f32_16x16x32_bf16 a[4:7], a[112:115], v[20:23], a[4:7]\n"
  "s_add_u32 s41, s66, 6144\n"
  "ds_read_b64_tr_b16 a[132:133], v231 offset:16384\n"
  "ds_read_b64_tr_b16 a[134:135], v231 offset:18432\n"
  "v_mfma_f32_16x16x32_bf16 a[8:11], a[96:99], v[8:11], a[8:11]\n"
  "buffer_load_dwordx4 v[196:199], v224, s[20:23], s41 offen sc1\n"
  "v_mfma_f32_16x16x32_bf16 a[8:11], a[112:115], v[24:27], a[8:11]\n"
  "s_add_u32 s41, s66, 10240\n"
  "ds_read_b64_tr_b16 a[136:137], v232 offset:16384\n"
  "ds_read_b64_tr_b16 a[138:139], v232 offset:18432\n"
  "v_mfma_f32_16x16x32_bf16 a[12:15], a[96:99], v[12:15], a[12:15]\n"
  "buffer_load_dwordx4 v[200:203], v224, s[20:23], s41 offen sc1\n"
  "v_mfma_f32_16x16x32_bf16 a[12:15], a[112:115], v[28:31], a[12:15]\n"
  "s_add_u32 s41, s66, 14336\n"
  "ds_read_b64_tr_b16 a[140:141], v233 offset:16384\n"
  "ds_read_b64_tr_b16 a[142:143], v233 offset:18432\n"
  "v_mfma_f32_16x16x32_bf16 a[16:19], a[100:103], v[0:3], a[16:19]\n"
  "buffer_load_dwordx4 v[204:207], v224, s[20:23], s41 offen sc1\n"
  "v_mfma_f32_16x16x32_bf16 a[16:19], a[116:119], v[16:19], a[16:19]\n"
  "s_add_u32 s41, s66, 3072\n"
  "ds_read_b64_tr_b16 a[144:145], v226 offset:49152\n"
  "ds_read_b64_tr_b16 a[146:147], v226 offset:51200\n"
  "v_mfma_f32_16x16x32_bf16 a[20:23], a[100:103], v[4:7], a[20:23]\n"
  "buffer_load_dwordx4 v[208:211], v224, s[20:23], s41 offen sc1\n"
  "v_mfma_f32_16x16x32_bf16 a[20:23], a[116:119], v[20:23], a[20:23]\n"
  "s_add_u32 s41, s66, 7168\n"
  "ds_read_b64_tr_b16 a[148:149], v227 offset:49152\n"
  "ds_read_b64_tr_b16 a[150:151], v227 offset:51200\n"
  "v_mfma_f32_16x16x32_bf16 a[24:27], a[100:103], v[8:11], a[24:27]\n"
  "buffer_load_dwordx4 v[212:215], v224, s[20:23], s41 offen sc1\n"
  "v_mfma_f32_16x16x32_bf16 a[24:27], a[116:119], v[24:27], a[24:27]\n"
  "s_add_u32 s41, s66, 11264\n"
  "ds_read_b64_tr_b16 a[152:153], v228 offset:49152\n"
  "ds_read_b64_tr_b16 a[154:155], v228 offset:51200\n"
  "v_mfma_f32_16x16x32_bf16 a[28:31], a[100:103], v[12:15], a[28:31]\n"
  "buffer_load_dwordx4 v[216:219], v224, s[20:23], s41 offen sc1\n"
  "v_mfma_f32_16x16x32_bf16 a[28:31], a[116:119], v[28:31], a[28:31]\n"
  "s_add_u32 s41, s66, 15360\n"
  "ds_read_b64_tr_b16 a[156:157], v229 offset:49152\n"
  "ds_read_b64_tr_b16 a[158:159], v229 offset:51200\n"
  "v_mfma_f32_16x16x32_bf16 a[32:35], a[104:107], v[0:3], a[32:35]\n"
  "buffer_load_dwordx4 v[220:223], v224, s[20:23], s41 offen sc1\n"
  "v_mfma_f32_16x16x32_bf16 a[32:35], a[120:123], v[16:19], a[32:35]\n"
  "s_add_u32 m0, s42, 65536\n"
  "s_add_u32 s46, s58, 0\n"
  "v_mfma_f32_16x16x32_bf16 a[36:39], a[104:107], v[4:7], a[36:39]\n"
  "buffer_load_dwordx4 v244, s[76:79], s46 offen offset:0 lds\n"
  "v_mfma_f32_16x16x32_bf16 a[36:39], a[120:123], v[20:23], a[36:39]\n"
  "buffer_load_dwordx4 v245, s[76:79], s46 offen offset:1024 lds\n"
  "v_mfma_f32_16x16x32_bf16 a[40:43], a[104:107], v[8:11], a[40:43]\n"
  "buffer_load_dwordx4 v246, s[76:79], s46 offen offset:2048 lds\n"
  "v_mfma_f32_16x16x32_bf16 a[40:43], a[120:123], v[24:27], a[40:43]\n"
  "buffer_load_dwordx4 v247, s[76:79], s46 offen offset:3072 lds\n"
  "v_mfma_f32_16x16x32_bf16 a[44:47], a[104:107], v[12:15], a[44:47]\n"
  "s_add_u32 m0, s42, 32768\n"
  "s_add_u32 s46, s58, 32768\n"
  "v_mfma_f32_16x16x32_bf16 a[44:47], a[120:123], v[28:31], a[44:47]\n"
  "buffer_load_dwordx4 v244, s[76:79], s46 offen offset:0 lds\n"
  "v_mfma_f32_16x16x32_bf16 a[48:51], a[108:111], v[0:3], a[48:51]\n"
  "buffer_load_dwordx4 v245, s[76:79], s46 offen offset:1024 lds\n"
  "v_mfma_f32_16x16x32_bf16 a[48:51], a[124:127], v[16:19], a[48:51]\n"
  "buffer_load_dwordx4 v246, s[76:79], s46 offen offset:2048 lds\n"
  "v_mfma_f32_16x16x32_bf16 a[52:55], a[108:111], v[4:7], a[52:55]\n"
  "buffer_load_dwordx4 v247, s[76:79], s46 offen offset:3072 lds\n"
  "v_mfma_f32_16x16x32_bf16 a[52:55], a[124:127], v[20:23], a[52:55]\n"
  "v_mfma_f32_16x16x32_bf16 a[56:59], a[108:111], v[8:11], a[56:59]\n"
  "v_mfma_f32_16x16x32_bf16 a[56:59], a[124:127], v[24:27], a[56:59]\n"
  "v_mfma_f32_16x16x32_bf16 a[60:63], a[108:111], v[12:15], a[60:63]\n"
  "s_waitcnt vmcnt(56)\n"
  "v_mfma_f32_16x16x32_bf16 a[60:63], a[124:127], v[28:31], a[60:63]\n"
  "s_waitcnt lgkmcnt(0)\n"
  "v_mfma_f32_16x16x32_bf16 a[0:3], a[128:131], v[32:35], a[0:3]\n"
  "s_waitcnt vmcnt(63)\n"
  "v_mfma_f32_16x16x32_bf16 a[0:3], a[144:147], v[48:51], a[0:3]\n"
  "s_add_u32 s41, s67, 0\n"
  "ds_read_b64_tr_b16 a[160:161], v230 offset:32768\n"
  "ds_read_b64_tr_b16 a[162:163], v230 offset:34816\n"
  "v_mfma_f32_16x16x32_bf16 a[4:7], a[128:131], v[36:39], a[4:7]\n"
  "buffer_load_dwordx4 v[0:3], v224, s[20:23], s41 offen sc1\n"
  "v_mfma_f32_16x16x32_bf16 a[4:7], a[144:147], v[52:55], a[4:7]\n"
  "s_add_u32 s41, s67, 4096\n"
  "ds_read_b64_tr_b16 a[164:165], v231 offset:32768\n"
  "ds_read_b64_tr_b16 a[166:167], v231 offset:34816\n"
  "v_mfma_f32_16x16x32_bf16 a[8:11], a[128:131], v[40:43], a[8:11]\n"
  "buffer_load_dwordx4 v[4:7], v224, s[20:23], s41 offen sc1\n"
  "v_mfma_f32_16x16x32_bf16 a[8:11], a[144:147], v[56:59], a[8:11]\n"
  "s_add_u32 s41, s67, 8192\n"
  "ds_read_b64_tr_b16 a[168:169], v232 offset:32768\n"
  "ds_read_b64_tr_b16 a[170:171], v232 offset:34816\n"
  "v_mfma_f32_16x16x32_bf16 a[12:15], a[128:131], v[44:47], a[12:15]\n"
  "buffer_load_dwordx4 v[8:11], v224, s[20:23], s41 offen sc1\n"
  "v_mfma_f32_16x16x32_bf16 a[12:15], a[144:147], v[60:63], a[12:15]\n"
  "s_add_u32 s41, s67, 12288\n"
  "ds_read_b64_tr_b16 a[172:173], v233 offset:32768\n"
  "ds_read_b64_tr_b16 a[174:175], v233 offset:34816\n"
  "v_mfma_f32_16x16x32_bf16 a[16:19], a[132:135], v[32:35], a[16:19]\n"
  "buffer_load_dwordx4 v[12:15], v224, s[20:23], s41 offen sc1\n"
  "v_mfma_f32_16x16x32_bf16 a[16:19], a[148:151], v[48:51], a[16:19]\n"
  "s_add_u32 s41, s67, 1024\n"
  "ds_read_b64_tr_b16 a[176:177], v230 offset:49152\n"
  "ds_read_b64_tr_b16 a[178:179], v230 offset:51200\n"
  "v_mfma_f32_16x16x32_bf16 a[20:23], a[132:135], v[36:39], a[20:23]\n"
  "buffer_load_dwordx4 v[16:19], v224, s[20:23], s41 offen sc1\n"
  "v_mfma_f32_16x16x32_bf16 a[20:23], a[148:151], v[52:55], a[20:23]\n"
  "s_add_u32 s41, s67, 5120\n"
  "ds_read_b64_tr_b16 a[180:181], v231 offset:49152\n"
  "ds_read_b64_tr_b16 a[182:183], v231 offset:51200\n"
  "v_mfma_f32_16x16x32_bf16 a[24:27], a[132:135], v[40:43], a[24:27]\n"
  "buffer_load_dwordx4 v[20:23], v224, s[20:23], s41 offen sc1\n"
  "v_mfma_f32_16x16x32_bf16 a[24:27], a[148:151], v[56:59], a[24:27]\n"
  "s_add_u32 s41, s67, 9216\n"
  "ds_read_b64_tr_b16 a[184:185], v232 offset:49152\n"
  "ds_read_b64_tr_b16 a[186:187], v232 offset:51200\n"
  "v_mfma_f32_16x16x32_bf16 a[28:31], a[132:135], v[44:47], a[28:31]\n"
  "buffer_load_dwordx4 v[24:27], v224, s[20:23], s41 offen sc1\n"
  "v_mfma_f32_16x16x32_bf16 a[28:31], a[148:151], v[60:63], a[28:31]\n"
  "s_add_u32 s41, s67, 13312\n"
  "ds_read_b64_tr_b16 a[188:189], v233 offset:49152\n"
  "ds_read_b64_tr_b16 a[190:191], v233 offset:51200\n"
  "v_mfma_f32_16x16x32_bf16 a[32:35], a[136:139], v[32:35], a[32:35]\n"
  "buffer_load_dwordx4 v[28:31], v224, s[20:23], s41 offen sc1\n"
  "v_mfma_f32_16x16x32_bf16 a[32:35], a[152:155], v[48:51], a[32:35]\n"
  "s_add_u32 m0, s42, 81920\n"
  "s_add_u32 s46, s58, 65536\n"
  "v_mfma_f32_16x16x32_bf16 a[36:39], a[136:139], v[36:39], a[36:39]\n"
  "buffer_load_dwordx4 v244, s[76:79], s46 offen offset:0 lds\n"
  "v_mfma_f32_16x16x32_bf16 a[36:39], a[152:155], v[52:55], a[36:39]\n"
  "buffer_load_dwordx4 v245, s[76:79], s46 offen offset:1024 lds\n"
  "v_mfma_f32_16x16x32_bf16 a[40:43], a[136:139], v[40:43], a[40:43]\n"
  "buffer_load_dwordx4 v246, s[76:79], s46 offen offset:2048 lds\n"
  "v_mfma_f32_16x16x32_bf16 a[40:43], a[152:155], v[56:59], a[40:43]\n"
  "buffer_load_dwordx4 v247, s[76:79], s46 offen offset:3072 lds\n"
  "v_mfma_f32_16x16x32_bf16 a[44:47], a[136:139], v[44:47], a[44:47]\n"
  "s_add_u32 m0, s42, 49152\n"
  "s_add_u32 s46, s58, 98304\n"
  "v_mfma_f32_16x16x32_bf16 a[44:47], a[152:155], v[60:63], a[44:47]\n"
  "buffer_load_dwordx4 v244, s[76:79], s46 offen offset:0 lds\n"
  "v_mfma_f32_16x16x32_bf16 a[48:51], a[140:143], v[32:35], a[48:51]\n"
  "buffer_load_dwordx4 v245, s[76:79], s46 offen offset:1024 lds\n"
  "v_mfma_f32_16x16x32_bf16 a[48:51], a[156:159], v[48:51], a[48:51]\n"
  "buffer_load_dwordx4 v246, s[76:79], s46 offen offset:2048 lds\n"
  "v_mfma_f32_16x16x32_bf16 a[52:55], a[140:143], v[36:39], a[52:55]\n"
  "buffer_load_dwordx4 v247, s[76:79], s46 offen offset:3072 lds\n"
  "v_mfma_f32_16x16x32_bf16 a[52:55], a[156:159], v[52:55], a[52:55]\n"
  "v_mfma_f32_16x16x32_bf16 a[56:59], a[140:143], v[40:43], a[56:59]\n"
  "v_mfma_f32_16x16x32_bf16 a[56:59], a[156:159], v[56:59], a[56:59]\n"
  "v_mfma_f32_16x16x32_bf16 a[60:63], a[140:143], v[44:47], a[60:63]\n"
  "s_waitcnt vmcnt(63)\n"
  "v_mfma_f32_16x16x32_bf16 a[60:63], a[156:159], v[60:63], a[60:63]\n"
  "s_waitcnt lgkmcnt(0)\n"
  "v_mfma_f32_16x16x32_bf16 a[0:3], a[160:163], v[64:67], a[0:3]\n"
  "s_waitcnt vmcnt(63)\n"
  "v_mfma_f32_16x16x32_bf16 a[0:3], a[176:179], v[80:83], a[0:3]\n"
  "s_add_u32 s41, s67, 2048\n"
  "ds_read_b64_tr_b16 a[192:193], v234 offset:0\n"
  "ds_read_b64_tr_b16 a[194:195], v234 offset:2048\n"
  "v_mfma_f32_16x16x32_bf16 a[4:7], a[160:163], v[68:71], a[4:7]\n"
  "buffer_load_dwordx4 v[32:35], v224, s[20:23], s41 offen sc1\n"
  "v_mfma_f32_16x16x32_bf16 a[4:7], a[176:179], v[84:87], a[4:7]\n"
  "s_add_u32 s41, s67, 6144\n"
  "ds_read_b64_tr_b16 a[196:197], v235 offset:0\n"
  "ds_read_b64_tr_b16 a[198:199], v235 offset:2048\n"
  "v_mfma_f32_16x16x32_bf16 a[8:11], a[160:163], v[72:75], a[8:11]\n"
  "buffer_load_dwordx4 v[36:39], v224, s[20:23], s41 offen sc1\n"
  "v_mfma_f32_16x16x32_bf16 a[8:11], a[176:179], v[88:91], a[8:11]\n"
  "s_add_u32 s41, s67, 10240\n"
  "ds_read_b64_tr_b16 a[200:201], v236 offset:0\n"
  "ds_read_b64_tr_b16 a[202:203], v236 offset:2048\n"
  "v_mfma_f32_16x16x32_bf16 a[12:15], a[160:163], v[76:79], a[12:15]\n"
  "buffer_load_dwordx4 v[40:43], v224, s[20:23], s41 offen sc1\n"
  "v_mfma_f32_16x16x32_bf16 a[12:15], a[176:179], v[92:95], a[12:15]\n"
  "s_add_u32 s41, s67, 14336\n"
  "ds_read_b64_tr_b16 a[204:205], v237 offset:0\n"
  "ds_read_b64_tr_b16 a[206:207], v237 offset:2048\n"
  "v_mfma_f32_16x16x32_bf16 a[16:19], a[164:167], v[64:67], a[16:19]\n"
  "buffer_load_dwordx4 v[44:47], v224, s[20:23], s41 offen sc1\n"
  "v_mfma_f32_16x16x32_bf16 a[16:19], a[180:183], v[80:83], a[16:19]\n"
  "s_add_u32 s41, s67, 3072\n"
  "ds_read_b64_tr_b16 a[208:209], v234 offset:4096\n"
  "ds_read_b64_tr_b16 a[210:211], v234 offset:6144\n"
  "v_mfma_f32_16x16x32_bf16 a[20:23], a[164:167], v[68:71], a[20:23]\n"
  "buffer_load_dwordx4 v[48:51], v224, s[20:23], s41 offen sc1\n"
  "v_mfma_f32_16x16x32_bf16 a[20:23], a[180:183], v[84:87], a[20:23]\n"
  "s_add_u32 s41, s67, 7168\n"
  "ds_read_b64_tr_b16 a[212:213], v235 offset:4096\n"
  "ds_read_b64_tr_b16 a[214:215], v235 offset:6144\n"
  "v_mfma_f32_16x16x32_bf16 a[24:27], a[164:167], v[72:75], a[24:27]\n"
  "buffer_load_dwordx4 v[52:55], v224, s[20:23], s41 offen sc1\n"
  "v_mfma_f32_16x16x32_bf16 a[24:27], a[180:183], v[88:91], a[24:27]\n"
  "s_add_u32 s41, s67, 11264\n"
  "ds_read_b64_tr_b16 a[216:217], v236 offset:4096\n"
  "ds_read_b64_tr_b16 a[218:219], v236 offset:6144\n"
  "v_mfma_f32_16x16x32_bf16 a[28:31], a[164:167], v[76:79], a[28:31]\n"
  "buffer_load_dwordx4 v[56:59], v224, s[20:23], s41 offen sc1\n"
  "v_mfma_f32_16x16x32_bf16 a[28:31], a[180:183], v[92:95], a[28:31]\n"
  "s_add_u32 s41, s67, 15360\n"
  "ds_read_b64_tr_b16 a[220:221], v237 offset:4096\n"
  "ds_read_b64_tr_b16 a[222:223], v237 offset:6144\n"
  "v_mfma_f32_16x16x32_bf16 a[32:35], a[168:171], v[64:67], a[32:35]\n"
  "buffer_load_dwordx4 v[60:63], v224, s[20:23], s41 offen sc1\n"
  "v_mfma_f32_16x16x32_bf16 a[32:35], a[184:187], v[80:83], a[32:35]\n"
  "s_add_u32 m0, s42, 98304\n"
  "s_add_u32 s46, s59, 0\n"
  "v_mfma_f32_16x16x32_bf16 a[36:39], a[168:171], v[68:71], a[36:39]\n"
  "buffer_load_dwordx4 v244, s[76:79], s46 offen offset:0 lds\n"
  "v_mfma_f32_16x16x32_bf16 a[36:39], a[184:187], v[84:87], a[36:39]\n"
  "buffer_load_dwordx4 v245, s[76:79], s46 offen offset:1024 lds\n"
  "v_mfma_f32_16x16x32_bf16 a[40:43], a[168:171], v[72:75], a[40:43]\n"
  "buffer_load_dwordx4 v246, s[76:79], s46 offen offset:2048 lds\n"
  "v_mfma_f32_16x16x32_bf16 a[40:43], a[184:187], v[88:91], a[40:43]\n"
  "buffer_load_dwordx4 v247, s[76:79], s46 offen offset:3072 lds\n"
  "v_mfma_f32_16x16x32_bf16 a[44:47], a[168:171], v[76:79], a[44:47]\n"
  "s_add_u32 m0, s42, 114688\n"
  "s_add_u32 s46, s59, 32768\n"
  "v_mfma_f32_16x16x32_bf16 a[44:47], a[184:187], v[92:95], a[44:47]\n"
  "buffer_load_dwordx4 v244, s[76:79], s46 offen offset:0 lds\n"
  "v_mfma_f32_16x16x32_bf16 a[48:51], a[172:175], v[64:67], a[48:51]\n"
  "buffer_load_dwordx4 v245, s[76:79], s46 offen offset:1024 lds\n"
  "v_mfma_f32_16x16x32_bf16 a[48:51], a[188:191], v[80:83], a[48:51]\n"
  "buffer_load_dwordx4 v246, s[76:79], s46 offen offset:2048 lds\n"
  "v_mfma_f32_16x16x32_bf16 a[52:55], a[172:175], v[68:71], a[52:55]\n"
  "buffer_load_dwordx4 v247, s[76:79], s46 offen offset:3072 lds\n"
  "v_mfma_f32_16x16x32_bf16 a[52:55], a[188:191], v[84:87], a[52:55]\n"
  "v_mfma_f32_16x16x32_bf16 a[56:59], a[172:175], v[72:75], a[56:59]\n"
  "v_mfma_f32_16x16x32_bf16 a[56:59], a[188:191], v[88:91], a[56:59]\n"
  "v_mfma_f32_16x16x32_bf16 a[60:63], a[172:175], v[76:79], a[60:63]\n"
  "s_waitcnt vmcnt(63)\n"
  "v_mfma_f32_16x16x32_bf16 a[60:63], a[188:191], v[92:95], a[60:63]\n"
  "s_waitcnt lgkmcnt(0)\n"
  "v_mfma_f32_16x16x32_bf16 a[0:3], a[192:195], v[96:99], a[0:3]\n"
  "s_waitcnt vmcnt(48)\n"
  "v_mfma_f32_16x16x32_bf16 a[0:3], a[208:211], v[112:115], a[0:3]\n"
  "ds_read_b64_tr_b16 a[224:225], v226 offset:0\n"
  "ds_read_b64_tr_b16 a[226:227], v226 offset:2048\n"
  "v_mfma_f32_16x16x32_bf16 a[4:7], a[192:195], v[100:103], a[4:7]\n"
  "v_mfma_f32_16x16x32_bf16 a[4:7], a[208:211], v[116:119], a[4:7]\n"
  "ds_read_b64_tr_b16 a[228:229], v227 offset:0\n"
  "ds_read_b64_tr_b16 a[230:231], v227 offset:2048\n"
  "v_mfma_f32_16x16x32_bf16 a[8:11], a[192:195], v[104:107], a[8:11]\n"
  "v_mfma_f32_16x16x32_bf16 a[8:11], a[208:211], v[120:123], a[8:11]\n"
  "ds_read_b64_tr_b16 a[232:233], v228 offset:0\n"
  "ds_read_b64_tr_b16 a[234:235], v228 offset:2048\n"
  "v_mfma_f32_16x16x32_bf16 a[12:15], a[192:195], v[108:111], a[12:15]\n"
  "v_mfma_f32_16x16x32_bf16 a[12:15], a[208:211], v[124:127], a[12:15]\n"
  "ds_read_b64_tr_b16 a[236:237], v229 offset:0\n"
  "ds_read_b64_tr_b16 a[238:239], v229 offset:2048\n"
  "v_mfma_f32_16x16x32_bf16 a[16:19], a[196:199], v[96:99], a[16:19]\n"
  "v_mfma_f32_16x16x32_bf16 a[16:19], a[212:215], v[112:115], a[16:19]\n"
  "ds_read_b64_tr_b16 a[240:241], v226 offset:16384\n"
  "ds_read_b64_tr_b16 a[242:243], v226 offset:18432\n"
  "v_mfma_f32_16x16x32_bf16 a[20:23], a[196:199], v[100:103], a[20:23]\n"
  "v_mfma_f32_16x16x32_bf16 a[20:23], a[212:215], v[116:119], a[20:23]\n"
  "ds_read_b64_tr_b16 a[244:245], v227 offset:16384\n"
  "ds_read_b64_tr_b16 a[246:247], v227 offset:18432\n"
  "v_mfma_f32_16x16x32_bf16 a[24:27], a[196:199], v[104:107], a[24:27]\n"
  "v_mfma_f32_16x16x32_bf16 a[24:27], a[212:215], v[120:123], a[24:27]\n"
  "ds_read_b64_tr_b16 a[248:249], v228 offset:16384\n"
  "ds_read_b64_tr_b16 a[250:251], v228 offset:18432\n"
  "v_mfma_f32_16x16x32_bf16 a[28:31], a[196:199], v[108:111], a[28:31]\n"
  "v_mfma_f32_16x16x32_bf16 a[28:31], a[212:215], v[124:127], a[28:31]\n"
  "ds_read_b64_tr_b16 a[252:253], v229 offset:16384\n"
  "ds_read_b64_tr_b16 a[254:255], v229 offset:18432\n"
  "v_mfma_f32_16x16x32_bf16 a[32:35], a[200:203], v[96:99], a[32:35]\n"
  "v_mfma_f32_16x16x32_bf16 a[32:35], a[216:219], v[112:115], a[32:35]\n"
  "s_add_u32 m0, s47, 0\n"
  "s_add_u32 s46, s59, 65536\n"
  "v_mfma_f32_16x16x32_bf16 a[36:39], a[200:203], v[100:103], a[36:39]\n"
  "buffer_load_dwordx4 v244, s[76:79], s46 offen offset:0 lds\n"
  "v_mfma_f32_16x16x32_bf16 a[36:39], a[216:219], v[116:119], a[36:39]\n"
  "buffer_load_dwordx4 v245, s[76:79], s46 offen offset:1024 lds\n"
  "v_mfma_f32_16x16x32_bf16 a[40:43], a[200:203], v[104:107], a[40:43]\n"
  "buffer_load_dwordx4 v246, s[76:79], s46 offen offset:2048 lds\n"
  "v_mfma_f32_16x16x32_bf16 a[40:43], a[216:219], v[120:123], a[40:43]\n"
  "buffer_load_dwordx4 v247, s[76:79], s46 offen offset:3072 lds\n"
  "v_mfma_f32_16x16x32_bf16 a[44:47], a[200:203], v[108:111], a[44:47]\n"
  "s_add_u32 m0, s47, 4096\n"
  "s_add_u32 s46, s59, 98304\n"
  "v_mfma_f32_16x16x32_bf16 a[44:47], a[216:219], v[124:127], a[44:47]\n"
  "buffer_load_dwordx4 v244, s[76:79], s46 offen offset:0 lds\n"
  "v_mfma_f32_16x16x32_bf16 a[48:51], a[204:207], v[96:99], a[48:51]\n"
  "buffer_load_dwordx4 v245, s[76:79], s46 offen offset:1024 lds\n"
  "v_mfma_f32_16x16x32_bf16 a[48:51], a[220:223], v[112:115], a[48:51]\n"
  "buffer_load_dwordx4 v246, s[76:79], s46 offen offset:2048 lds\n"
  "v_mfma_f32_16x16x32_bf16 a[52:55], a[204:207], v[100:103], a[52:55]\n"
  "buffer_load_dwordx4 v247, s[76:79], s46 offen offset:3072 lds\n"
  "v_mfma_f32_16x16x32_bf16 a[52:55], a[220:223], v[116:119], a[52:55]\n"
  "v_mfma_f32_16x16x32_bf16 a[56:59], a[204:207], v[104:107], a[56:59]\n"
  "v_mfma_f32_16x16x32_bf16 a[56:59], a[220:223], v[120:123], a[56:59]\n"
  "v_mfma_f32_16x16x32_bf16 a[60:63], a[204:207], v[108:111], a[60:63]\n"
  "s_waitcnt vmcnt(63)\n"
  "v_mfma_f32_16x16x32_bf16 a[60:63], a[220:223], v[124:127], a[60:63]\n"
  "s_waitcnt lgkmcnt(0)\n"
  "v_mfma_f32_16x16x32_bf16 a[0:3], a[224:227], v[128:131], a[0:3]\n"
  "s_waitcnt vmcnt(40)\n"
  "v_mfma_f32_16x16x32_bf16 a[0:3], a[240:243], v[144:147], a[0:3]\n"
  "ds_read_b64_tr_b16 a[64:65], v230 offset:0\n"
  "ds_read_b64_tr_b16 a[66:67], v230 offset:2048\n"
  "v_mfma_f32_16x16x32_bf16 a[4:7], a[224:227], v[132:135], a[4:7]\n"
  "v_mfma_f32_16x16x32_bf16 a[4:7], a[240:243], v[148:151], a[4:7]\n"
  "ds_read_b64_tr_b16 a[68:69], v231 offset:0\n"
  "ds_read_b64_tr_b16 a[70:71], v231 offset:2048\n"
  "v_mfma_f32_16x16x32_bf16 a[8:11], a[224:227], v[136:139], a[8:11]\n"
  "v_mfma_f32_16x16x32_bf16 a[8:11], a[240:243], v[152:155], a[8:11]\n"
  "ds_read_b64_tr_b16 a[72:73], v232 offset:0\n"
  "ds_read_b64_tr_b16 a[74:75], v232 offset:2048\n"
  "v_mfma_f32_16x16x32_bf16 a[12:15], a[224:227], v[140:143], a[12:15]\n"
  "v_mfma_f32_16x16x32_bf16 a[12:15], a[240:243], v[156:159], a[12:15]\n"
  "ds_read_b64_tr_b16 a[76:77], v233 offset:0\n"
  "ds_read_b64_tr_b16 a[78:79], v233 offset:2048\n"
  "v_mfma_f32_16x16x32_bf16 a[16:19], a[228:231], v[128:131], a[16:19]\n"
  "v_mfma_f32_16x16x32_bf16 a[16:19], a[244:247], v[144:147], a[16:19]\n"
  "ds_read_b64_tr_b16 a[80:81], v226 offset:32768\n"
  "ds_read_b64_tr_b16 a[82:83], v226 offset:34816\n"
  "v_mfma_f32_16x16x32_bf16 a[20:23], a[228:231], v[132:135], a[20:23]\n"
  "v_mfma_f32_16x16x32_bf16 a[20:23], a[244:247], v[148:151], a[20:23]\n"
  "ds_read_b64_tr_b16 a[84:85], v227 offset:32768\n"
  "ds_read_b64_tr_b16 a[86:87], v227 offset:34816\n"
  "v_mfma_f32_16x16x32_bf16 a[24:27], a[228:231], v[136:139], a[24:27]\n"
  "v_mfma_f32_16x16x32_bf16 a[24:27], a[244:247], v[152:155], a[24:27]\n"
  "ds_read_b64_tr_b16 a[88:89], v228 offset:32768\n"
  "ds_read_b64_tr_b16 a[90:91], v228 offset:34816\n"
  "v_mfma_f32_16x16x32_bf16 a[28:31], a[228:231], v[140:143], a[28:31]\n"
  "v_mfma_f32_16x16x32_bf16 a[28:31], a[244:247], v[156:159], a[28:31]\n"
  "ds_read_b64_tr_b16 a[92:93], v229 offset:32768\n"
  "ds_read_b64_tr_b16 a[94:95], v229 offset:34816\n"
  "v_mfma_f32_16x16x32_bf16 a[32:35], a[232:235], v[128:131], a[32:35]\n"
  "v_mfma_f32_16x16x32_bf16 a[32:35], a[248:251], v[144:147], a[32:35]\n"
  "v_mfma_f32_16x16x32_bf16 a[36:39], a[232:235], v[132:135], a[36:39]\n"
  "v_mfma_f32_16x16x32_bf16 a[36:39], a[248:251], v[148:151], a[36:39]\n"
  "v_mfma_f32_16x16x32_bf16 a[40:43], a[232:235], v[136:139], a[40:43]\n"
  "v_mfma_f32_16x16x32_bf16 a[40:43], a[248:251], v[152:155], a[40:43]\n"
  "v_mfma_f32_16x16x32_bf16 a[44:47], a[232:235], v[140:143], a[44:47]\n"
  "v_mfma_f32_16x16x32_bf16 a[44:47], a[248:251], v[156:159], a[44:47]\n"
  "v_mfma_f32_16x16x32_bf16 a[48:51], a[236:239], v[128:131], a[48:51]\n"
  "v_mfma_f32_16x16x32_bf16 a[48:51], a[252:255], v[144:147], a[48:51]\n"
  "v_mfma_f32_16x16x32_bf16 a[52:55], a[236:239], v[132:135], a[52:55]\n"
  "v_mfma_f32_16x16x32_bf16 a[52:55], a[252:255], v[148:151], a[52:55]\n"
  "v_mfma_f32_16x16x32_bf16 a[56:59], a[236:239], v[136:139], a[56:59]\n"
  "v_mfma_f32_16x16x32_bf16 a[56:59], a[252:255], v[152:155], a[56:59]\n"
  "v_mfma_f32_16x16x32_bf16 a[60:63], a[236:239], v[140:143], a[60:63]\n"
  "s_waitcnt vmcnt(63)\n"
  "v_mfma_f32_16x16x32_bf16 a[60:63], a[252:255], v[156:159], a[60:63]\n"
  "s_waitcnt lgkmcnt(0)\n"
  "v_mfma_f32_16x16x32_bf16 a[0:3], a[64:67], v[160:163], a[0:3]\n"
  "s_waitcnt vmcnt(24)\n"
  "v_mfma_f32_16x16x32_bf16 a[0:3], a[80:83], v[176:179], a[0:3]\n"
  "ds_read_b64_tr_b16 a[96:97], v230 offset:16384\n"
  "ds_read_b64_tr_b16 a[98:99], v230 offset:18432\n"
  "v_mfma_f32_16x16x32_bf16 a[4:7], a[64:67], v[164:167], a[4:7]\n"
  "v_mfma_f32_16x16x32_bf16 a[4:7], a[80:83], v[180:183], a[4:7]\n"
  "ds_read_b64_tr_b16 a[100:101], v231 offset:16384\n"
  "ds_read_b64_tr_b16 a[102:103], v231 offset:18432\n"
  "v_mfma_f32_16x16x32_bf16 a[8:11], a[64:67], v[168:171], a[8:11]\n"
  "v_mfma_f32_16x16x32_bf16 a[8:11], a[80:83], v[184:187], a[8:11]\n"
  "ds_read_b64_tr_b16 a[104:105], v232 offset:16384\n"
  "ds_read_b64_tr_b16 a[106:107], v232 offset:18432\n"
  "v_mfma_f32_16x16x32_bf16 a[12:15], a[64:67], v[172:175], a[12:15]\n"
  "v_mfma_f32_16x16x32_bf16 a[12:15], a[80:83], v[188:191], a[12:15]\n"
  "ds_read_b64_tr_b16 a[108:109], v233 offset:16384\n"
  "ds_read_b64_tr_b16 a[110:111], v233 offset:18432\n"
  "v_mfma_f32_16x16x32_bf16 a[16:19], a[68:71], v[160:163], a[16:19]\n"
  "v_mfma_f32_16x16x32_bf16 a[16:19], a[84:87], v[176:179], a[16:19]\n"
  "ds_read_b64_tr_b16 a[112:113], v226 offset:49152\n"
  "ds_read_b64_tr_b16 a[114:115], v226 offset:51200\n"
  "v_mfma_f32_16x16x32_bf16 a[20:23], a[68:71], v[164:167], a[20:23]\n"
  "v_mfma_f32_16x16x32_bf16 a[20:23], a[84:87], v[180:183], a[20:23]\n"
  "ds_read_b64_tr_b16 a[116:117], v227 offset:49152\n"
  "ds_read_b64_tr_b16 a[118:119], v227 offset:51200\n"
  "v_mfma_f32_16x16x32_bf16 a[24:27], a[68:71], v[168:171], a[24:27]\n"
  "v_mfma_f32_16x16x32_bf16 a[24:27], a[84:87], v[184:187], a[24:27]\n"
  "ds_read_b64_tr_b16 a[120:121], v228 offset:49152\n"
  "ds_read_b64_tr_b16 a[122:123], v228 offset:51200\n"
  "v_mfma_f32_16x16x32_bf16 a[28:31], a[68:71], v[172:175], a[28:31]\n"
  "v_mfma_f32_16x16x32_bf16 a[28:31], a[84:87], v[188:191], a[28:31]\n"
  "ds_read_b64_tr_b16 a[124:125], v229 offset:49152\n"
  "ds_read_b64_tr_b16 a[126:127], v229 offset:51200\n"
  "v_mfma_f32_16x16x32_bf16 a[32:35], a[72:75], v[160:163], a[32:35]\n"
  "v_mfma_f32_16x16x32_bf16 a[32:35], a[88:91], v[176:179], a[32:35]\n"
  "v_mfma_f32_16x16x32_bf16 a[36:39], a[72:75], v[164:167], a[36:39]\n"
  "v_mfma_f32_16x16x32_bf16 a[36:39], a[88:91], v[180:183], a[36:39]\n"
  "v_mfma_f32_16x16x32_bf16 a[40:43], a[72:75], v[168:171], a[40:43]\n"
  "v_mfma_f32_16x16x32_bf16 a[40:43], a[88:91], v[184:187], a[40:43]\n"
  "v_mfma_f32_16x16x32_bf16 a[44:47], a[72:75], v[172:175], a[44:47]\n"
  "v_mfma_f32_16x16x32_bf16 a[44:47], a[88:91], v[188:191], a[44:47]\n"
  "v_mfma_f32_16x16x32_bf16 a[48:51], a[76:79], v[160:163], a[48:51]\n"
  "v_mfma_f32_16x16x32_bf16 a[48:51], a[92:95], v[176:179], a[48:51]\n"
  "v_mfma_f32_16x16x32_bf16 a[52:55], a[76:79], v[164:167], a[52:55]\n"
  "v_mfma_f32_16x16x32_bf16 a[52:55], a[92:95], v[180:183], a[52:55]\n"
  "v_mfma_f32_16x16x32_bf16 a[56:59], a[76:79], v[168:171], a[56:59]\n"
  "v_mfma_f32_16x16x32_bf16 a[56:59], a[92:95], v[184:187], a[56:59]\n"
  "v_mfma_f32_16x16x32_bf16 a[60:63], a[76:79], v[172:175], a[60:63]\n"
  "s_waitcnt vmcnt(48)\n"
  "v_mfma_f32_16x16x32_bf16 a[60:63], a[92:95], v[188:191], a[60:63]\n"
  "s_waitcnt lgkmcnt(0)\n"
  "v_mfma_f32_16x16x32_bf16 a[0:3], a[96:99], v[192:195], a[0:3]\n"
  "s_waitcnt vmcnt(8)\n"
  "v_mfma_f32_16x16x32_bf16 a[0:3], a[112:115], v[208:211], a[0:3]\n"
  "ds_read_b64_tr_b16 a[128:129], v230 offset:32768\n"
  "ds_read_b64_tr_b16 a[130:131], v230 offset:34816\n"
  "v_mfma_f32_16x16x32_bf16 a[4:7], a[96:99], v[196:199], a[4:7]\n"
  "v_mfma_f32_16x16x32_bf16 a[4:7], a[112:115], v[212:215], a[4:7]\n"
  "ds_read_b64_tr_b16 a[132:133], v231 offset:32768\n"
  "ds_read_b64_tr_b16 a[134:135], v231 offset:34816\n"
  "v_mfma_f32_16x16x32_bf16 a[8:11], a[96:99], v[200:203], a[8:11]\n"
  "v_mfma_f32_16x16x32_bf16 a[8:11], a[112:115], v[216:219], a[8:11]\n"
  "ds_read_b64_tr_b16 a[136:137], v232 offset:32768\n"
  "ds_read_b64_tr_b16 a[138:139], v232 offset:34816\n"
  "v_mfma_f32_16x16x32_bf16 a[12:15], a[96:99], v[204:207], a[12:15]\n"
  "v_mfma_f32_16x16x32_bf16 a[12:15], a[112:115], v[220:223], a[12:15]\n"
  "ds_read_b64_tr_b16 a[140:141], v233 offset:32768\n"
  "ds_read_b64_tr_b16 a[142:143], v233 offset:34816\n"
  "v_mfma_f32_16x16x32_bf16 a[16:19], a[100:103], v[192:195], a[16:19]\n"
  "v_mfma_f32_16x16x32_bf16 a[16:19], a[116:119], v[208:211], a[16:19]\n"
  "ds_read_b64_tr_b16 a[144:145], v230 offset:49152\n"
  "ds_read_b64_tr_b16 a[146:147], v230 offset:51200\n"
  "v_mfma_f32_16x16x32_bf16 a[20:23], a[100:103], v[196:199], a[20:23]\n"
  "v_mfma_f32_16x16x32_bf16 a[20:23], a[116:119], v[212:215], a[20:23]\n"
  "ds_read_b64_tr_b16 a[148:149], v231 offset:49152\n"
  "ds_read_b64_tr_b16 a[150:151], v231 offset:51200\n"
  "v_mfma_f32_16x16x32_bf16 a[24:27], a[100:103], v[200:203], a[24:27]\n"
  "v_mfma_f32_16x16x32_bf16 a[24:27], a[116:119], v[216:219], a[24:27]\n"
  "ds_read_b64_tr_b16 a[152:153], v232 offset:49152\n"
  "ds_read_b64_tr_b16 a[154:155], v232 offset:51200\n"
  "v_mfma_f32_16x16x32_bf16 a[28:31], a[100:103], v[204:207], a[28:31]\n"
  "v_mfma_f32_16x16x32_bf16 a[28:31], a[116:119], v[220:223], a[28:31]\n"
  "ds_read_b64_tr_b16 a[156:157], v233 offset:49152\n"
  "ds_read_b64_tr_b16 a[158:159], v233 offset:51200\n"
  "v_mfma_f32_16x16x32_bf16 a[32:35], a[104:107], v[192:195], a[32:35]\n"
  "v_mfma_f32_16x16x32_bf16 a[32:35], a[120:123], v[208:211], a[32:35]\n"
  "v_mfma_f32_16x16x32_bf16 a[36:39], a[104:107], v[196:199], a[36:39]\n"
  "v_mfma_f32_16x16x32_bf16 a[36:39], a[120:123], v[212:215], a[36:39]\n"
  "v_mfma_f32_16x16x32_bf16 a[40:43], a[104:107], v[200:203], a[40:43]\n"
  "v_mfma_f32_16x16x32_bf16 a[40:43], a[120:123], v[216:219], a[40:43]\n"
  "v_mfma_f32_16x16x32_bf16 a[44:47], a[104:107], v[204:207], a[44:47]\n"
  "v_mfma_f32_16x16x32_bf16 a[44:47], a[120:123], v[220:223], a[44:47]\n"
  "v_mfma_f32_16x16x32_bf16 a[48:51], a[108:111], v[192:195], a[48:51]\n"
  "v_mfma_f32_16x16x32_bf16 a[48:51], a[124:127], v[208:211], a[48:51]\n"
  "v_mfma_f32_16x16x32_bf16 a[52:55], a[108:111], v[196:199], a[52:55]\n"
  "v_mfma_f32_16x16x32_bf16 a[52:55], a[124:127], v[212:215], a[52:55]\n"
  "v_mfma_f32_16x16x32_bf16 a[56:59], a[108:111], v[200:203], a[56:59]\n"
  "v_mfma_f32_16x16x32_bf16 a[56:59], a[124:127], v[216:219], a[56:59]\n"
  "v_mfma_f32_16x16x32_bf16 a[60:63], a[108:111], v[204:207], a[60:63]\n"
  "s_waitcnt vmcnt(32)\n"
  "v_mfma_f32_16x16x32_bf16 a[60:63], a[124:127], v[220:223], a[60:63]\n"
  "s_waitcnt lgkmcnt(0)\n"
  "v_mfma_f32_16x16x32_bf16 a[0:3], a[128:131], v[0:3], a[0:3]\n"
  "s_waitcnt vmcnt(0)\n"
  "v_mfma_f32_16x16x32_bf16 a[0:3], a[144:147], v[16:19], a[0:3]\n"
  "ds_read_b64_tr_b16 a[160:161], v234 offset:0\n"
  "ds_read_b64_tr_b16 a[162:163], v234 offset:2048\n"
  "v_mfma_f32_16x16x32_bf16 a[4:7], a[128:131], v[4:7], a[4:7]\n"
  "v_mfma_f32_16x16x32_bf16 a[4:7], a[144:147], v[20:23], a[4:7]\n"
  "ds_read_b64_tr_b16 a[164:165], v235 offset:0\n"
  "ds_read_b64_tr_b16 a[166:167], v235 offset:2048\n"
  "v_mfma_f32_16x16x32_bf16 a[8:11], a[128:131], v[8:11], a[8:11]\n"
  "v_mfma_f32_16x16x32_bf16 a[8:11], a[144:147], v[24:27], a[8:11]\n"
  "ds_read_b64_tr_b16 a[168:169], v236 offset:0\n"
  "ds_read_b64_tr_b16 a[170:171], v236 offset:2048\n"
  "v_mfma_f32_16x16x32_bf16 a[12:15], a[128:131], v[12:15], a[12:15]\n"
  "v_mfma_f32_16x16x32_bf16 a[12:15], a[144:147], v[28:31], a[12:15]\n"
  "ds_read_b64_tr_b16 a[172:173], v237 offset:0\n"
  "ds_read_b64_tr_b16 a[174:175], v237 offset:2048\n"
  "v_mfma_f32_16x16x32_bf16 a[16:19], a[132:135], v[0:3], a[16:19]\n"
  "v_mfma_f32_16x16x32_bf16 a[16:19], a[148:151], v[16:19], a[16:19]\n"
  "ds_read_b64_tr_b16 a[176:177], v234 offset:4096\n"
  "ds_read_b64_tr_b16 a[178:179], v234 offset:6144\n"
  "v_mfma_f32_16x16x32_bf16 a[20:23], a[132:135], v[4:7], a[20:23]\n"
  "v_mfma_f32_16x16x32_bf16 a[20:23], a[148:151], v[20:23], a[20:23]\n"
  "ds_read_b64_tr_b16 a[180:181], v235 offset:4096\n"
  "ds_read_b64_tr_b16 a[182:183], v235 offset:6144\n"
  "v_mfma_f32_16x16x32_bf16 a[24:27], a[132:135], v[8:11], a[24:27]\n"
  "v_mfma_f32_16x16x32_bf16 a[24:27], a[148:151], v[24:27], a[24:27]\n"
  "ds_read_b64_tr_b16 a[184:185], v236 offset:4096\n"
  "ds_read_b64_tr_b16 a[186:187], v236 offset:6144\n"
  "v_mfma_f32_16x16x32_bf16 a[28:31], a[132:135], v[12:15], a[28:31]\n"
  "v_mfma_f32_16x16x32_bf16 a[28:31], a[148:151], v[28:31], a[28:31]\n"
  "ds_read_b64_tr_b16 a[188:189], v237 offset:4096\n"
  "ds_read_b64_tr_b16 a[190:191], v237 offset:6144\n"
  "v_mfma_f32_16x16x32_bf16 a[32:35], a[136:139], v[0:3], a[32:35]\n"
  "v_mfma_f32_16x16x32_bf16 a[32:35], a[152:155], v[16:19], a[32:35]\n"
  "v_mfma_f32_16x16x32_bf16 a[36:39], a[136:139], v[4:7], a[36:39]\n"
  "v_mfma_f32_16x16x32_bf16 a[36:39], a[152:155], v[20:23], a[36:39]\n"
  "v_mfma_f32_16x16x32_bf16 a[40:43], a[136:139], v[8:11], a[40:43]\n"
  "v_mfma_f32_16x16x32_bf16 a[40:43], a[152:155], v[24:27], a[40:43]\n"
  "v_mfma_f32_16x16x32_bf16 a[44:47], a[136:139], v[12:15], a[44:47]\n"
  "v_mfma_f32_16x16x32_bf16 a[44:47], a[152:155], v[28:31], a[44:47]\n"
  "v_mfma_f32_16x16x32_bf16 a[48:51], a[140:143], v[0:3], a[48:51]\n"
  "v_mfma_f32_16x16x32_bf16 a[48:51], a[156:159], v[16:19], a[48:51]\n"
  "v_mfma_f32_16x16x32_bf16 a[52:55], a[140:143], v[4:7], a[52:55]\n"
  "v_mfma_f32_16x16x32_bf16 a[52:55], a[156:159], v[20:23], a[52:55]\n"
  "v_mfma_f32_16x16x32_bf16 a[56:59], a[140:143], v[8:11], a[56:59]\n"
  "v_mfma_f32_16x16x32_bf16 a[56:59], a[156:159], v[24:27], a[56:59]\n"
  "v_mfma_f32_16x16x32_bf16 a[60:63], a[140:143], v[12:15], a[60:63]\n"
  "s_waitcnt vmcnt(16)\n"
  "v_mfma_f32_16x16x32_bf16 a[60:63], a[156:159], v[28:31], a[60:63]\n"
  "s_waitcnt lgkmcnt(0)\n"
  "v_mfma_f32_16x16x32_bf16 a[0:3], a[160:163], v[32:35], a[0:3]\n"
  "v_mfma_f32_16x16x32_bf16 a[0:3], a[176:179], v[48:51], a[0:3]\n"
  "v_mfma_f32_16x16x32_bf16 a[4:7], a[160:163], v[36:39], a[4:7]\n"
  "v_mfma_f32_16x16x32_bf16 a[4:7], a[176:179], v[52:55], a[4:7]\n"
  "v_mfma_f32_16x16x32_bf16 a[8:11], a[160:163], v[40:43], a[8:11]\n"
  "v_mfma_f32_16x16x32_bf16 a[8:11], a[176:179], v[56:59], a[8:11]\n"
  "v_mfma_f32_16x16x32_bf16 a[12:15], a[160:163], v[44:47], a[12:15]\n"
  "v_mfma_f32_16x16x32_bf16 a[12:15], a[176:179], v[60:63], a[12:15]\n"
  "v_mfma_f32_16x16x32_bf16 a[16:19], a[164:167], v[32:35], a[16:19]\n"
  "v_mfma_f32_16x16x32_bf16 a[16:19], a[180:183], v[48:51], a[16:19]\n"
  "v_mfma_f32_16x16x32_bf16 a[20:23], a[164:167], v[36:39], a[20:23]\n"
  "v_mfma_f32_16x16x32_bf16 a[20:23], a[180:183], v[52:55], a[20:23]\n"
  "v_mfma_f32_16x16x32_bf16 a[24:27], a[164:167], v[40:43], a[24:27]\n"
  "v_mfma_f32_16x16x32_bf16 a[24:27], a[180:183], v[56:59], a[24:27]\n"
  "v_mfma_f32_16x16x32_bf16 a[28:31], a[164:167], v[44:47], a[28:31]\n"
  "v_mfma_f32_16x16x32_bf16 a[28:31], a[180:183], v[60:63], a[28:31]\n"
  "v_mfma_f32_16x16x32_bf16 a[32:35], a[168:171], v[32:35], a[32:35]\n"
  "v_mfma_f32_16x16x32_bf16 a[32:35], a[184:187], v[48:51], a[32:35]\n"
  "v_mfma_f32_16x16x32_bf16 a[36:39], a[168:171], v[36:39], a[36:39]\n"
  "v_mfma_f32_16x16x32_bf16 a[36:39], a[184:187], v[52:55], a[36:39]\n"
  "v_mfma_f32_16x16x32_bf16 a[40:43], a[168:171], v[40:43], a[40:43]\n"
  "v_mfma_f32_16x16x32_bf16 a[40:43], a[184:187], v[56:59], a[40:43]\n"
  "v_mfma_f32_16x16x32_bf16 a[44:47], a[168:171], v[44:47], a[44:47]\n"
  "v_mfma_f32_16x16x32_bf16 a[44:47], a[184:187], v[60:63], a[44:47]\n"
  "v_mfma_f32_16x16x32_bf16 a[48:51], a[172:175], v[32:35], a[48:51]\n"
  "v_mfma_f32_16x16x32_bf16 a[48:51], a[188:191], v[48:51], a[48:51]\n"
  "v_mfma_f32_16x16x32_bf16 a[52:55], a[172:175], v[36:39], a[52:55]\n"
  "v_mfma_f32_16x16x32_bf16 a[52:55], a[188:191], v[52:55], a[52:55]\n"
  "v_mfma_f32_16x16x32_bf16 a[56:59], a[172:175], v[40:43], a[56:59]\n"
  "v_mfma_f32_16x16x32_bf16 a[56:59], a[188:191], v[56:59], a[56:59]\n"
  "v_mfma_f32_16x16x32_bf16 a[60:63], a[172:175], v[44:47], a[60:63]\n"
  "v_mfma_f32_16x16x32_bf16 a[60:63], a[188:191], v[60:63], a[60:63]\n"
  "s_nop 7\n"
  "s_waitcnt lgkmcnt(0)\n"
  "v_lshlrev_b32 v238, 4, v249\n"
  "v_add_u32 v240, s42, v238\n"
  "ds_write_b128 v240, a[0:3] offset:0\n"
  "ds_write_b128 v240, a[4:7] offset:1024\n"
  "ds_write_b128 v240, a[8:11] offset:2048\n"
  "ds_write_b128 v240, a[12:15] offset:3072\n"
  "ds_write_b128 v240, a[16:19] offset:16384\n"
  "ds_write_b128 v240, a[20:23] offset:17408\n"
  "ds_write_b128 v240, a[24:27] offset:18432\n"
  "ds_write_b128 v240, a[28:31] offset:19456\n"
  "ds_write_b128 v240, a[32:35] offset:32768\n"
  "ds_write_b128 v240, a[36:39] offset:33792\n"
  "ds_write_b128 v240, a[40:43] offset:34816\n"
  "ds_write_b128 v240, a[44:47] offset:35840\n"
  "ds_write_b128 v240, a[48:51] offset:49152\n"
  "ds_write_b128 v240, a[52:55] offset:50176\n"
  "ds_write_b128 v240, a[56:59] offset:51200\n"
  "ds_write_b128 v240, a[60:63] offset:52224\n"
  "v_and_b32 v241, 15, v249\n"
  "v_lshlrev_b32 v241, 10, v241\n"
  "v_lshl_add_u32 v241, v239, 3, v241\n"
  "s_lshl_b32 s45, %[wid], 5\n"
  "v_add_u32 v241, s45, v241\n"
  "s_lshl_b32 s45, %[wid], 14\n"
  "v_add_u32 v240, s45, v238\n"
  "s_waitcnt lgkmcnt(0)\n"
  "s_barrier\n"
  "ds_read_b128 v[0:3], v240 offset:0\n"
  "ds_read_b128 v[4:7], v240 offset:4096\n"
  "ds_read_b128 v[8:11], v240 offset:8192\n"
  "ds_read_b128 v[12:15], v240 offset:12288\n"
  "ds_read_b128 v[16:19], v240 offset:1024\n"
  "ds_read_b128 v[20:23], v240 offset:5120\n"
  "ds_read_b128 v[24:27], v240 offset:9216\n"
  "ds_read_b128 v[28:31], v240 offset:13312\n"
  "ds_read_b128 v[32:35], v240 offset:2048\n"
  "ds_read_b128 v[36:39], v240 offset:6144\n"
  "ds_read_b128 v[40:43], v240 offset:10240\n"
  "ds_read_b128 v[44:47], v240 offset:14336\n"
  "ds_read_b128 v[48:51], v240 offset:3072\n"
  "ds_read_b128 v[52:55], v240 offset:7168\n"
  "ds_read_b128 v[56:59], v240 offset:11264\n"
  "ds_read_b128 v[60:63], v240 offset:15360\n"
  "s_waitcnt lgkmcnt(12)\n"
  "v_pk_add_f32 v[0:1], v[0:1], v[4:5]\n"
  "v_pk_add_f32 v[8:9], v[8:9], v[12:13]\n"
  "v_pk_add_f32 v[2:3], v[2:3], v[6:7]\n"
  "v_pk_add_f32 v[10:11], v[10:11], v[14:15]\n"
  "v_pk_add_f32 v[0:1], v[0:1], v[8:9]\n"
  "v_pk_add_f32 v[2:3], v[2:3], v[10:11]\n"
  "v_cvt_pk_bf16_f32 v0, v0, v1\n"
  "v_cvt_pk_bf16_f32 v1, v2, v3\n"
  "s_mov_b32 s46, 0\n"
  "buffer_store_dwordx2 v[0:1], v241, s[24:27], s46 offen\n"
  "s_waitcnt lgkmcnt(8)\n"
  "v_pk_add_f32 v[16:17], v[16:17], v[20:21]\n"
  "v_pk_add_f32 v[24:25], v[24:25], v[28:29]\n"
  "v_pk_add_f32 v[18:19], v[18:19], v[22:23]\n"
  "v_pk_add_f32 v[26:27], v[26:27], v[30:31]\n"
  "v_pk_add_f32 v[16:17], v[16:17], v[24:25]\n"
  "v_pk_add_f32 v[18:19], v[18:19], v[26:27]\n"
  "v_cvt_pk_bf16_f32 v16, v16, v17\n"
  "v_cvt_pk_bf16_f32 v17, v18, v19\n"
  "s_mov_b32 s46, 16384\n"
  "buffer_store_dwordx2 v[16:17], v241, s[24:27], s46 offen\n"
  "s_waitcnt lgkmcnt(4)\n"
  "v_pk_add_f32 v[32:33], v[32:33], v[36:37]\n"
  "v_pk_add_f32 v[40:41], v[40:41], v[44:45]\n"
  "v_pk_add_f32 v[34:35], v[34:35], v[38:39]\n"
  "v_pk_add_f32 v[42:43], v[42:43], v[46:47]\n"
  "v_pk_add_f32 v[32:33], v[32:33], v[40:41]\n"
  "v_pk_add_f32 v[34:35], v[34:35], v[42:43]\n"
  "v_cvt_pk_bf16_f32 v32, v32, v33\n"
  "v_cvt_pk_bf16_f32 v33, v34, v35\n"
  "s_mov_b32 s46, 32768\n"
  "buffer_store_dwordx2 v[32:33], v241, s[24:27], s46 offen\n"
  "s_waitcnt lgkmcnt(0)\n"
  "v_pk_add_f32 v[48:49], v[48:49], v[52:53]\n"
  "v_pk_add_f32 v[56:57], v[56:57], v[60:61]\n"
  "v_pk_add_f32 v[50:51], v[50:51], v[54:55]\n"
  "v_pk_add_f32 v[58:59], v[58:59], v[62:63]\n"
  "v_pk_add_f32 v[48:49], v[48:49], v[56:57]\n"
  "v_pk_add_f32 v[50:51], v[50:51], v[58:59]\n"
  "v_cvt_pk_bf16_f32 v48, v48, v49\n"
  "v_cvt_pk_bf16_f32 v49, v50, v51\n"
  "s_mov_b32 s46, 49152\n"
  "buffer_store_dwordx2 v[48:49], v241, s[24:27], s46 offen\n"
  "s_or_b32 s45, %[wid], %[bid]\n"
  "s_cmp_lg_u32 s45, 0\n"
  "s_cbranch_scc1 L_noep%=\n"
  "s_mov_b32 s38, 16777216\n"
  "s_mov_b32 s39, 0\n"
  "s_add_u32 s38, s38, %[ws_lo]\n"
  "s_addc_u32 s39, s39, %[ws_hi]\n"
  "v_lshlrev_b32 v238, 6, v249\n"
  "s_mov_b32 s44, 524288\n"
  "L_all%=:\n"
  "s_mov_b32 s45, 0\n"
  "v_add_u32 v240, s45, v238\n"
  "global_load_dwordx2 v[0:1], v240, s[38:39] sc1\n"
  "s_mov_b32 s45, 4096\n"
  "v_add_u32 v240, s45, v238\n"
  "global_load_dwordx2 v[2:3], v240, s[38:39] sc1\n"
  "s_mov_b32 s45, 8192\n"
  "v_add_u32 v240, s45, v238\n"
  "global_load_dwordx2 v[4:5], v240, s[38:39] sc1\n"
  "s_mov_b32 s45, 12288\n"
  "v_add_u32 v240, s45, v238\n"
  "global_load_dwordx2 v[6:7], v240, s[38:39] sc1\n"
  "s_waitcnt vmcnt(0)\n"
  "v_xor_b32 v0, %[tag_lo], v0\n"
  "v_xor_b32 v1, %[tag_hi], v1\n"
  "v_xor_b32 v2, %[tag_lo], v2\n"
  "v_xor_b32 v3, %[tag_hi], v3\n"
  "v_xor_b32 v4, %[tag_lo], v4\n"
  "v_xor_b32 v5, %[tag_hi], v5\n"
  "v_xor_b32 v6, %[tag_lo], v6\n"
  "v_xor_b32 v7, %[tag_hi], v7\n"
  "v_or3_b32 v0, v0, v1, v2\n"
  "v_or3_b32 v3, v3, v4, v5\n"
  "v_or3_b32 v0, v0, v6, v7\n"
  "v_or_b32 v0, v0, v3\n"
  "v_cmp_ne_u32 vcc, 0, v0\n"
  "s_nop 3\n"
  "s_cmp_eq_u64 vcc, 0\n"
  "s_cbranch_scc1 L_allset%=\n"
  "s_sleep 8\n"
  "s_sub_u32 s44, s44, 1\n"
  "s_cmp_lg_u32 s44, 0\n"
  "s_cbranch_scc1 L_all%=\n"
  "L_allset%=:\n"
  "s_mov_b32 s68, 16793600\n"
  "s_mov_b32 s69, 0\n"
  "s_add_u32 s68, s68, %[ws_lo]\n"
  "s_addc_u32 s69, s69, %[ws_hi]\n"
  "v_mov_b32 v238, 0\n"
  "v_mov_b32 v240, %[tag_lo]\n"
  "v_mov_b32 v241, %[tag_hi]\n"
  "s_mov_b64 exec, 1\n"
  "global_store_dwordx2 v238, v[240:241], s[68:69] sc1\n"
  "s_mov_b64 exec, -1\n"
  "L_noep%=:\n"
  :: [wo] "s"(wo), [ws] "s"(ws), [ws_lo] "s"((unsigned)(unsigned long)ws), [ws_hi] "s"((unsigned)((unsigned long)ws >> 32)), [out] "s"(out),
     [bid] "s"(bid), [wid] "s"(wid), [tag_lo] "s"(tag_lo), [tag_hi] "s"(tag_hi)
  : "v0", "v1", "v2", "v3", "v4", "v5", "v6", "v7", "v8", "v9", "v10", "v11", "v12", "v13", "v14", "v15", "v16", "v17", "v18", "v19", "v20", "v21", "v22", "v23", "v24", "v25", "v26", "v27", "v28", "v29", "v30", "v31", "v32", "v33", "v34", "v35", "v36", "v37", "v38", "v39", "v40", "v41", "v42", "v43", "v44", "v45", "v46", "v47", "v48", "v49", "v50", "v51", "v52", "v53", "v54", "v55", "v56", "v57", "v58", "v59", "v60", "v61", "v62", "v63", "v64", "v65", "v66", "v67", "v68", "v69", "v70", "v71", "v72", "v73", "v74", "v75", "v76", "v77", "v78", "v79", "v80", "v81", "v82", "v83", "v84", "v85", "v86", "v87", "v88", "v89", "v90", "v91", "v92", "v93", "v94", "v95", "v96", "v97", "v98", "v99", "v100", "v101", "v102", "v103", "v104", "v105", "v106", "v107", "v108", "v109", "v110", "v111", "v112", "v113", "v114", "v115", "v116", "v117", "v118", "v119", "v120", "v121", "v122", "v123", "v124", "v125", "v126", "v127", "v128", "v129", "v130", "v131", "v132", "v133", "v134", "v135", "v136", "v137", "v138", "v139", "v140", "v141", "v142", "v143", "v144", "v145", "v146", "v147", "v148", "v149", "v150", "v151", "v152", "v153", "v154", "v155", "v156", "v157", "v158", "v159", "v160", "v161", "v162", "v163", "v164", "v165", "v166", "v167", "v168", "v169", "v170", "v171", "v172", "v173", "v174", "v175", "v176", "v177", "v178", "v179", "v180", "v181", "v182", "v183", "v184", "v185", "v186", "v187", "v188", "v189", "v190", "v191", "v192", "v193", "v194", "v195", "v196", "v197", "v198", "v199", "v200", "v201", "v202", "v203", "v204", "v205", "v206", "v207", "v208", "v209", "v210", "v211", "v212", "v213", "v214", "v215", "v216", "v217", "v218", "v219", "v220", "v221", "v222", "v223", "v224", "v225", "v226", "v227", "v228", "v229", "v230", "v231", "v232", "v233", "v234", "v235", "v236", "v237", "v238", "v239", "v240", "v241", "v242", "v243", "v244", "v245", "v246", "v247", "v248", "v249", "v250", "v251", "v252", "v253", "v254", "v255", "a0", "a1", "a2", "a3", "a4", "a5", "a6", "a7", "a8", "a9", "a10", "a11", "a12", "a13", "a14", "a15", "a16", "a17", "a18", "a19", "a20", "a21", "a22", "a23", "a24", "a25", "a26", "a27", "a28", "a29", "a30", "a31", "a32", "a33", "a34", "a35", "a36", "a37", "a38", "a39", "a40", "a41", "a42", "a43", "a44", "a45", "a46", "a47", "a48", "a49", "a50", "a51", "a52", "a53", "a54", "a55", "a56", "a57", "a58", "a59", "a60", "a61", "a62", "a63", "a64", "a65", "a66", "a67", "a68", "a69", "a70", "a71", "a72", "a73", "a74", "a75", "a76", "a77", "a78", "a79", "a80", "a81", "a82", "a83", "a84", "a85", "a86", "a87", "a88", "a89", "a90", "a91", "a92", "a93", "a94", "a95", "a96", "a97", "a98", "a99", "a100", "a101", "a102", "a103", "a104", "a105", "a106", "a107", "a108", "a109", "a110", "a111", "a112", "a113", "a114", "a115", "a116", "a117", "a118", "a119", "a120", "a121", "a122", "a123", "a124", "a125", "a126", "a127", "a128", "a129", "a130", "a131", "a132", "a133", "a134", "a135", "a136", "a137", "a138", "a139", "a140", "a141", "a142", "a143", "a144", "a145", "a146", "a147", "a148", "a149", "a150", "a151", "a152", "a153", "a154", "a155", "a156", "a157", "a158", "a159", "a160", "a161", "a162", "a163", "a164", "a165", "a166", "a167", "a168", "a169", "a170", "a171", "a172", "a173", "a174", "a175", "a176", "a177", "a178", "a179", "a180", "a181", "a182", "a183", "a184", "a185", "a186", "a187", "a188", "a189", "a190", "a191", "a192", "a193", "a194", "a195", "a196", "a197", "a198", "a199", "a200", "a201", "a202", "a203", "a204", "a205", "a206", "a207", "a208", "a209", "a210", "a211", "a212", "a213", "a214", "a215", "a216", "a217", "a218", "a219", "a220", "a221", "a222", "a223", "a224", "a225", "a226", "a227", "a228", "a229", "a230", "a231", "a232", "a233", "a234", "a235", "a236", "a237", "a238", "a239", "a240", "a241", "a242", "a243", "a244", "a245", "a246", "a247", "a248", "a249", "a250", "a251", "a252", "a253", "a254", "a255", "s20", "s21", "s22", "s23", "s24", "s25", "s26", "s27", "s28", "s29", "s30", "s31", "s36", "s37", "s38", "s39", "s40", "s41", "s42", "s43", "s44", "s45", "s46", "s47", "s48", "s49", "s50", "s51", "s52", "s53", "s54", "s55", "s56", "s57", "s58", "s59", "s60", "s61", "s62", "s63", "s64", "s65", "s66", "s67", "s68", "s69", "s70", "s71", "s72", "s73", "s74", "s75", "s76", "s77", "s78", "s79", "s80", "s81", "s82", "s83", "s84", "s85", "s86", "s87", "vcc", "scc", "memory");
}

extern "C" void kernel_launch(void* const* d_in, const int* in_sizes, int n_in, void* d_out, int out_size, void* d_ws, size_t ws_size, hipStream_t stream) {
  (void)hipFuncSetAttribute((const void*)mha_block, hipFuncAttributeMaxDynamicSharedMemorySize, 163840);
  mha_block<<<256, 256, 163840, stream>>>((const unsigned short*)d_in[0], (const unsigned short*)d_in[1], (const unsigned short*)d_in[2],
                                          (const unsigned short*)d_in[3], (const unsigned short*)d_in[4], (char*)d_ws, (unsigned short*)d_out);
}
```
